```python
import torch

def _softmax(x, axis=-1):
    return torch.softmax(x, dim=axis)
def _gelu(x):
    return torch.nn.functional.gelu(x, approximate="tanh")
def _silu(x):
    return torch.nn.functional.silu(x)
def _sigmoid(x):
    return torch.sigmoid(x)
def _layernorm(x, axis=-1, eps=1e-5):
    m = x.mean(dim=axis, keepdim=True)
    v = x.var(dim=axis, keepdim=True, unbiased=False)
    return (x - m) / torch.sqrt(v + eps)
def _scatter_add(tgt, idx, src, axis=0):
    return tgt.clone().index_add_(axis, idx, src)
def _segment_sum(src, seg, n):
    out = torch.zeros((n,) + src.shape[1:], dtype=src.dtype)
    return out.index_add_(0, seg, src)
def _pad(x, pw):
    flat = tuple(v for pair in reversed(pw) for v in pair)
    return torch.nn.functional.pad(x, flat)

kernel_name = 'concat_heads__ssm_conv_s5__ssm_conv_s4__prompt'

def setup_inputs():

    _w = (lambda a: (lambda t: t.to(torch.bfloat16) if t.is_floating_point() else t)(torch.as_tensor(a)))
    def _A_setup():
        B, S, D = 1, 2048, 512
        P = D
        Lambda_re = -0.5 * (lambda _x: torch.exp(torch.as_tensor(_x)))((lambda *s: torch.randn(*s, dtype=torch.bfloat16) * 0.2)(P))
        Lambda_im = (lambda *s: torch.randn(*s, dtype=torch.bfloat16) * 0.4)(P) * 1.0
        B_re = (lambda *s: torch.randn(*s, dtype=torch.bfloat16) * 0.2)(P, D) * (D ** -0.5)
        B_im = (lambda *s: torch.randn(*s, dtype=torch.bfloat16) * 0.2)(P, D) * (D ** -0.5)
        C_re = (lambda *s: torch.randn(*s, dtype=torch.bfloat16) * 0.2)(D, P) * (P ** -0.5)
        C_im = (lambda *s: torch.randn(*s, dtype=torch.bfloat16) * 0.2)(D, P) * (P ** -0.5)
        log_dt = (lambda *s: torch.randn(*s, dtype=torch.bfloat16) * 0.2)(P) * 0.3 - 2.0
        Dskip = (lambda *s: torch.randn(*s, dtype=torch.bfloat16) * 0.05)(D)
        return {
            "x": _w((lambda *s: torch.randn(*s, dtype=torch.bfloat16) * 0.05)(B, S, D)),
            "Lambda_re": _w(Lambda_re),
            "Lambda_im": _w(Lambda_im),
            "B_re": _w(B_re),
            "B_im": _w(B_im),
            "C_re": _w(C_re),
            "C_im": _w(C_im),
            "log_dt": _w(log_dt),
            "Dskip": _w(Dskip),
        }
    _A_in = _A_setup()

    def _B_setup():
        B, L, D, N = 1, 2048, 512, 128
        A_re = -(lambda _x: torch.exp(torch.as_tensor(_x)))((lambda *s: torch.randn(*s, dtype=torch.bfloat16) * 3.2)(D, N))
        A_im = torch.pi * (lambda *s: torch.randn(*s, dtype=torch.bfloat16) * 0.05)(D, N)
        B_re = (lambda *s: torch.randn(*s, dtype=torch.bfloat16) * 0.1)(D, N)
        B_im = (lambda *s: torch.randn(*s, dtype=torch.bfloat16) * 0.1)(D, N)
        C_re = (lambda *s: torch.randn(*s, dtype=torch.bfloat16) * 0.1)(D, N)
        C_im = (lambda *s: torch.randn(*s, dtype=torch.bfloat16) * 0.1)(D, N)
        log_dt = (lambda *s: torch.randn(*s, dtype=torch.bfloat16) * 0.05)(D) * 0.5 - 4.0
        Dskip = (lambda *s: torch.randn(*s, dtype=torch.bfloat16) * 0.05)(D)
        return {
            "u": _w((lambda *s: torch.randn(*s, dtype=torch.bfloat16) * 0.05)(B, L, D)),
            "A_re": _w(A_re),
            "A_im": _w(A_im),
            "B_re": _w(B_re),
            "B_im": _w(B_im),
            "C_re": _w(C_re),
            "C_im": _w(C_im),
            "log_dt": _w(log_dt),
            "Dskip": _w(Dskip),
        }
    _B_in = _B_setup()

    return {
        "A_x": _A_in["x"],
        "A_Lambda_re": _A_in["Lambda_re"],
        "A_Lambda_im": _A_in["Lambda_im"],
        "A_B_re": _A_in["B_re"],
        "A_B_im": _A_in["B_im"],
        "A_C_re": _A_in["C_re"],
        "A_C_im": _A_in["C_im"],
        "A_log_dt": _A_in["log_dt"],
        "A_Dskip": _A_in["Dskip"],
        "B_u": _B_in["u"],
        "B_A_re": _B_in["A_re"],
        "B_A_im": _B_in["A_im"],
        "B_B_re": _B_in["B_re"],
        "B_B_im": _B_in["B_im"],
        "B_C_re": _B_in["C_re"],
        "B_C_im": _B_in["C_im"],
        "B_log_dt": _B_in["log_dt"],
        "B_Dskip": _B_in["Dskip"],
    }

def reference(A_x, A_Lambda_re, A_Lambda_im, A_B_re, A_B_im, A_C_re, A_C_im, A_log_dt, A_Dskip, B_u, B_A_re, B_A_im, B_B_re, B_B_im, B_C_re, B_C_im, B_log_dt, B_Dskip):
    def _A_ref(x, Lambda_re, Lambda_im, B_re, B_im, C_re, C_im, log_dt, Dskip):
        Bsz, L, H = x.shape
        Lam = Lambda_re + 1j * Lambda_im
        Bmat = B_re + 1j * B_im
        Cmat = C_re + 1j * C_im
        dt = torch.exp(log_dt)
        Lam_bar = torch.exp(dt * Lam)
        B_bar = ((Lam_bar - 1.0) / Lam)[:, None] * Bmat
        Bu = torch.einsum("bld,pd->blp", x + 0j, B_bar)
        h = torch.zeros_like(Bu[:, 0, :])
        hs = []
        for t in range(L):
            h = Lam_bar[None, :] * h + Bu[:, t, :]
            hs.append(h)
        hseq = torch.stack(hs, 1)
        y = 2.0 * torch.einsum("blp,dp->bld", hseq, Cmat).real + x * Dskip[None, None, :]
        return y

    def _B_ref(u, A_re, A_im, B_re, B_im, C_re, C_im, log_dt, Dskip):
        Bsz, L, D = u.shape
        dt = torch.exp(log_dt)[:, None]
        A = A_re + 1j * A_im
        Bc = B_re + 1j * B_im
        Cc = C_re + 1j * C_im
        logdA = dt * A
        dB = dt * Bc
        l = (lambda x, dt: x.to(dt))(torch.arange(L), A_re.dtype)
        P = torch.exp(logdA[:, :, None] * l[None, None, :])
        K = torch.einsum("dn,dnl,dn->dl", Cc, P, dB).real
        L2 = 2 * L
        Kp = _pad(K, ((0, 0), (0, L)))
        up = (lambda x, *d: x.permute(*d))(u, 0, 2, 1)
        up = _pad(up, ((0, 0), (0, 0), (0, L)))
        Kf = (lambda x, axes=(-2,-1): torch.fft.rfft2(x.float(), dim=axes))(Kp[:, None, :], axes=(-2, -1))
        Uf = (lambda x, axes=(-2,-1): torch.fft.rfft2(x.float(), dim=axes))(up[:, :, None, :], axes=(-2, -1))
        Yf = Uf * Kf[None, :, :, :]
        y = (lambda x, s, axes=(-2,-1): torch.fft.irfft2(x, s=s, dim=axes).to(torch.bfloat16))(Yf, (1, L2), axes=(-2, -1))[:, :, 0, :L]
        y = (lambda x, *d: x.permute(*d))(y, 0, 2, 1) + u * Dskip[None, None, :]
        return y

    a = _A_ref(A_x, A_Lambda_re, A_Lambda_im, A_B_re, A_B_im, A_C_re, A_C_im, A_log_dt, A_Dskip)
    b = _B_ref(B_u, B_A_re, B_A_im, B_B_re, B_B_im, B_C_re, B_C_im, B_log_dt, B_Dskip)
    return torch.concatenate([a, b], axis=-1)
```

```cpp
#include <hip/hip_runtime.h>
#include <hip/hip_bf16.h>
#include <stdint.h>

typedef unsigned short u16;
__device__ __forceinline__ float bf2f(u16 v) { return __uint_as_float(((unsigned)v) << 16); }
__device__ __forceinline__ u16 f2bf(float f) {
    unsigned u = __float_as_uint(f);
    return (u16)((u + 0x7FFFu + ((u >> 16) & 1u)) >> 16);
}
constexpr int L = 2048, D = 512, P = 512, N = 128;

__global__ void a1(const u16* x, const u16* Lre, const u16* Lim, const u16* Bre, const u16* Bim, const u16* ldt, float2* Bu) {
    int p = blockIdx.x * blockDim.x + threadIdx.x;
    int t = blockIdx.y;
    float sr = 0, si = 0;
    for (int d = 0; d < D; ++d) { float xv = bf2f(x[t * D + d]); sr += xv * bf2f(Bre[p * D + d]); si += xv * bf2f(Bim[p * D + d]); }
    float dt = bf2f(f2bf(expf(bf2f(ldt[p]))));
    float lr = bf2f(Lre[p]), li = bf2f(Lim[p]);
    float m = expf(dt * lr); float br = m * cosf(dt * li), bi = m * sinf(dt * li);
    float nr = br - 1.f, ni = bi; float den = lr * lr + li * li;
    float cr = (nr * lr + ni * li) / den, ci = (ni * lr - nr * li) / den;
    Bu[t * P + p] = make_float2(cr * sr - ci * si, cr * si + ci * sr);
}
__global__ void a2(const u16* Lre, const u16* Lim, const u16* ldt, float2* Bu) {
    int p = blockIdx.x * blockDim.x + threadIdx.x;
    float dt = bf2f(f2bf(expf(bf2f(ldt[p]))));
    float lr = bf2f(Lre[p]), li = bf2f(Lim[p]);
    float m = expf(dt * lr); float br = m * cosf(dt * li), bi = m * sinf(dt * li);
    float hr = 0, hi = 0;
    for (int t = 0; t < L; ++t) { float2 b = Bu[t * P + p]; float nr = br * hr - bi * hi + b.x, ni = br * hi + bi * hr + b.y; hr = nr; hi = ni; Bu[t * P + p] = make_float2(hr, hi); }
}
__global__ void a3(const u16* x, const u16* Cre, const u16* Cim, const u16* Dk, const float2* h, float* out) {
    int d = blockIdx.x * blockDim.x + threadIdx.x;
    int t = blockIdx.y;
    float s = 0;
    for (int p = 0; p < P; ++p) { float2 hv = h[t * P + p]; s += hv.x * bf2f(Cre[d * P + p]) - hv.y * bf2f(Cim[d * P + p]); }
    out[t * 1024 + d] = 2.f * s + bf2f(f2bf(bf2f(x[t * D + d]) * bf2f(Dk[d])));
}
__global__ void b1(const u16* Are, const u16* Aim, const u16* Bre, const u16* Bim, const u16* Cre, const u16* Cim, const u16* ldt, float* K) {
    int l = blockIdx.x * blockDim.x + threadIdx.x;
    int d = blockIdx.y;
    float dt = bf2f(f2bf(expf(bf2f(ldt[d]))));
    float lf = bf2f(f2bf((float)l));
    float s = 0;
    for (int n = 0; n < N; ++n) {
        int i = d * N + n;
        float zr = dt * bf2f(Are[i]) * lf, zi = dt * bf2f(Aim[i]) * lf;
        float br = dt * bf2f(Bre[i]), bi = dt * bf2f(Bim[i]);
        float cr = bf2f(Cre[i]), ci = bf2f(Cim[i]);
        float wr = cr * br - ci * bi, wi = cr * bi + ci * br;
        float m = expf(zr);
        s += m * (wr * cosf(zi) - wi * sinf(zi));
    }
    K[d * L + l] = s;
}
__global__ void b2(const u16* u, const float* K, const u16* Dk, float* out) {
    int d = blockIdx.x * blockDim.x + threadIdx.x;
    int t = blockIdx.y;
    float s = 0;
    for (int l = 0; l <= t; ++l) s += K[d * L + l] * bf2f(u[(t - l) * D + d]);
    float y = bf2f(f2bf(s));
    float sk = bf2f(f2bf(bf2f(u[t * D + d]) * bf2f(Dk[d])));
    out[t * 1024 + 512 + d] = bf2f(f2bf(y + sk));
}

extern "C" void kernel_launch(void* const* d_in, const int* in_sizes, int n_in, void* d_out, int out_size, void* d_ws, size_t ws_size, hipStream_t stream) {
    const u16* const* in = (const u16* const*)d_in;
    float* out = (float*)d_out;
    float2* Bu = (float2*)d_ws;
    float* K = (float*)((char*)d_ws + (size_t)L * P * 8);
    a1<<<dim3(P / 64, L), 64, 0, stream>>>(in[0], in[1], in[2], in[3], in[4], in[7], Bu);
    a2<<<dim3(P / 64), 64, 0, stream>>>(in[1], in[2], in[7], Bu);
    a3<<<dim3(D / 64, L), 64, 0, stream>>>(in[0], in[5], in[6], in[8], Bu, out);
    b1<<<dim3(L / 64, D), 64, 0, stream>>>(in[10], in[11], in[12], in[13], in[14], in[15], in[16], K);
    b2<<<dim3(D / 64, L), 64, 0, stream>>>(in[9], K, in[17], out);
}
```

```cpp
#include <hip/hip_runtime.h>
#include <stdint.h>

typedef unsigned short u16;
typedef unsigned u32;
typedef unsigned long long u64;
typedef short bf16x8 __attribute__((ext_vector_type(8)));
typedef short s16x4 __attribute__((ext_vector_type(4)));
typedef float f32x2 __attribute__((ext_vector_type(2)));
typedef float f32x4 __attribute__((ext_vector_type(4)));
typedef float f32x16 __attribute__((ext_vector_type(16)));
typedef unsigned u32x2 __attribute__((ext_vector_type(2)));
typedef unsigned u32x4 __attribute__((ext_vector_type(4)));
typedef __bf16 bf16x2_t __attribute__((ext_vector_type(2)));
#define LAS __attribute__((address_space(3)))
typedef __amdgpu_buffer_rsrc_t rsrc_t;

#define ABL 0
#define PRA 2
#define PRK 1
#define UM 7
#define DBG1 0
#define LSTAG 5
#define CTHR 0
#define PRL 0
#define WSL 0
#define LWIN 12
#define UFN 6
#define STAMPS 0

constexpr u32 WS_W = 0;
constexpr u32 FS = 128;
constexpr u32 WS_UFLAG = 524288;
constexpr u32 WS_HFLAG = 131072;
constexpr u32 WS_YFLAG = 262144;
constexpr u32 WS_E = 393216;
constexpr u32 WS_UT = 1u << 20;
constexpr u32 WS_YT = 3u << 20;
constexpr u32 WS_H = 5u << 20;
constexpr u32 WS_BYTES = 9u << 20;
constexpr u32 WS_STAMP = 9u << 20;
constexpr u32 OOB = 0x7FFFFF00u;
constexpr u32 L_R2 = 131072;
constexpr u32 L_S0 = L_R2, S0_SZ = 4672;
constexpr u32 L_KC = L_R2 + 4 * S0_SZ, KC_SZ = 1440;
constexpr u32 L_UB = L_KC + 2 * KC_SZ, UB_PAD = 480, UB_SZ = UB_PAD + 4096;
constexpr u32 L_CB1 = L_UB + 2 * UB_SZ;
constexpr u32 L_CB2 = L_CB1 + 768;
constexpr u32 L_SYNC = L_CB2 + 1024;
constexpr u32 LDS_BYTES = 163840;
static_assert(L_SYNC + 64 <= LDS_BYTES, "lds");
constexpr int NWAVES = 12;
#define HWIN 10
#define PSLEEP 1
constexpr u32 SPIN_CAP = 1u << 15;
constexpr u32 CODE_BYTES = 24576;
constexpr u32 CODE_SLICES = 3;
constexpr u32 SENT = 0xFFFFFFFFu;

struct Params { const void* in[18]; float* out; char* ws; };

#if defined(__HIP_DEVICE_COMPILE__)
#pragma clang attribute push(__attribute__((target("no-packed-fp32-ops"))), apply_to = function)
#endif
template <int I> struct ic { static constexpr int v = I; };
template <int B, int E, typename F> __device__ __forceinline__ void sfor(F&& f) { if constexpr (B < E) { f(ic<B>{}); sfor<B + 1, E>(f); } }
#define INL __attribute__((always_inline))
__device__ __forceinline__ u32 umin(u32 a, u32 b) { return a < b ? a : b; }
__device__ __forceinline__ float u2f(u32 v) { return __builtin_bit_cast(float, v); }
__device__ __forceinline__ u32 f2u(float v) { return __builtin_bit_cast(u32, v); }

__device__ __forceinline__ float bf2f(u32 v) { return u2f(v << 16); }
__device__ __forceinline__ u32 pkbf(float lo, float hi) { f32x2 a = {lo, hi}; return __builtin_bit_cast(u32, __builtin_convertvector(a, bf16x2_t)); }
__device__ __forceinline__ float rbf(float x) { return u2f(pkbf(x, x) << 16); }
__device__ __forceinline__ u32x4 mk_srd(const void* p, u32 bytes) { u64 a = (u64)p; u32x4 r = {(u32)a, (u32)(a >> 32) & 0xFFFFu, bytes, 0x00020000u}; return r; }
__device__ __forceinline__ rsrc_t mk_rsrc(const void* p, u32 bytes) { return __builtin_amdgcn_make_buffer_rsrc((void*)p, 0, bytes, 0x00020000); }
template <int IMM, bool SC1> __device__ __forceinline__ void st128(u32x4 srd, u32x4 v, u32 voff, u32 soff) {
    if (SC1) asm volatile("buffer_store_dwordx4 %0, %1, %2, %3 offen offset:%4 sc1\n\ts_nop 1" :: "v"(v), "v"(voff), "s"(srd), "s"(soff), "i"(IMM) : "memory");
    else asm volatile("buffer_store_dwordx4 %0, %1, %2, %3 offen offset:%4\n\ts_nop 1" :: "v"(v), "v"(voff), "s"(srd), "s"(soff), "i"(IMM) : "memory");
}
#define SBAR() __builtin_amdgcn_sched_barrier(0)
#define WAITV(N) asm volatile("s_waitcnt vmcnt(" #N ")" ::: "memory")
#define WAITL() asm volatile("s_waitcnt lgkmcnt(0)" ::: "memory")
#define FENCE() asm volatile("" ::: "memory")
template <typename T> __device__ __forceinline__ T lds_ld(u32 a) { return *(const LAS T*)(uintptr_t)a; }
template <typename T> __device__ __forceinline__ void lds_st(u32 a, T v) { *(LAS T*)(uintptr_t)a = v; }
__device__ __forceinline__ u32x2 lds_tr(u32 a) { return __builtin_bit_cast(u32x2, __builtin_amdgcn_ds_read_tr16_b64_v4i16((LAS s16x4*)(uintptr_t)a)); }
__device__ __forceinline__ void sig(u32 addr, u32 val) { asm volatile("ds_write_b32 %0, %1" :: "v"(addr), "v"(val) : "memory"); }
__device__ __forceinline__ u32 rfl(u32 v) { return (u32)__builtin_amdgcn_readfirstlane(v); }
__device__ __forceinline__ u32 peek4(u32 addr) { u32x4 v; asm volatile("ds_read_b128 %0, %1\n\ts_waitcnt lgkmcnt(0)" : "=v"(v) : "v"(addr) : "memory"); return rfl(umin(umin(v.x, v.y), umin(v.z, v.w))); }
__device__ __forceinline__ u32 peek3(u32 addr) { u32x4 v; asm volatile("ds_read_b128 %0, %1\n\ts_waitcnt lgkmcnt(0)" : "=v"(v) : "v"(addr) : "memory"); return rfl(umin(umin(v.x, v.y), v.z)); }
__device__ __forceinline__ u32 peek1(u32 addr) { u32 v; asm volatile("ds_read_b32 %0, %1\n\ts_waitcnt lgkmcnt(0)" : "=v"(v) : "v"(addr) : "memory"); return rfl(v); }
#define SPINW(cond, slp) do { _Pragma("unroll 1") for (u32 sp_ = 0; (cond) && sp_ < (1u << 22); ++sp_) { if (slp) __builtin_amdgcn_s_sleep(slp); } } while (0)
__device__ __forceinline__ void wait4(u32 addr, u32 val) { SPINW(peek4(addr) < val, WSL); }
__device__ __forceinline__ void wait4s(u32 addr, u32 val) { SPINW(peek4(addr) < val, 2); }
__device__ __forceinline__ void wait3(u32 addr, u32 val) { SPINW(peek3(addr) < val, WSL); }
__device__ __forceinline__ void wait1(u32 addr, u32 val) { SPINW(peek1(addr) < val, WSL); }
__device__ __forceinline__ void wait1s(u32 addr, u32 val) { SPINW(peek1(addr) < val, 2); }
__device__ __forceinline__ u32 get_tag(u32 addr) {
    u32x2 v = {0u, 0u};
#pragma unroll 1
    for (u32 s = 0; s < (1u << 22); ++s) {
        asm volatile("ds_read_b64 %0, %1\n\ts_waitcnt lgkmcnt(0)" : "=v"(v) : "v"(addr) : "memory");
        if (__builtin_amdgcn_readfirstlane(v.y) != 0) break;
    }
    return __builtin_amdgcn_readfirstlane(v.x);
}
__device__ __forceinline__ u32 tag2(u32 t) { return t * 2654435761u + 0x2545F491u; }
typedef f32x2 cf;
__device__ __forceinline__ cf cmul(cf a, cf b) { return cf{__builtin_fmaf(-a.y, b.y, a.x * b.x), __builtin_fmaf(a.y, b.x, a.x * b.y)}; }
__device__ __forceinline__ cf csq(cf a) { return cf{__builtin_fmaf(-a.y, a.y, a.x * a.x), (a.x + a.x) * a.y}; }
__device__ __forceinline__ cf cfma(cf a, cf b, cf c) { return cf{__builtin_fmaf(-a.y, b.y, __builtin_fmaf(a.x, b.x, c.x)), __builtin_fmaf(a.y, b.x, __builtin_fmaf(a.x, b.y, c.y))}; }
__device__ __forceinline__ f32x2 fma2(float a, f32x2 b, f32x2 c) { return f32x2{__builtin_fmaf(a, b.x, c.x), __builtin_fmaf(a, b.y, c.y)}; }
__device__ __forceinline__ u32 pkc(cf a) { return __builtin_bit_cast(u32, __builtin_convertvector(a, bf16x2_t)); }
__device__ __forceinline__ cf cexpk(float zr, float zi, float k) {
    float m = __builtin_amdgcn_exp2f(zr * (k * 1.4426950408889634f));
    float f = __builtin_amdgcn_fractf(zi * (k * 0.15915494309189535f));
    return cf{m * __builtin_amdgcn_cosf(f), m * __builtin_amdgcn_sinf(f)};
}
__device__ __forceinline__ void swap_halves(cf v, cf& lo, cf& hi) {
    auto a = __builtin_amdgcn_permlane32_swap(f2u(v.x), f2u(v.x), false, false);
    auto b = __builtin_amdgcn_permlane32_swap(f2u(v.y), f2u(v.y), false, false);
    lo = cf{u2f(a[0]), u2f(b[0])}; hi = cf{u2f(a[1]), u2f(b[1])};
}
__device__ __forceinline__ void poll_flags(rsrc_t ws, u32 voff, u32 ta, u32 tb) {
#pragma unroll 1
    for (u32 s = 0; s < SPIN_CAP; ++s) {
        FENCE();
        u32x2 v = __builtin_amdgcn_raw_buffer_load_b64(ws, voff, 0, 16);
        voff = ((v.x ^ ta) | (v.y ^ tb)) == 0 ? OOB : voff;
        if (__builtin_amdgcn_ballot_w64(voff != OOB) == 0) break;
        __builtin_amdgcn_s_sleep(PSLEEP);
    }
    FENCE();
}
__device__ __forceinline__ f32x4 sel4(bool c, f32x4 a, f32x4 b) { asm volatile("" : "+v"(a), "+v"(b)); return c ? a : b; }
#define MFMA32(a, b, c) __builtin_amdgcn_mfma_f32_32x32x16_bf16(a, b, c, 0, 0, 0)
#define MFMA16(a, b, c) __builtin_amdgcn_mfma_f32_16x16x32_bf16(a, b, c, 0, 0, 0)

#define STAMP(i) do {} while (0)
#define STAMP_OUT() do {} while (0)

__global__ __launch_bounds__(768, 3) void ssm_fused(Params p) {
    extern __shared__ __attribute__((aligned(16))) char smem[];
    const u32 lds0 = (u32)(uintptr_t)(LAS char*)smem;
    const u32 tid = __builtin_amdgcn_workitem_id_x();
    const int lane = tid & 63;
    const int wv = __builtin_amdgcn_readfirstlane(tid >> 6);
    const int xcd = __builtin_amdgcn_workgroup_id_x() & 7, idx = __builtin_amdgcn_workgroup_id_x() >> 3;
    STAMP(0);
    u32 cpf = 0;
    if (wv == 8) {
        u64 pc0; asm volatile("s_getpc_b64 %0" : "=s"(pc0));
        cpf = __builtin_amdgcn_raw_buffer_load_b32(mk_rsrc((const void*)(pc0 & ~127ull), CODE_BYTES), (u32)lane * 128, (u32)(idx % CODE_SLICES) * 8192, 0);
    }
    sig(lds0 + L_CB1 + umin(tid, 463u) * 4, tid < 448 ? SENT : 0u);
    asm volatile("s_waitcnt lgkmcnt(0)\n\ts_barrier" ::: "memory");
    asm volatile("" :: "s"(p.in[0]), "s"(p.in[1]), "s"(p.in[2]), "s"(p.in[3]), "s"(p.in[4]), "s"(p.in[5]), "s"(p.in[6]), "s"(p.in[7]), "s"(p.in[8]), "s"(p.in[9]),
                 "s"(p.in[10]), "s"(p.in[11]), "s"(p.in[12]), "s"(p.in[13]), "s"(p.in[14]), "s"(p.in[15]), "s"(p.in[16]), "s"(p.in[17]), "s"(p.out), "s"(p.ws));
    const rsrc_t ws = mk_rsrc(p.ws, WS_BYTES);
    const u32x4 wss = mk_srd(p.ws, WS_BYTES);
    const u32 syncL = lds0 + L_SYNC, syncA = syncL + 16, syncK = syncL + 32, tagw = syncL + 48;
    const int rr = lane >> 3, sp = lane & 7;
    const int m16 = lane & 15, q4 = lane >> 4;
    const int ppos = (xcd < 4 ? xcd : (xcd ^ 2)) >> 1;
    const int c = 4 * ppos + (idx & 3), pb = 8 * (xcd & 1) + (idx >> 2);
    const int tb64 = 8 * ppos + 4 * (xcd & 1) + (idx & 3), db = idx >> 2;

    if (wv < 4) {
        const int uw = wv;
        const u32 mysig = syncL + uw * 4;
        {
            __builtin_amdgcn_s_setprio(PRL);
            const rsrc_t rx = mk_rsrc(p.in[0], 2048 * 1024), rbr = mk_rsrc(p.in[3], 512 * 1024), rbi = mk_rsrc(p.in[4], 512 * 1024);
            const rsrc_t rc = mk_rsrc((uw & 2) ? p.in[6] : p.in[5], 512 * 1024);
            const u32 vg = (u32)rr * 4096 + (u32)sp * 16;
            const u32 xs0 = (u32)c * 131072 + (u32)uw * 32768, bs0 = (u32)pb * 32768 + (u32)uw * 1024, cs0 = (u32)db * 65536 + (u32)(uw & 1) * 32768;
            const int r0 = (idx >> 2) + 2 * (idx & 3);
            auto ld = [&](auto ii) INL {
                constexpr int i = decltype(ii)::v;
                if constexpr (i < 48) { constexpr int s = i / 6, j = i % 6; const u32 ch = (u32)((r0 + s) & 7) * 128;
                    if constexpr (j == 0) return __builtin_amdgcn_raw_buffer_load_b128(rbr, vg, bs0 + ch, 0);
                    else if constexpr (j == 1) return __builtin_amdgcn_raw_buffer_load_b128(rbi, vg, bs0 + ch, 0);
                    else return __builtin_amdgcn_raw_buffer_load_b128(rx, vg + (j - 2) * 1024, xs0 + ch, 0); }
                else { constexpr int cg = (0x74321065u >> (4 * ((i - 48) >> 2))) & 15, b = (i - 48) & 3;
                    return __builtin_amdgcn_raw_buffer_load_b128(rc, vg + b * 1024 + cg * 128, cs0, 0); } };
            u32x4 buf[LWIN];
            sfor<0, LWIN>([&](auto ii) INL { buf[decltype(ii)::v] = ld(ii); SBAR(); });
            const u32 w0 = lds0 + (u32)rr * 128 + (u32)((sp ^ (rr >> 1)) * 16);
            u32 wx[2], wxh[2], wb, wbh, wch[2];
#pragma unroll
            for (int k = 0; k < 2; ++k) { wx[k] = (w0 ^ (k * 64)) + uw * 4096; wxh[k] = wx[k] + 65536; wch[k] = (wx[k] ^ 4096) + 65536; }
            wb = (w0 ^ ((uw & 1) * 64)) + uw * 1024; wbh = wb + 65536;
            u32 msg = mysig, vsa = syncA;
            asm volatile("" : "+v"(wx[0]), "+v"(wx[1]), "+v"(wxh[0]), "+v"(wxh[1]), "+v"(wch[0]), "+v"(wch[1]), "+v"(wb), "+v"(wbh), "+v"(msg), "+v"(vsa));
            STAMP(1);
            sfor<0, 80>([&](auto ii) INL {
                constexpr int i = decltype(ii)::v;
                u32 ad;
                if constexpr (i < 48) { constexpr int s = i / 6, j = i % 6, o = (s % 5) * 24576;
                    if constexpr (j == 0 && s >= 5) wait4(vsa, s - 4);
                    if constexpr (j < 2) { constexpr int ob = o + 16384 + j * 4096; ad = ob < 65536 ? wb + ob : wbh + (ob - 65536); }
                    else { constexpr int ox = o + (j - 2) * 1024; ad = ox < 65536 ? wx[j & 1] + ox : wxh[j & 1] + (ox - 65536); }
                } else { constexpr int q = (i - 48) >> 2, cg = (0x74321065u >> (4 * q)) & 15, need = (0x98877654u >> (4 * q)) & 15, pneed = (0x88776540u >> (4 * q)) & 15, b = (i - 48) & 3;
                    if constexpr (b == 0 && need != pneed) wait4s(vsa, need);
                    ad = (cg < 4 ? wx[b & 1] : wch[b & 1]) + (cg & 3) * 16384 + b * 1024;
                }
                lds_st<u32x4>(ad, buf[i % LWIN]);
                SBAR();
                if constexpr (i + LWIN < 80) { buf[i % LWIN] = ld(ic<i + LWIN>{}); SBAR(); }
                if constexpr (i < 48 && i % 6 == 5) { sig(msg, i / 6 + 1); SBAR(); }
                if constexpr (i == 5) STAMP(2);
                if constexpr (i == 47) { STAMP(3); __builtin_amdgcn_s_setprio(0); }
            });
            sig(mysig, 9);
            STAMP(5);
        }
    }
    int wv2 = wv;
    asm volatile("" : "+s"(wv2));
    if (wv2 < 4) {
        const int uw = wv;
        const rsrc_t ru = mk_rsrc(p.in[9], 2048 * 1024);
        __builtin_amdgcn_s_setprio(1);
        const int dsel = uw >> 1, half = uw & 1;
        const u32 im = lds0 + L_UB + dsel * UB_SZ + UB_PAD + half * 2048;
        const int dd = 64 * xcd + half * 32;
        const u32 ta = get_tag(tagw), tb = tag2(ta);
        const int sel = 2 * idx + dsel, prr = sel >> 4;
        const u32 trow = (u32)(256 * ((m16 >> 2) & 1) + 16 * (m16 >> 3) + (m16 & 3)), tbase = (u32)(512 * prr + 64 * (sel & 3) + 4 * ((sel >> 2) & 3));
        f32x4 sk[4];
        {
            u32x2 uu[4], dk[2];
#pragma unroll
            for (int k = 0; k < 4; ++k) uu[k] = __builtin_amdgcn_raw_buffer_load_b64(ru, trow * 1024 + (u32)q4 * 8 + (k & 1) * 32, (tbase + 32 * (k >> 1)) * 1024 + (u32)dd * 2, 0);
#pragma unroll
            for (int k = 0; k < 2; ++k) dk[k] = __builtin_amdgcn_raw_buffer_load_b64(mk_rsrc(p.in[17], 1024), (u32)q4 * 8 + k * 32, (u32)dd * 2, 0);
#pragma unroll
            for (int k = 0; k < 4; ++k) {
                const u32x2 a = uu[k], b = dk[k & 1];
                const u32 w0 = pkbf(u2f(a.x << 16) * u2f(b.x << 16), u2f(a.x & 0xFFFF0000u) * u2f(b.x & 0xFFFF0000u));
                const u32 w1 = pkbf(u2f(a.y << 16) * u2f(b.y << 16), u2f(a.y & 0xFFFF0000u) * u2f(b.y & 0xFFFF0000u));
                sk[k] = f32x4{u2f(w0 << 16), u2f(w0 & 0xFFFF0000u), u2f(w1 << 16), u2f(w1 & 0xFFFF0000u)};
                asm volatile("" : "+v"(sk[k]));
            }
        }
        STAMP(6);
        if (ABL == 0) {
            wait4s(syncK, 4);
            STAMP(7);
            poll_flags(ws, lane < 32 ? WS_YFLAG + (u32)((prr >> 1) * 512 + dd + lane) * FS : OOB, ta, tb);
            __builtin_amdgcn_s_setprio(3);
            STAMP(8);
            const u32 so = WS_YT + (u32)dd * 4096 + (u32)sel * 64;
            u32x4 a = __builtin_amdgcn_raw_buffer_load_b128(ws, (u32)(lane >> 2) * 4096 + (u32)(lane & 3) * 16, so, 16);
            u32x4 b = __builtin_amdgcn_raw_buffer_load_b128(ws, (u32)(lane >> 2) * 4096 + (u32)(lane & 3) * 16, so + 65536, 16);
            lds_st<u32x4>(im + lane * 16, a);
            lds_st<u32x4>(im + 1024 + lane * 16, b);
            FENCE();
            const u32x4 ro = mk_srd(p.out, 2048 * 4096);
            const u32 ra = im + (u32)(4 * q4 + (m16 >> 2)) * 64 + (u32)(lane & 3) * 8;
#pragma unroll
            for (int k = 0; k < 4; ++k) {
                u32x2 v = lds_tr(ra + (k & 1) * 1024 + (k >> 1) * 32);
                f32x4 f = f32x4{u2f(v.x << 16), u2f(v.x & 0xFFFF0000u), u2f(v.y << 16), u2f(v.y & 0xFFFF0000u)} + sk[k];
                const u32 w0 = pkbf(f[0], f[1]), w1 = pkbf(f[2], f[3]);
                st128<0, false>(ro, u32x4{w0 << 16, w0 & 0xFFFF0000u, w1 << 16, w1 & 0xFFFF0000u}, trow * 4096 + (u32)q4 * 16, (tbase + 32 * (k >> 1)) * 4096 + (u32)(512 + dd) * 4 + (k & 1) * 64);
            }
        }
        STAMP(9);
    } else if (__builtin_expect(wv < 8, 1)) {
        const int aw = wv - 4;
        __builtin_amdgcn_s_setprio(PRA);
        const int r32 = lane & 31, hh = lane >> 5;
        const int g32 = 4 * (r32 & 7) + (r32 >> 3);
        const u32 q_lr = __builtin_amdgcn_raw_buffer_load_b16(mk_rsrc(p.in[1], 1024), (u32)g32 * 2, (u32)pb * 64, 0);
        const u32 q_li = __builtin_amdgcn_raw_buffer_load_b16(mk_rsrc(p.in[2], 1024), (u32)g32 * 2, (u32)pb * 64, 0);
        const u32 q_ld = __builtin_amdgcn_raw_buffer_load_b16(mk_rsrc(p.in[7], 1024), (u32)g32 * 2, (u32)pb * 64, 0);
        const u32 wv0 = __builtin_amdgcn_raw_buffer_load_b32(ws, 0, WS_W, 16);
        u32x4 ut[2];
#pragma unroll
        for (int k = 0; k < 2; ++k) ut[k] = __builtin_amdgcn_raw_buffer_load_b128(mk_rsrc(p.in[9], 2048 * 1024), (u32)(lane >> 2) * 1024 + (u32)(lane & 3) * 16, (u32)(idx * 64 + (aw >> 1) * 32 + 16 * k) * 1024 + (u32)xcd * 128 + (u32)(aw & 1) * 64, 0);
        SBAR();
        u32 fo[4], xo[4], fh[4], xh[4];
#pragma unroll
        for (int k = 0; k < 4; ++k) { fo[k] = lds0 + (u32)r32 * 128 + (u32)(((2 * k + hh) ^ ((r32 >> 1) & 7)) * 16); xo[k] = fo[k] + aw * 4096; fh[k] = fo[k] + 65536; xh[k] = xo[k] + 65536; }
#pragma unroll
        for (int k = 0; k < 4; ++k) asm volatile("" : "+v"(fo[k]), "+v"(xo[k]), "+v"(fh[k]), "+v"(xh[k]));
        u32 mysig = syncA + aw * 4, vsl = syncL, c1r = lds0 + L_CB1 + r32 * 8, c2r = lds0 + L_CB2 + r32 * 32, l16 = (u32)lane * 16;
        u32 c1w = c1r + aw * 256, c2w = c2r + aw * 8, evo = hh ? OOB : (u32)r32 * 16;
        u32 vo[2];
        {
            const int cb0 = c - 1 - hh - 4 * aw;
            const u32 vo0 = WS_E + (u32)(cb0 * 512 + pb * 32 + r32) * 16;
            vo[0] = cb0 >= 0 ? vo0 : OOB; vo[1] = cb0 >= 2 ? vo0 - 16384 : OOB;
        }
        asm volatile("" : "+v"(mysig), "+v"(vsl), "+v"(c1r), "+v"(c2r), "+v"(c1w), "+v"(c2w), "+v"(evo), "+v"(vo[0]), "+v"(vo[1]), "+v"(l16));
        STAMP(1);
        cf P[4], L8, L16, L32, W0, W1, coef, Q;
        {
            const float a_lr = bf2f(q_lr), a_li = bf2f(q_li);
            const float dt = rbf(__builtin_expf(bf2f(q_ld)));
            P[0] = cexpk(dt * a_lr, dt * a_li, 1.f);
            P[1] = csq(P[0]); P[2] = cmul(P[0], P[1]); P[3] = csq(P[1]);
            L8 = csq(P[3]); L16 = csq(L8); L32 = csq(L16);
            const cf L64 = csq(L32), L128 = csq(L64), L256 = csq(L128), L512 = csq(L256);
            W0 = hh ? L128 : cf{1.f, 0.f};
            if (aw & 1) W0 = cmul(W0, L512);
            if (aw & 2) W0 = cmul(W0, csq(L512));
            W1 = cmul(W0, L256);
            const float nr = P[0].x - 1.f, ni = P[0].y, inv = 2.f / (a_lr * a_lr + a_li * a_li);
            coef = cf{(nr * a_lr + ni * a_li) * inv, (ni * a_lr - nr * a_li) * inv};
            Q = hh ? cmul(coef, L16) : coef;
            if (aw & 1) Q = cmul(Q, L32);
            if (aw & 2) Q = cmul(Q, L64);
            asm volatile("" : "+v"(P[0]), "+v"(P[1]), "+v"(P[2]), "+v"(P[3]), "+v"(L8), "+v"(L16), "+v"(L32), "+v"(W0), "+v"(W1), "+v"(coef), "+v"(Q));
        }
        const u32 ta = rfl(wv0) + 1u, tb = tag2(ta);
        {
            if (aw == 0) lds_st<u32x2>(tagw, u32x2{ta, 1u});
            const u32 im = lds0 + L_UB + UB_PAD + (aw >> 1) * UB_SZ + (aw & 1) * 2048;
            lds_st<u32x4>(im + l16, ut[0]);
            lds_st<u32x4>(im + 1024 + l16, ut[1]);
            FENCE();
            const u32 tra = im + (u32)(8 * (lane & 3) + (m16 >> 2)) * 64 + (u32)q4 * 8;
#pragma unroll
            for (int k = 0; k < 2; ++k) { const u32x2 x = lds_tr(tra + k * 32), y = lds_tr(tra + k * 32 + 256); ut[k] = u32x4{x.x, x.y, y.x, y.y}; }
#pragma unroll
            for (int k = 0; k < 2; ++k) st128<0, true>(wss, ut[k], (u32)q4 * 16384 + (u32)m16 * 16, WS_UT + (u32)(16 * xcd + 8 * (aw & 1) + 4 * k) * 16384 + (u32)(2 * idx + (aw >> 1)) * 256);
        }
        u32 fvo = lane ? OOB : 0u;
        u32x2 tg = {ta, tb};
        asm volatile("" : "+v"(fvo), "+v"(tg));
        STAMP(2);
        f32x16 accR = {}, accI = {};
        {
            bf16x8 fa[4], fr[4], fi[4];
            auto rd = [&](int n) { const int s = n >> 2, k = n & 3, o = (s % 5) * 24576, ob = o + 16384;
                fa[n & 3] = lds_ld<bf16x8>(o < 65536 ? xo[k] + o : xh[k] + (o - 65536));
                fr[n & 3] = lds_ld<bf16x8>(ob < 65536 - 4096 ? fo[k] + ob : fh[k] + (ob - 65536));
                fi[n & 3] = lds_ld<bf16x8>(ob < 65536 - 4096 ? fo[k] + ob + 4096 : fh[k] + (ob + 4096 - 65536)); };
            wait4(syncL, 1);
            STAMP(10);
            u32x4 fl = *(volatile const LAS u32x4*)(uintptr_t)vsl;
            rd(0); rd(1); rd(2);
            SBAR();
#pragma unroll
            for (int n = 0; n < 32; ++n) {
                const int s = n >> 2, k = n & 3;
                if (k == 1 && s < 7) {
                    if (rfl(umin(umin(fl.x, fl.y), umin(fl.z, fl.w))) < (u32)(s + 2)) wait4(syncL, s + 2);
                    FENCE();
                }
                if (k == 2 && s < 6) fl = *(volatile const LAS u32x4*)(uintptr_t)vsl;
                accR = MFMA32(fa[k], fr[k], accR);
                SBAR();
                if (n + 3 < 32) rd(n + 3);
                if (k == 0) sig(mysig, s + 1);
                if (n == UFN) { WAITV(0); __builtin_amdgcn_raw_buffer_store_b64(tg, ws, fvo, WS_UFLAG + (u32)((2 * xcd + (aw & 1)) * 64 + 2 * idx + (aw >> 1)) * FS, 16); FENCE(); }
                accI = MFMA32(fa[k], fi[k], accI);
                SBAR();
            }
        }
        __builtin_amdgcn_s_setprio(3);
        STAMP(3);
        f32x2 hr[8], hi[8];
#pragma unroll
        for (int i = 0; i < 8; ++i) { hr[i] = f32x2{accR[2 * i], accR[2 * i + 1]}; hi[i] = f32x2{accI[2 * i], accI[2 * i + 1]}; }
#pragma unroll
        for (int i = 2; i < 8; ++i) {
            f32x2 nr = fma2(-P[0].y, hi[i - 2], fma2(P[0].x, hr[i - 2], hr[i]));
            f32x2 ni = fma2(P[0].y, hr[i - 2], fma2(P[0].x, hi[i - 2], hi[i]));
            hr[i] = nr; hi[i] = ni;
        }
        cf e0, e1, pre = {0.f, 0.f};
        {
            const f32x2 zr = fma2(-L8.y, hi[6], fma2(L8.x, hr[6], hr[7])), zi = fma2(L8.y, hr[6], fma2(L8.x, hi[6], hi[7]));
            const cf e16 = {__builtin_fmaf(-P[3].y, zi.x, __builtin_fmaf(P[3].x, zr.x, zr.y)), __builtin_fmaf(P[3].y, zr.x, __builtin_fmaf(P[3].x, zi.x, zi.y))};
            swap_halves(e16, e0, e1);
        }
        const cf e32 = cfma(e0, L16, e1);
        if (aw < 3) { lds_st<f32x2>(c1w, e32); FENCE(); }
        u32x4 g[2];
        if (aw < 3) {
#pragma unroll
            for (int k = 0; k < 2; ++k) g[k] = __builtin_amdgcn_raw_buffer_load_b128(ws, vo[k], 0, 16);
        }
        if (aw > 0) {
            cf w0, w1, w2;
#pragma unroll 1
            for (u32 spn = 0; spn < (1u << 22); ++spn) {
                asm volatile("ds_read_b64 %0, %3\n\tds_read_b64 %1, %3 offset:256\n\tds_read_b64 %2, %3 offset:512\n\ts_waitcnt lgkmcnt(0)" : "=&v"(w0), "=&v"(w1), "=&v"(w2) : "v"(c1r) : "memory");
                const u32 a = f2u(w0.x), b = f2u(w1.x), cc = f2u(w2.x);
                const u32 mx = a > b ? (a > cc ? a : cc) : (b > cc ? b : cc);
                if (__builtin_amdgcn_ballot_w64(mx == SENT) == 0) break;
            }
            const cf p2 = cfma(w0, L32, w1), p3 = cfma(p2, L32, w2);
            pre = aw == 1 ? w0 : (aw == 2 ? p2 : p3);
            if (aw == 3) {
                if (c < 15) { const cf E = cfma(p3, L32, e32); st128<0, true>(wss, u32x4{f2u(E.x), ta, f2u(E.y), tb}, evo, WS_E + (u32)(c * 512 + pb * 32) * 16); }
#pragma unroll
                for (int k = 0; k < 2; ++k) g[k] = __builtin_amdgcn_raw_buffer_load_b128(ws, vo[k], 0, 16);
            }
        }
        STAMP(4);
        const int th = aw & 1, kh = aw >> 1;
        const int t0 = tb64 * 64 + th * 32, dcol = db * 64 + kh * 32, t32 = tb64 * 2 + th;
        u32 xk[16];
        {
            const rsrc_t rx = mk_rsrc(p.in[0], 2048 * 1024);
#pragma unroll
            for (int r = 0; r < 16; ++r)
                xk[r] = __builtin_amdgcn_raw_buffer_load_b16(rx, (u32)hh * 4096 + (u32)g32 * 2 + (r & 3) * 1024, (u32)t0 * 1024 + (u32)dcol * 2 + (r >> 2) * 8192, 0);
        }
        const u32 dsk = __builtin_amdgcn_raw_buffer_load_b16(mk_rsrc(p.in[8], 1024), (u32)g32 * 2, (u32)dcol * 2, 0);
        SBAR();
        f32x2 cpr[2], cpi[2], cqr[2], cqi[2];
        {
            { const cf pre1 = cfma(pre, L16, e0); pre = hh ? pre1 : pre; }
            cf s[4], q[4];
            s[0] = pre; q[0] = Q;
            s[1] = cfma(s[0], P[3], cf{hr[6].x, hi[6].x}); s[2] = cfma(s[1], P[3], cf{hr[6].y, hi[6].y}); s[3] = cfma(s[2], P[3], cf{hr[7].x, hi[7].x});
#pragma unroll
            for (int a = 1; a < 4; ++a) q[a] = cmul(q[a - 1], P[3]);
#pragma unroll
            for (int a = 0; a < 4; ++a) s[a] = cmul(s[a], coef);
#pragma unroll
            for (int u = 0; u < 2; ++u) { cpr[u] = f32x2{s[2 * u].x, s[2 * u + 1].x}; cpi[u] = f32x2{s[2 * u].y, s[2 * u + 1].y}; cqr[u] = f32x2{q[2 * u].x, q[2 * u + 1].x}; cqi[u] = f32x2{q[2 * u].y, q[2 * u + 1].y};
                asm volatile("" : "+v"(cpr[u]), "+v"(cpi[u]), "+v"(cqr[u]), "+v"(cqi[u])); }
        }
#pragma unroll
        for (int i = 0; i < 8; ++i) {
            f32x2 nr = fma2(-coef.y, hi[i], coef.x * hr[i]);
            f32x2 ni = fma2(-coef.y, hr[i], -coef.x * hi[i]);
            hr[i] = nr; hi[i] = ni;
            asm volatile("" : "+v"(hr[i]), "+v"(hi[i]));
        }
        const u32 scr = lds0 + 7 * 16384 + aw * 4096;
        u32 wa0 = scr + (u32)(r32 ^ ((r32 >> 3) & 3)) * 64 + (u32)(((2 * hh) ^ ((r32 >> 1) & 3)) * 16), wa1 = wa0 ^ 16;
        u32 ra[4], pvo = lane < 8 ? WS_HFLAG + (u32)(t32 * 16 + kh * 8 + lane) * FS : OOB;
        { const int q = m16 >> 2, pp = lane & 3;
          const u32 b0 = scr + (u32)(8 * q + ((2 * hh) ^ q)) * 64 + (u32)(((2 * (q4 & 1) + (pp >> 1)) ^ hh) * 16) + (u32)(pp & 1) * 8;
          ra[0] = b0; ra[1] = b0 ^ 64; ra[2] = (b0 ^ 32) + 256; ra[3] = (b0 ^ 96) + 256; }
        asm volatile("" : "+v"(ra[0]), "+v"(ra[1]), "+v"(ra[2]), "+v"(ra[3]), "+v"(wa0), "+v"(wa1), "+v"(pvo));
        SBAR();
        cf ev[2] = {cf{0.f, 0.f}, cf{0.f, 0.f}};
#pragma unroll 1
        for (u32 spn = 0; spn < SPIN_CAP; ++spn) {
#pragma unroll
            for (int k = 0; k < 2; ++k) {
                const bool good = ((g[k].y ^ ta) | (g[k].w ^ tb)) == 0;
                ev[k].x = good ? u2f(g[k].x) : ev[k].x; ev[k].y = good ? u2f(g[k].z) : ev[k].y;
                vo[k] = good ? OOB : vo[k];
            }
            if (__builtin_amdgcn_ballot_w64(umin(vo[0], vo[1]) != OOB) == 0) break;
            FENCE();
#pragma unroll
            for (int k = 0; k < 2; ++k) g[k] = __builtin_amdgcn_raw_buffer_load_b128(ws, vo[k], 0, 16);
        }
        {
            cf G0, G1;
            swap_halves(cfma(ev[1], W1, cmul(ev[0], W0)), G0, G1);
            lds_st<f32x2>(c2w, G0 + G1);
            f32x4 h01, h23;
#pragma unroll 1
            for (u32 spn = 0; spn < (1u << 22); ++spn) {
                asm volatile("ds_read_b128 %0, %2\n\tds_read_b128 %1, %2 offset:16\n\ts_waitcnt lgkmcnt(0)" : "=&v"(h01), "=&v"(h23) : "v"(c2r) : "memory");
                const u32 a = f2u(h01[0]), b = f2u(h01[2]), cc = f2u(h23[0]), dd = f2u(h23[2]);
                const u32 m1 = a > b ? a : b, m2 = cc > dd ? cc : dd;
                if (__builtin_amdgcn_ballot_w64((m1 > m2 ? m1 : m2) == SENT) == 0) break;
            }
            STAMP(5);
            const f32x4 hs4 = h01 + h23;
            const cf H = {hs4[0] + hs4[2], hs4[1] + hs4[3]};
            f32x2 gr[2], gi[2];
#pragma unroll
            for (int u = 0; u < 2; ++u) { gr[u] = fma2(-H.y, cqi[u], fma2(H.x, cqr[u], cpr[u])); gi[u] = fma2(H.x, cqi[u], fma2(H.y, cqr[u], cpi[u])); }
#pragma unroll
            for (int i = 0; i < 8; ++i) hr[i] = fma2(-P[i >> 1].y, gi[i & 1], fma2(P[i >> 1].x, gr[i & 1], hr[i]));
            lds_st<u32x4>(wa0, u32x4{pkbf(hr[0].x, hr[2].x), pkbf(hr[4].x, hr[6].x), pkbf(hr[0].y, hr[2].y), pkbf(hr[4].y, hr[6].y)});
            lds_st<u32x4>(wa1, u32x4{pkbf(hr[1].x, hr[3].x), pkbf(hr[5].x, hr[7].x), pkbf(hr[1].y, hr[3].y), pkbf(hr[5].y, hr[7].y)});
            FENCE();
            u32x2 tv[8];
#pragma unroll
            for (int s = 0; s < 4; ++s) tv[s] = lds_tr(ra[s]);
            SBAR();
#pragma unroll
            for (int i = 0; i < 8; ++i) hi[i] = fma2(-P[i >> 1].y, gr[i & 1], fma2(-P[i >> 1].x, gi[i & 1], hi[i]));
            lds_st<u32x4>(wa0 + 2048, u32x4{pkbf(hi[0].x, hi[2].x), pkbf(hi[4].x, hi[6].x), pkbf(hi[0].y, hi[2].y), pkbf(hi[4].y, hi[6].y)});
            lds_st<u32x4>(wa1 + 2048, u32x4{pkbf(hi[1].x, hi[3].x), pkbf(hi[5].x, hi[7].x), pkbf(hi[1].y, hi[3].y), pkbf(hi[5].y, hi[7].y)});
            FENCE();
#pragma unroll
            for (int s = 0; s < 4; ++s) tv[4 + s] = lds_tr(ra[s] + 2048);
            const u32 hs = WS_H + (u32)((c * 4 + aw) * 16 + pb) * 4096;
            st128<0, true>(wss, u32x4{tv[0].x, tv[0].y, tv[1].x, tv[1].y}, l16, hs);
            st128<1024, true>(wss, u32x4{tv[2].x, tv[2].y, tv[3].x, tv[3].y}, l16, hs);
            st128<2048, true>(wss, u32x4{tv[4].x, tv[4].y, tv[5].x, tv[5].y}, l16, hs);
            st128<3072, true>(wss, u32x4{tv[6].x, tv[6].y, tv[7].x, tv[7].y}, l16, hs);
            sig(mysig, 9);
        }
        f32x16 y0, y1 = {};
        {
            const float dk = bf2f(dsk);
#pragma unroll
            for (int r = 0; r < 16; r += 2) { const u32 w = pkbf(bf2f(xk[r]) * dk, bf2f(xk[r + 1]) * dk); y0[r] = u2f(w << 16); y0[r + 1] = u2f(w & 0xFFFF0000u); }
            asm volatile("" : "+v"(y0));
        }
        WAITV(0);
        __builtin_amdgcn_raw_buffer_store_b64(tg, ws, fvo, WS_HFLAG + (u32)((c * 4 + aw) * 16 + pb) * FS, 16);
        FENCE();
        STAMP(6);
        u32 ca[4], ov[4];
#pragma unroll
        for (int k = 0; k < 4; ++k) { ca[k] = fo[k] + (u32)kh * 65536; ov[k] = (u32)hh * 16384 + (u32)g32 * 4 + k * 4096; asm volatile("" : "+v"(ca[k]), "+v"(ov[k])); }
        u32 xb = lds0 + (u32)(4 * kh + (db & 3)) * 16384 + (u32)th * 4096 + l16;
        asm volatile("" : "+v"(xb));
        wait4(syncL, 9);
        bf16x8 cb0[4], cb1[4];
        auto cad = [&](int f) { const int jj = f >> 3, a = (f >> 2) & 1, s = f & 3;
            return ca[2 * a + (s & 1)] + (u32)((jj + db) & 3) * 16384 + (s >> 1) * 8192; };
#pragma unroll
        for (int f = 0; f < 3; ++f) { const u32 ad = cad(f); cb0[f] = lds_ld<bf16x8>(ad); cb1[f] = lds_ld<bf16x8>(ad + 4096); }
        poll_flags(ws, pvo, ta, tb);
        STAMP(7);
        {
            u32x4 hf[16];
            auto ldh = [&](int f) { const u32 cg = (u32)(4 * kh + (((f >> 3) + db) & 3));
                return __builtin_amdgcn_raw_buffer_load_b128(ws, l16 + (f & 3) * 1024, WS_H + (u32)t32 * 65536 + cg * 8192 + ((f >> 2) & 1) * 4096, 16); };
#pragma unroll
            for (int f = 0; f < HWIN; ++f) { hf[f] = ldh(f); SBAR(); }
#pragma unroll
            for (int f = 0; f < 32; ++f) {
                bf16x8 av = __builtin_bit_cast(bf16x8, hf[f & 15]);
                y0 = MFMA32(av, cb0[f & 3], y0);
                SBAR();
                if (f + 3 < 32) { const u32 ad = cad(f + 3); cb0[(f + 3) & 3] = lds_ld<bf16x8>(ad); cb1[(f + 3) & 3] = lds_ld<bf16x8>(ad + 4096); }
                y1 = MFMA32(av, cb1[f & 3], y1);
                SBAR();
                if (f + HWIN < 32) { hf[(f + HWIN) & 15] = ldh(f + HWIN); SBAR(); }
                if (f == 8) { sig(mysig, 10); SBAR(); }
                if (f == 28) { wait4(syncA, 10); SBAR(); }
            }
        }
        STAMP(8);
        {
#pragma unroll
            for (int k = 0; k < 4; ++k) lds_st<f32x4>(xb + k * 1024, f32x4{y1[4 * k], y1[4 * k + 1], y1[4 * k + 2], y1[4 * k + 3]});
            sig(mysig, 11);
            wait1(syncA + (aw ^ 2) * 4, 11);
            const rsrc_t ro = mk_rsrc(p.out, 2048 * 4096);
            f32x4 o[4];
#pragma unroll
            for (int k = 0; k < 4; ++k) o[k] = lds_ld<f32x4>((xb ^ 65536) + k * 1024);
#pragma unroll
            for (int k = 0; k < 4; ++k) {
#pragma unroll
                for (int j = 0; j < 4; ++j)
                    __builtin_amdgcn_raw_buffer_store_b32(f2u(y0[4 * k + j] + o[k][j]), ro, ov[j], (u32)(t0 + 8 * k) * 4096 + (u32)dcol * 4, 0);
            }
            if (tb64 == 31 && db == 0 && aw == 0) __builtin_amdgcn_raw_buffer_store_b32(ta, ws, fvo, WS_W, 16);
        }
        STAMP(9);
    } else {
        const int bw = wv - 8, dsel = bw >> 1, half = bw & 1;
        if (ABL >= 1) return;
        __builtin_amdgcn_s_setprio(PRK);
        const int d = 64 * xcd + 2 * idx + dsel;
        const u32 s0 = lds0 + L_S0 + bw * S0_SZ, kc = lds0 + L_KC + dsel * KC_SZ, ub = lds0 + L_UB + dsel * UB_SZ;
        const u32 mysig = syncK + bw * 4, psig = syncK + (bw ^ 1) * 4;
        u32 pr[6];
#pragma unroll
        for (int i = 0; i < 6; ++i) pr[i] = __builtin_amdgcn_raw_buffer_load_b16(mk_rsrc(p.in[10 + i], 512 * 256), (u32)lane * 2, (u32)(d * 128 + half * 64) * 2, 0);
        const u32 q_ld = __builtin_amdgcn_raw_buffer_load_b16(mk_rsrc(p.in[16], 1024), 0, (u32)d * 2, 0);
        SBAR();
        lds_st<u32x2>(ub + umin(lane, 59) * 8, u32x2{0u, 0u});
        u32 ga[19];
        {
            const int lag0 = 256 * rr + 128 * half + 127 - 18 * sp;
#pragma unroll
            for (int e = 0; e < 19; ++e) {
                const int lag = lag0 - e;
                const u32 bits = pkbf((float)lag, 0.f) & 0xFFFFu;
                const u32 ix = lag < 128 ? (u32)lag : bits - (0x4300u - 128u);
                ga[e] = lag < 0 ? ub : kc + 2 * ix;
                asm volatile("" : "+v"(ga[e]));
            }
        }
        asm volatile("" :: "v"(cpf));
        STAMP(1);
        f32x4 ka[3];
        {
            const float dt = rbf(__builtin_expf(bf2f(q_ld)));
            const float zr = dt * bf2f(pr[0]), zi = dt * bf2f(pr[1]);
            const cf db_ = {dt * bf2f(pr[2]), -dt * bf2f(pr[3])}, cc = {bf2f(pr[4]), -bf2f(pr[5])};
            const cf E1 = cexpk(zr, zi, 1.f), E8c = cexpk(zr, -zi, 8.f), E128 = cexpk(zr, zi, 128.f);
            const u32 wa = s0 + lane * 4, ra = s0 + (u32)m16 * 288 + (u32)q4 * 16;
            cf V[16], R[8], W[17];
            { const cf E16 = cmul(E8c, E8c), E32 = cmul(E16, E16), E64 = cmul(E32, E32);
              V[0] = cmul(cc, db_); V[1] = cmul(V[0], E8c);
#pragma unroll
              for (int j = 0; j < 2; ++j) V[2 + j] = cmul(V[j], E16);
#pragma unroll
              for (int j = 0; j < 4; ++j) V[4 + j] = cmul(V[j], E32);
#pragma unroll
              for (int j = 0; j < 8; ++j) V[8 + j] = cmul(V[j], E64); }
            R[0] = cf{1.f, 0.f}; R[1] = E1; R[2] = cmul(E1, E1); R[3] = cmul(R[1], R[2]); R[4] = cmul(R[2], R[2]);
#pragma unroll
            for (int r = 1; r < 4; ++r) R[4 + r] = cmul(R[r], R[4]);
            W[1] = E128; W[2] = cmul(W[1], W[1]); W[3] = cmul(W[1], W[2]); W[4] = cmul(W[2], W[2]);
#pragma unroll
            for (int k = 1; k <= 4; ++k) W[4 + k] = cmul(W[k], W[4]);
#pragma unroll
            for (int k = 1; k <= 8; ++k) W[8 + k] = cmul(W[k], W[8]);
#pragma unroll
            for (int j = 0; j < 16; ++j) lds_st<u32>(wa + j * 288, pkc(V[j]));
            FENCE();
            bf16x8 vf[4], uf[4];
#pragma unroll
            for (int s = 0; s < 4; ++s) vf[s] = lds_ld<bf16x8>(ra + s * 64);
            FENCE();
#pragma unroll
            for (int r = 0; r < 8; ++r) { lds_st<u32>(wa + r * 288, pkc(R[r])); lds_st<u32>(wa + (8 + r) * 288, pkc(r ? cmul(R[r], W[1]) : W[1])); }
            FENCE();
#pragma unroll
            for (int s = 0; s < 4; ++s) uf[s] = lds_ld<bf16x8>(ra + s * 64);
            FENCE();
#pragma unroll
            for (int k = 0; k < 2; ++k)
#pragma unroll
                for (int r = 0; r < 4; ++r) lds_st<u32>(wa + (4 * k + r) * 288, pkc(r ? cmul(R[2 * r], W[2 + k]) : W[2 + k]));
#pragma unroll
            for (int k = 0; k < 4; ++k) { lds_st<u32>(wa + (8 + 2 * k) * 288, pkc(W[4 + k])); lds_st<u32>(wa + (9 + 2 * k) * 288, pkc(cmul(W[4 + k], R[4]))); }
            FENCE();
            ka[0] = f32x4{};
#pragma unroll
            for (int s = 0; s < 4; ++s) ka[0] = MFMA16(uf[s], vf[s], ka[0]);
#pragma unroll
            for (int s = 0; s < 4; ++s) uf[s] = lds_ld<bf16x8>(ra + s * 64);
            FENCE();
#pragma unroll
            for (int k = 0; k < 9; ++k) lds_st<u32>(wa + k * 288, pkc(W[8 + k]));
            FENCE();
            ka[1] = f32x4{};
#pragma unroll
            for (int s = 0; s < 4; ++s) ka[1] = MFMA16(uf[s], vf[s], ka[1]);
#pragma unroll
            for (int s = 0; s < 4; ++s) uf[s] = lds_ld<bf16x8>(ra + s * 64);
            FENCE();
            ka[2] = f32x4{};
#pragma unroll
            for (int s = 0; s < 4; ++s) ka[2] = MFMA16(uf[s], vf[s], ka[2]);
        }
        STAMP(3);
        const u32 ta = get_tag(tagw), tb = tag2(ta);
        u32 uvo = WS_UFLAG + (u32)((d >> 5) * 64 + lane) * FS;
        u32x2 uf = __builtin_amdgcn_raw_buffer_load_b64(ws, uvo, 0, 16);
        SBAR();
#pragma unroll
        for (int t = 0; t < 3; ++t) lds_st<f32x4>(s0 + t * 1024 + lane * 16, ka[t]);
        sig(mysig, 1);
        wait1(psig, 1);
        u32x4 ur[2];
        {
            const u32 ps0 = lds0 + L_S0 + (bw ^ 1) * S0_SZ;
#pragma unroll
            for (int t = 0; t < 3; ++t) ka[t] += lds_ld<f32x4>(ps0 + t * 1024 + lane * 16);
            sig(mysig, 2);
            lds_st<u32x2>(kc + 2 * (128 * (q4 >> 1) + 4 * (q4 & 1) + 8 * m16), u32x2{pkbf(ka[0][0], ka[0][1]), pkbf(ka[0][2], ka[0][3])});
            const u32 a0 = q4 < 2 ? 256 + 64 * q4 + 4 * m16 : 384 + 64 * (q4 - 2) + 2 * m16, a1 = a0 + (q4 < 2 ? 2 : 32);
            lds_st<u32>(kc + 2 * a0, pkbf(ka[1][0], ka[1][1]));
            lds_st<u32>(kc + 2 * a1, pkbf(ka[1][2], ka[1][3]));
            const u32 w01 = pkbf(ka[2][0], ka[2][1]), w23 = pkbf(ka[2][2], ka[2][3]);
            const u32 a2 = kc + 2 * (512 + (q4 == 3 ? 144 : 64 * q4) + m16);
            lds_st<u16>(a2, (u16)w01); lds_st<u16>(a2 + 32, (u16)(w01 >> 16)); lds_st<u16>(a2 + 64, (u16)w23); lds_st<u16>(a2 + 96, (u16)(w23 >> 16));
            FENCE();
            uvo = ((uf.x ^ ta) | (uf.y ^ tb)) == 0 ? OOB : uvo;
            if (__builtin_amdgcn_ballot_w64(uvo != OOB) != 0) poll_flags(ws, uvo, ta, tb);
            FENCE();
#pragma unroll
            for (int k = 0; k < 2; ++k) ur[k] = __builtin_amdgcn_raw_buffer_load_b128(ws, (u32)(lane >> 1) * 64 + (u32)(lane & 1) * 16 + k * 2048, WS_UT + (u32)(16 * xcd + (idx >> 1)) * 16384 + (u32)(idx & 1) * 32 + (u32)bw * 4096, 16);
            SBAR();
            u32 gv[19];
#pragma unroll
            for (int e = 0; e < 19; ++e) gv[e] = lds_ld<u16>(ga[e]);
            wait1(psig, 2);
#pragma unroll
            for (int k = 0; k < 9; ++k) {
                lds_st<u32>(s0 + 36 * lane + 4 * k, gv[2 * k] | (gv[2 * k + 1] << 16));
                lds_st<u32>(s0 + 2368 + 36 * lane + 4 * k, gv[2 * k + 1] | (gv[2 * k + 2] << 16));
            }
            FENCE();
        }
        STAMP(4);
        wait4(syncA, 1);
#pragma unroll
        for (int k = 0; k < 2; ++k) lds_st<u32x4>(lds0 + L_UB + UB_PAD + (u32)(lane & 1) * UB_SZ + (u32)(lane >> 1) * 16 + (u32)bw * 1024 + k * 512, ur[k]);
        sig(mysig, 3);
        wait4(syncK, 3);
        __builtin_amdgcn_s_setprio(0);
        STAMP(5);
        f32x4 acc[8];
#pragma unroll
        for (int i = 0; i < 8; ++i) acc[i] = f32x4{};
        {
            const u32 bb = ub + 256 - 256 * half + 32 * m16 + 16 * q4;
            const u32 sgl = (u32)(1 - (m16 & 1));
            const u32 ab = s0 + 4 * (592 * sgl + ((111 - m16 - sgl) >> 1) + 4 * q4 - 48);
            bf16x8 bfr[2][8], afr[4];
            auto lda = [&](int n) { const int r = n >> 3, i = n & 7; const u32 o = ab + 4 * (72 * i + 16 * (3 - r));
                u32x4 av = {lds_ld<u32>(o), lds_ld<u32>(o + 4), lds_ld<u32>(o + 8), lds_ld<u32>(o + 12)}; return __builtin_bit_cast(bf16x8, av); };
            afr[0] = lda(0);
#pragma unroll
            for (int m = 0; m < 8; ++m) bfr[0][m] = lds_ld<bf16x8>(bb + 512 * m + 192);
            afr[1] = lda(1); afr[2] = lda(2);
            SBAR();
#pragma unroll
            for (int r = 0; r < 4; ++r) {
                const int cu = r & 1, nx = cu ^ 1;
#pragma unroll
                for (int i = 0; i < 8; ++i) {
                    const int n = 8 * r + i;
                    if (n + 3 < 32) afr[(n + 3) & 3] = lda(n + 3);
                    if (r < 3 && i < 4) { bfr[nx][2 * i] = lds_ld<bf16x8>(bb + 1024 * i + 64 * (2 - r)); bfr[nx][2 * i + 1] = lds_ld<bf16x8>(bb + 1024 * i + 512 + 64 * (2 - r)); }
#pragma unroll
                    for (int cb = i; cb < 8; ++cb) acc[cb] = MFMA16(afr[n & 3], bfr[cu][cb - i], acc[cb]);
                    SBAR();
                }
            }
        }
        FENCE();
        __builtin_amdgcn_s_setprio(3);
        STAMP(6);
        {
            const u32 ps0 = lds0 + L_S0 + (bw ^ 1) * S0_SZ;
            f32x4 kp[4];
            if (half) {
#pragma unroll
                for (int k = 0; k < 4; ++k) { lds_st<f32x4>(s0 + k * 1024 + lane * 16, acc[k]); kp[k] = acc[4 + k]; }
            } else {
#pragma unroll
                for (int k = 0; k < 4; ++k) { lds_st<f32x4>(s0 + k * 1024 + lane * 16, acc[4 + k]); kp[k] = acc[k]; }
            }
            sig(mysig, 4);
            wait1(psig, 4);
#pragma unroll
            for (int k = 0; k < 4; ++k) kp[k] += lds_ld<f32x4>(ps0 + k * 1024 + lane * 16);
            st128<0, true>(wss, u32x4{pkbf(kp[0][0], kp[0][1]), pkbf(kp[0][2], kp[0][3]), pkbf(kp[1][0], kp[1][1]), pkbf(kp[1][2], kp[1][3])}, (u32)lane * 16, WS_YT + (u32)d * 4096 + (u32)half * 2048);
            st128<1024, true>(wss, u32x4{pkbf(kp[2][0], kp[2][1]), pkbf(kp[2][2], kp[2][3]), pkbf(kp[3][0], kp[3][1]), pkbf(kp[3][2], kp[3][3])}, (u32)lane * 16, WS_YT + (u32)d * 4096 + (u32)half * 2048);
            WAITV(0);
            __builtin_amdgcn_raw_buffer_store_b64(u32x2{ta, tb}, ws, lane ? OOB : 0u, WS_YFLAG + (u32)(half * 512 + d) * FS, 16);
        }
        STAMP(7);
    }
    STAMP_OUT();
}

#if defined(__HIP_DEVICE_COMPILE__)
#pragma clang attribute pop
#endif

extern "C" void kernel_launch(void* const* d_in, const int* in_sizes, int n_in, void* d_out, int out_size, void* d_ws, size_t ws_size, hipStream_t stream) {
    Params p;
    for (int i = 0; i < 18; ++i) p.in[i] = d_in[i];
    p.out = (float*)d_out;
    p.ws = (char*)d_ws;
    (void)hipFuncSetAttribute((const void*)ssm_fused, hipFuncAttributeMaxDynamicSharedMemorySize, LDS_BYTES);
    ssm_fused<<<256, 768, LDS_BYTES, stream>>>(p);
}
```

```cpp
#define OAUX 2
#include <hip/hip_runtime.h>
#include <stdint.h>

typedef unsigned short u16;
typedef unsigned u32;
typedef unsigned long long u64;
typedef short bf16x8 __attribute__((ext_vector_type(8)));
typedef short s16x4 __attribute__((ext_vector_type(4)));
typedef float f32x2 __attribute__((ext_vector_type(2)));
typedef float f32x4 __attribute__((ext_vector_type(4)));
typedef float f32x16 __attribute__((ext_vector_type(16)));
typedef unsigned u32x2 __attribute__((ext_vector_type(2)));
typedef unsigned u32x4 __attribute__((ext_vector_type(4)));
typedef __bf16 bf16x2_t __attribute__((ext_vector_type(2)));
#define LAS __attribute__((address_space(3)))
typedef __amdgpu_buffer_rsrc_t rsrc_t;

#define ABL 0
#define PRA 2
#define PRK 1
#define UM 7
#define DBG1 0
#define LSTAG 5
#define CTHR 0
#define PRL 0
#define WSL 0
#define LWIN 12
#define UFN 6
#define TOUCH 0
#define CPF 1
#define STAMPS 0

constexpr u32 WS_W = 0;
constexpr u32 FS = 128;
constexpr u32 WS_UFLAG = 524288;
constexpr u32 WS_HFLAG = 131072;
constexpr u32 WS_YFLAG = 262144;
constexpr u32 WS_E = 393216;
constexpr u32 WS_UT = 1u << 20;
constexpr u32 WS_YT = 3u << 20;
constexpr u32 WS_H = 5u << 20;
constexpr u32 WS_BYTES = 9u << 20;
constexpr u32 WS_STAMP = 9u << 20;
constexpr u32 OOB = 0x7FFFFF00u;
constexpr u32 L_R2 = 131072;
constexpr u32 L_S0 = L_R2, S0_SZ = 4672;
constexpr u32 L_KC = L_R2 + 4 * S0_SZ, KC_SZ = 1440;
constexpr u32 L_UB = L_KC + 2 * KC_SZ, UB_PAD = 480, UB_SZ = UB_PAD + 4096;
constexpr u32 L_CB1 = L_UB + 2 * UB_SZ;
constexpr u32 L_CB2 = L_CB1 + 768;
constexpr u32 L_SYNC = L_CB2 + 1024;
constexpr u32 LDS_BYTES = 163840;
static_assert(L_SYNC + 64 <= LDS_BYTES, "lds");
constexpr int NWAVES = 12;
#define HWIN 10
#define PSLEEP 1
constexpr u32 SPIN_CAP = 1u << 15;
constexpr u32 CODE_BYTES = 24576;
constexpr u32 CODE_SLICES = 3;
constexpr u32 SENT = 0xFFFFFFFFu;

struct Params { const void* in[18]; float* out; char* ws; };

#if defined(__HIP_DEVICE_COMPILE__)
#pragma clang attribute push(__attribute__((target("no-packed-fp32-ops"))), apply_to = function)
#endif
template <int I> struct ic { static constexpr int v = I; };
template <int B, int E, typename F> __device__ __forceinline__ void sfor(F&& f) { if constexpr (B < E) { f(ic<B>{}); sfor<B + 1, E>(f); } }
#define INL __attribute__((always_inline))
__device__ __forceinline__ u32 umin(u32 a, u32 b) { return a < b ? a : b; }
__device__ __forceinline__ float u2f(u32 v) { return __builtin_bit_cast(float, v); }
__device__ __forceinline__ u32 f2u(float v) { return __builtin_bit_cast(u32, v); }

__device__ __forceinline__ float bf2f(u32 v) { return u2f(v << 16); }
__device__ __forceinline__ u32 pkbf(float lo, float hi) { f32x2 a = {lo, hi}; return __builtin_bit_cast(u32, __builtin_convertvector(a, bf16x2_t)); }
__device__ __forceinline__ float rbf(float x) { return u2f(pkbf(x, x) << 16); }
__device__ __forceinline__ u32x4 mk_srd(const void* p, u32 bytes) { u64 a = (u64)p; u32x4 r = {(u32)a, (u32)(a >> 32) & 0xFFFFu, bytes, 0x00020000u}; return r; }
__device__ __forceinline__ rsrc_t mk_rsrc(const void* p, u32 bytes) { return __builtin_amdgcn_make_buffer_rsrc((void*)p, 0, bytes, 0x00020000); }
template <int IMM, bool SC1> __device__ __forceinline__ void st128(u32x4 srd, u32x4 v, u32 voff, u32 soff) {
    if (SC1) asm volatile("buffer_store_dwordx4 %0, %1, %2, %3 offen offset:%4 sc1\n\ts_nop 1" :: "v"(v), "v"(voff), "s"(srd), "s"(soff), "i"(IMM) : "memory");
    else if (OAUX == 2) asm volatile("buffer_store_dwordx4 %0, %1, %2, %3 offen offset:%4 nt\n\ts_nop 1" :: "v"(v), "v"(voff), "s"(srd), "s"(soff), "i"(IMM) : "memory");
    else if (OAUX == 16) asm volatile("buffer_store_dwordx4 %0, %1, %2, %3 offen offset:%4 sc1\n\ts_nop 1" :: "v"(v), "v"(voff), "s"(srd), "s"(soff), "i"(IMM) : "memory");
    else asm volatile("buffer_store_dwordx4 %0, %1, %2, %3 offen offset:%4\n\ts_nop 1" :: "v"(v), "v"(voff), "s"(srd), "s"(soff), "i"(IMM) : "memory");
}
#define SBAR() __builtin_amdgcn_sched_barrier(0)
#define WAITV(N) asm volatile("s_waitcnt vmcnt(" #N ")" ::: "memory")
#define WAITL() asm volatile("s_waitcnt lgkmcnt(0)" ::: "memory")
#define FENCE() asm volatile("" ::: "memory")
template <typename T> __device__ __forceinline__ T lds_ld(u32 a) { return *(const LAS T*)(uintptr_t)a; }
template <typename T> __device__ __forceinline__ void lds_st(u32 a, T v) { *(LAS T*)(uintptr_t)a = v; }
__device__ __forceinline__ u32x2 lds_tr(u32 a) { return __builtin_bit_cast(u32x2, __builtin_amdgcn_ds_read_tr16_b64_v4i16((LAS s16x4*)(uintptr_t)a)); }
__device__ __forceinline__ void sig(u32 addr, u32 val) { asm volatile("ds_write_b32 %0, %1" :: "v"(addr), "v"(val) : "memory"); }
__device__ __forceinline__ u32 rfl(u32 v) { return (u32)__builtin_amdgcn_readfirstlane(v); }
__device__ __forceinline__ u32 peek4(u32 addr) { u32x4 v; asm volatile("ds_read_b128 %0, %1\n\ts_waitcnt lgkmcnt(0)" : "=v"(v) : "v"(addr) : "memory"); return rfl(umin(umin(v.x, v.y), umin(v.z, v.w))); }
__device__ __forceinline__ u32 peek3(u32 addr) { u32x4 v; asm volatile("ds_read_b128 %0, %1\n\ts_waitcnt lgkmcnt(0)" : "=v"(v) : "v"(addr) : "memory"); return rfl(umin(umin(v.x, v.y), v.z)); }
__device__ __forceinline__ u32 peek1(u32 addr) { u32 v; asm volatile("ds_read_b32 %0, %1\n\ts_waitcnt lgkmcnt(0)" : "=v"(v) : "v"(addr) : "memory"); return rfl(v); }
#define SPINW(cond, slp) do { _Pragma("unroll 1") for (u32 sp_ = 0; (cond) && sp_ < (1u << 22); ++sp_) { if (slp) __builtin_amdgcn_s_sleep(slp); } } while (0)
__device__ __forceinline__ void wait4(u32 addr, u32 val) { SPINW(peek4(addr) < val, WSL); }
__device__ __forceinline__ void wait4s(u32 addr, u32 val) { SPINW(peek4(addr) < val, 2); }
__device__ __forceinline__ void wait3(u32 addr, u32 val) { SPINW(peek3(addr) < val, WSL); }
__device__ __forceinline__ void wait1(u32 addr, u32 val) { SPINW(peek1(addr) < val, WSL); }
__device__ __forceinline__ void wait1s(u32 addr, u32 val) { SPINW(peek1(addr) < val, 2); }
__device__ __forceinline__ u32 get_tag(u32 addr) {
    u32x2 v = {0u, 0u};
#pragma unroll 1
    for (u32 s = 0; s < (1u << 22); ++s) {
        asm volatile("ds_read_b64 %0, %1\n\ts_waitcnt lgkmcnt(0)" : "=v"(v) : "v"(addr) : "memory");
        if (__builtin_amdgcn_readfirstlane(v.y) != 0) break;
    }
    return __builtin_amdgcn_readfirstlane(v.x);
}
__device__ __forceinline__ u32 tag2(u32 t) { return t * 2654435761u + 0x2545F491u; }
typedef f32x2 cf;
__device__ __forceinline__ cf cmul(cf a, cf b) { return cf{__builtin_fmaf(-a.y, b.y, a.x * b.x), __builtin_fmaf(a.y, b.x, a.x * b.y)}; }
__device__ __forceinline__ cf csq(cf a) { return cf{__builtin_fmaf(-a.y, a.y, a.x * a.x), (a.x + a.x) * a.y}; }
__device__ __forceinline__ cf cfma(cf a, cf b, cf c) { return cf{__builtin_fmaf(-a.y, b.y, __builtin_fmaf(a.x, b.x, c.x)), __builtin_fmaf(a.y, b.x, __builtin_fmaf(a.x, b.y, c.y))}; }
__device__ __forceinline__ f32x2 fma2(float a, f32x2 b, f32x2 c) { return f32x2{__builtin_fmaf(a, b.x, c.x), __builtin_fmaf(a, b.y, c.y)}; }
__device__ __forceinline__ u32 pkc(cf a) { return __builtin_bit_cast(u32, __builtin_convertvector(a, bf16x2_t)); }
__device__ __forceinline__ cf cexpk(float zr, float zi, float k) {
    float m = __builtin_amdgcn_exp2f(zr * (k * 1.4426950408889634f));
    float f = __builtin_amdgcn_fractf(zi * (k * 0.15915494309189535f));
    return cf{m * __builtin_amdgcn_cosf(f), m * __builtin_amdgcn_sinf(f)};
}
__device__ __forceinline__ void swap_halves(cf v, cf& lo, cf& hi) {
    auto a = __builtin_amdgcn_permlane32_swap(f2u(v.x), f2u(v.x), false, false);
    auto b = __builtin_amdgcn_permlane32_swap(f2u(v.y), f2u(v.y), false, false);
    lo = cf{u2f(a[0]), u2f(b[0])}; hi = cf{u2f(a[1]), u2f(b[1])};
}
__device__ __forceinline__ void poll_flags(rsrc_t ws, u32 voff, u32 ta, u32 tb) {
#pragma unroll 1
    for (u32 s = 0; s < SPIN_CAP; ++s) {
        FENCE();
        u32x2 v = __builtin_amdgcn_raw_buffer_load_b64(ws, voff, 0, 16);
        voff = ((v.x ^ ta) | (v.y ^ tb)) == 0 ? OOB : voff;
        if (__builtin_amdgcn_ballot_w64(voff != OOB) == 0) break;
        __builtin_amdgcn_s_sleep(PSLEEP);
    }
    FENCE();
}
__device__ __forceinline__ f32x4 sel4(bool c, f32x4 a, f32x4 b) { asm volatile("" : "+v"(a), "+v"(b)); return c ? a : b; }
#define MFMA32(a, b, c) __builtin_amdgcn_mfma_f32_32x32x16_bf16(a, b, c, 0, 0, 0)
#define MFMA16(a, b, c) __builtin_amdgcn_mfma_f32_16x16x32_bf16(a, b, c, 0, 0, 0)

#define STAMP(i) do {} while (0)
#define STAMP_OUT() do {} while (0)

__global__ __launch_bounds__(768, 3) void ssm_fused(Params p) {
    extern __shared__ __attribute__((aligned(16))) char smem[];
    const u32 lds0 = (u32)(uintptr_t)(LAS char*)smem;
    const u32 tid = __builtin_amdgcn_workitem_id_x();
    const int lane = tid & 63;
    const int wv = __builtin_amdgcn_readfirstlane(tid >> 6);
    const int xcd = __builtin_amdgcn_workgroup_id_x() & 7, idx = __builtin_amdgcn_workgroup_id_x() >> 3;
    STAMP(0);
    u32 cpf = 0;
    if (CPF && wv == 8) {
        u64 pc0; asm volatile("s_getpc_b64 %0" : "=s"(pc0));
        cpf = __builtin_amdgcn_raw_buffer_load_b32(mk_rsrc((const void*)(pc0 & ~127ull), CODE_BYTES), (u32)lane * 128, (u32)(idx % CODE_SLICES) * 8192, 0);
    }
    sig(lds0 + L_CB1 + umin(tid, 463u) * 4, tid < 448 ? SENT : 0u);
    asm volatile("s_waitcnt lgkmcnt(0)\n\ts_barrier" ::: "memory");
    asm volatile("" :: "s"(p.in[0]), "s"(p.in[1]), "s"(p.in[2]), "s"(p.in[3]), "s"(p.in[4]), "s"(p.in[5]), "s"(p.in[6]), "s"(p.in[7]), "s"(p.in[8]), "s"(p.in[9]),
                 "s"(p.in[10]), "s"(p.in[11]), "s"(p.in[12]), "s"(p.in[13]), "s"(p.in[14]), "s"(p.in[15]), "s"(p.in[16]), "s"(p.in[17]), "s"(p.out), "s"(p.ws));
    const rsrc_t ws = mk_rsrc(p.ws, WS_BYTES);
    const u32x4 wss = mk_srd(p.ws, WS_BYTES);
    const u32 syncL = lds0 + L_SYNC, syncA = syncL + 16, syncK = syncL + 32, tagw = syncL + 48;
    const int rr = lane >> 3, sp = lane & 7;
    const int m16 = lane & 15, q4 = lane >> 4;
    const int ppos = (xcd < 4 ? xcd : (xcd ^ 2)) >> 1;
    const int c = 4 * ppos + (idx & 3), pb = 8 * (xcd & 1) + (idx >> 2);
    const int tb64 = 8 * ppos + 4 * (xcd & 1) + (idx & 3), db = idx >> 2;

    if (wv < 4) {
        const int uw = wv;
        const u32 mysig = syncL + uw * 4;
        {
            __builtin_amdgcn_s_setprio(PRL);
            const rsrc_t rx = mk_rsrc(p.in[0], 2048 * 1024), rbr = mk_rsrc(p.in[3], 512 * 1024), rbi = mk_rsrc(p.in[4], 512 * 1024);
            const rsrc_t rc = mk_rsrc((uw & 2) ? p.in[6] : p.in[5], 512 * 1024);
            const u32 vg = (u32)rr * 4096 + (u32)sp * 16;
            const u32 xs0 = (u32)c * 131072 + (u32)uw * 32768, bs0 = (u32)pb * 32768 + (u32)uw * 1024, cs0 = (u32)db * 65536 + (u32)(uw & 1) * 32768;
            const int r0 = (idx >> 2) + 2 * (idx & 3);
            auto ld = [&](auto ii) INL {
                constexpr int i = decltype(ii)::v;
                if constexpr (i < 48) { constexpr int s = i / 6, j = i % 6; const u32 ch = (u32)((r0 + s) & 7) * 128;
                    if constexpr (j == 0) return __builtin_amdgcn_raw_buffer_load_b128(rbr, vg, bs0 + ch, 0);
                    else if constexpr (j == 1) return __builtin_amdgcn_raw_buffer_load_b128(rbi, vg, bs0 + ch, 0);
                    else return __builtin_amdgcn_raw_buffer_load_b128(rx, vg + (j - 2) * 1024, xs0 + ch, 0); }
                else { constexpr int cg = (0x74321065u >> (4 * ((i - 48) >> 2))) & 15, b = (i - 48) & 3;
                    return __builtin_amdgcn_raw_buffer_load_b128(rc, vg + b * 1024 + cg * 128, cs0, 0); } };
            u32x4 buf[LWIN];
            sfor<0, LWIN>([&](auto ii) INL { buf[decltype(ii)::v] = ld(ii); SBAR(); });
            const u32 w0 = lds0 + (u32)rr * 128 + (u32)((sp ^ (rr >> 1)) * 16);
            u32 wx[2], wxh[2], wb, wbh, wch[2];
#pragma unroll
            for (int k = 0; k < 2; ++k) { wx[k] = (w0 ^ (k * 64)) + uw * 4096; wxh[k] = wx[k] + 65536; wch[k] = (wx[k] ^ 4096) + 65536; }
            wb = (w0 ^ ((uw & 1) * 64)) + uw * 1024; wbh = wb + 65536;
            u32 msg = mysig, vsa = syncA;
            asm volatile("" : "+v"(wx[0]), "+v"(wx[1]), "+v"(wxh[0]), "+v"(wxh[1]), "+v"(wch[0]), "+v"(wch[1]), "+v"(wb), "+v"(wbh), "+v"(msg), "+v"(vsa));
            STAMP(1);
            sfor<0, 80>([&](auto ii) INL {
                constexpr int i = decltype(ii)::v;
                u32 ad;
                if constexpr (i < 48) { constexpr int s = i / 6, j = i % 6, o = (s % 5) * 24576;
                    if constexpr (j == 0 && s >= 5) wait4(vsa, s - 4);
                    if constexpr (j < 2) { constexpr int ob = o + 16384 + j * 4096; ad = ob < 65536 ? wb + ob : wbh + (ob - 65536); }
                    else { constexpr int ox = o + (j - 2) * 1024; ad = ox < 65536 ? wx[j & 1] + ox : wxh[j & 1] + (ox - 65536); }
                } else { constexpr int q = (i - 48) >> 2, cg = (0x74321065u >> (4 * q)) & 15, need = (0x98877654u >> (4 * q)) & 15, pneed = (0x88776540u >> (4 * q)) & 15, b = (i - 48) & 3;
                    if constexpr (b == 0 && need != pneed) wait4s(vsa, need);
                    ad = (cg < 4 ? wx[b & 1] : wch[b & 1]) + (cg & 3) * 16384 + b * 1024;
                }
                lds_st<u32x4>(ad, buf[i % LWIN]);
                SBAR();
                if constexpr (i + LWIN < 80) { buf[i % LWIN] = ld(ic<i + LWIN>{}); SBAR(); }
                if constexpr (i < 48 && i % 6 == 5) { sig(msg, i / 6 + 1); SBAR(); }
                if constexpr (i == 5) STAMP(2);
                if constexpr (i == 47) { STAMP(3); __builtin_amdgcn_s_setprio(0); }
            });
            sig(mysig, 9);
            STAMP(5);
        }
    }
    int wv2 = wv;
    asm volatile("" : "+s"(wv2));
    if (wv2 < 4) {
        const int uw = wv;
        const rsrc_t ru = mk_rsrc(p.in[9], 2048 * 1024);
        __builtin_amdgcn_s_setprio(1);
        const int dsel = uw >> 1, half = uw & 1;
        const u32 im = lds0 + L_UB + dsel * UB_SZ + UB_PAD + half * 2048;
        const int dd = 64 * xcd + half * 32;
        const u32 ta = get_tag(tagw), tb = tag2(ta);
        const int sel = 2 * idx + dsel, prr = sel >> 4;
        const u32 trow = (u32)(256 * ((m16 >> 2) & 1) + 16 * (m16 >> 3) + (m16 & 3)), tbase = (u32)(512 * prr + 64 * (sel & 3) + 4 * ((sel >> 2) & 3));
        f32x4 sk[4];
        {
            u32x2 uu[4], dk[2];
#pragma unroll
            for (int k = 0; k < 4; ++k) uu[k] = __builtin_amdgcn_raw_buffer_load_b64(ru, trow * 1024 + (u32)q4 * 8 + (k & 1) * 32, (tbase + 32 * (k >> 1)) * 1024 + (u32)dd * 2, 0);
#pragma unroll
            for (int k = 0; k < 2; ++k) dk[k] = __builtin_amdgcn_raw_buffer_load_b64(mk_rsrc(p.in[17], 1024), (u32)q4 * 8 + k * 32, (u32)dd * 2, 0);
#pragma unroll
            for (int k = 0; k < 4; ++k) {
                const u32x2 a = uu[k], b = dk[k & 1];
                const u32 w0 = pkbf(u2f(a.x << 16) * u2f(b.x << 16), u2f(a.x & 0xFFFF0000u) * u2f(b.x & 0xFFFF0000u));
                const u32 w1 = pkbf(u2f(a.y << 16) * u2f(b.y << 16), u2f(a.y & 0xFFFF0000u) * u2f(b.y & 0xFFFF0000u));
                sk[k] = f32x4{u2f(w0 << 16), u2f(w0 & 0xFFFF0000u), u2f(w1 << 16), u2f(w1 & 0xFFFF0000u)};
                asm volatile("" : "+v"(sk[k]));
            }
        }
        STAMP(6);
        if (ABL == 0) {
            wait4s(syncK, 4);
            STAMP(7);
            poll_flags(ws, lane < 32 ? WS_YFLAG + (u32)((prr >> 1) * 512 + dd + lane) * FS : OOB, ta, tb);
            __builtin_amdgcn_s_setprio(3);
            STAMP(8);
            const u32 so = WS_YT + (u32)dd * 4096 + (u32)sel * 64;
            u32x4 a = __builtin_amdgcn_raw_buffer_load_b128(ws, (u32)(lane >> 2) * 4096 + (u32)(lane & 3) * 16, so, 16);
            u32x4 b = __builtin_amdgcn_raw_buffer_load_b128(ws, (u32)(lane >> 2) * 4096 + (u32)(lane & 3) * 16, so + 65536, 16);
            lds_st<u32x4>(im + lane * 16, a);
            lds_st<u32x4>(im + 1024 + lane * 16, b);
            FENCE();
            const u32x4 ro = mk_srd(p.out, 2048 * 4096);
            const u32 ra = im + (u32)(4 * q4 + (m16 >> 2)) * 64 + (u32)(lane & 3) * 8;
#pragma unroll
            for (int k = 0; k < 4; ++k) {
                u32x2 v = lds_tr(ra + (k & 1) * 1024 + (k >> 1) * 32);
                f32x4 f = f32x4{u2f(v.x << 16), u2f(v.x & 0xFFFF0000u), u2f(v.y << 16), u2f(v.y & 0xFFFF0000u)} + sk[k];
                const u32 w0 = pkbf(f[0], f[1]), w1 = pkbf(f[2], f[3]);
                st128<0, false>(ro, u32x4{w0 << 16, w0 & 0xFFFF0000u, w1 << 16, w1 & 0xFFFF0000u}, trow * 4096 + (u32)q4 * 16, (tbase + 32 * (k >> 1)) * 4096 + (u32)(512 + dd) * 4 + (k & 1) * 64);
            }
        }
        STAMP(9);
    } else if (__builtin_expect(wv < 8, 1)) {
        const int aw = wv - 4;
        __builtin_amdgcn_s_setprio(PRA);
        const int r32 = lane & 31, hh = lane >> 5;
        const int g32 = 4 * (r32 & 7) + (r32 >> 3);
        const u32 q_lr = __builtin_amdgcn_raw_buffer_load_b16(mk_rsrc(p.in[1], 1024), (u32)g32 * 2, (u32)pb * 64, 0);
        const u32 q_li = __builtin_amdgcn_raw_buffer_load_b16(mk_rsrc(p.in[2], 1024), (u32)g32 * 2, (u32)pb * 64, 0);
        const u32 q_ld = __builtin_amdgcn_raw_buffer_load_b16(mk_rsrc(p.in[7], 1024), (u32)g32 * 2, (u32)pb * 64, 0);
        const u32 wv0 = __builtin_amdgcn_raw_buffer_load_b32(ws, 0, WS_W, 16);
        u32x4 ut[2];
#pragma unroll
        for (int k = 0; k < 2; ++k) ut[k] = __builtin_amdgcn_raw_buffer_load_b128(mk_rsrc(p.in[9], 2048 * 1024), (u32)(lane >> 2) * 1024 + (u32)(lane & 3) * 16, (u32)(idx * 64 + (aw >> 1) * 32 + 16 * k) * 1024 + (u32)xcd * 128 + (u32)(aw & 1) * 64, 0);
        SBAR();
        u32 fo[4], xo[4], fh[4], xh[4];
#pragma unroll
        for (int k = 0; k < 4; ++k) { fo[k] = lds0 + (u32)r32 * 128 + (u32)(((2 * k + hh) ^ ((r32 >> 1) & 7)) * 16); xo[k] = fo[k] + aw * 4096; fh[k] = fo[k] + 65536; xh[k] = xo[k] + 65536; }
#pragma unroll
        for (int k = 0; k < 4; ++k) asm volatile("" : "+v"(fo[k]), "+v"(xo[k]), "+v"(fh[k]), "+v"(xh[k]));
        u32 mysig = syncA + aw * 4, vsl = syncL, c1r = lds0 + L_CB1 + r32 * 8, c2r = lds0 + L_CB2 + r32 * 32, l16 = (u32)lane * 16;
        u32 c1w = c1r + aw * 256, c2w = c2r + aw * 8, evo = hh ? OOB : (u32)r32 * 16;
        u32 vo[2];
        {
            const int cb0 = c - 1 - hh - 4 * aw;
            const u32 vo0 = WS_E + (u32)(cb0 * 512 + pb * 32 + r32) * 16;
            vo[0] = cb0 >= 0 ? vo0 : OOB; vo[1] = cb0 >= 2 ? vo0 - 16384 : OOB;
        }
        asm volatile("" : "+v"(mysig), "+v"(vsl), "+v"(c1r), "+v"(c2r), "+v"(c1w), "+v"(c2w), "+v"(evo), "+v"(vo[0]), "+v"(vo[1]), "+v"(l16));
        STAMP(1);
        cf P[4], L8, L16, L32, W0, W1, coef, Q;
        {
            const float a_lr = bf2f(q_lr), a_li = bf2f(q_li);
            const float dt = rbf(__builtin_expf(bf2f(q_ld)));
            P[0] = cexpk(dt * a_lr, dt * a_li, 1.f);
            P[1] = csq(P[0]); P[2] = cmul(P[0], P[1]); P[3] = csq(P[1]);
            L8 = csq(P[3]); L16 = csq(L8); L32 = csq(L16);
            const cf L64 = csq(L32), L128 = csq(L64), L256 = csq(L128), L512 = csq(L256);
            W0 = hh ? L128 : cf{1.f, 0.f};
            if (aw & 1) W0 = cmul(W0, L512);
            if (aw & 2) W0 = cmul(W0, csq(L512));
            W1 = cmul(W0, L256);
            const float nr = P[0].x - 1.f, ni = P[0].y, inv = 2.f / (a_lr * a_lr + a_li * a_li);
            coef = cf{(nr * a_lr + ni * a_li) * inv, (ni * a_lr - nr * a_li) * inv};
            Q = hh ? cmul(coef, L16) : coef;
            if (aw & 1) Q = cmul(Q, L32);
            if (aw & 2) Q = cmul(Q, L64);
            asm volatile("" : "+v"(P[0]), "+v"(P[1]), "+v"(P[2]), "+v"(P[3]), "+v"(L8), "+v"(L16), "+v"(L32), "+v"(W0), "+v"(W1), "+v"(coef), "+v"(Q));
        }
        const u32 ta = rfl(wv0) + 1u, tb = tag2(ta);
        {
            if (aw == 0) lds_st<u32x2>(tagw, u32x2{ta, 1u});
            const u32 im = lds0 + L_UB + UB_PAD + (aw >> 1) * UB_SZ + (aw & 1) * 2048;
            lds_st<u32x4>(im + l16, ut[0]);
            lds_st<u32x4>(im + 1024 + l16, ut[1]);
            FENCE();
            const u32 tra = im + (u32)(8 * (lane & 3) + (m16 >> 2)) * 64 + (u32)q4 * 8;
#pragma unroll
            for (int k = 0; k < 2; ++k) { const u32x2 x = lds_tr(tra + k * 32), y = lds_tr(tra + k * 32 + 256); ut[k] = u32x4{x.x, x.y, y.x, y.y}; }
#pragma unroll
            for (int k = 0; k < 2; ++k) st128<0, true>(wss, ut[k], (u32)q4 * 16384 + (u32)m16 * 16, WS_UT + (u32)(16 * xcd + 8 * (aw & 1) + 4 * k) * 16384 + (u32)(2 * idx + (aw >> 1)) * 256);
        }
        u32 fvo = lane ? OOB : 0u;
        u32x2 tg = {ta, tb};
        asm volatile("" : "+v"(fvo), "+v"(tg));
        STAMP(2);
        f32x16 accR = {}, accI = {};
        {
            bf16x8 fa[4], fr[4], fi[4];
            auto rd = [&](int n) { const int s = n >> 2, k = n & 3, o = (s % 5) * 24576, ob = o + 16384;
                fa[n & 3] = lds_ld<bf16x8>(o < 65536 ? xo[k] + o : xh[k] + (o - 65536));
                fr[n & 3] = lds_ld<bf16x8>(ob < 65536 - 4096 ? fo[k] + ob : fh[k] + (ob - 65536));
                fi[n & 3] = lds_ld<bf16x8>(ob < 65536 - 4096 ? fo[k] + ob + 4096 : fh[k] + (ob + 4096 - 65536)); };
            wait4(syncL, 1);
            STAMP(10);
            u32x4 fl = *(volatile const LAS u32x4*)(uintptr_t)vsl;
            rd(0); rd(1); rd(2);
            SBAR();
#pragma unroll
            for (int n = 0; n < 32; ++n) {
                const int s = n >> 2, k = n & 3;
                if (k == 1 && s < 7) {
                    if (rfl(umin(umin(fl.x, fl.y), umin(fl.z, fl.w))) < (u32)(s + 2)) wait4(syncL, s + 2);
                    FENCE();
                }
                if (k == 2 && s < 6) fl = *(volatile const LAS u32x4*)(uintptr_t)vsl;
                accR = MFMA32(fa[k], fr[k], accR);
                SBAR();
                if (n + 3 < 32) rd(n + 3);
                if (k == 0) sig(mysig, s + 1);
                if (n == UFN) { WAITV(0); __builtin_amdgcn_raw_buffer_store_b64(tg, ws, fvo, WS_UFLAG + (u32)((2 * xcd + (aw & 1)) * 64 + 2 * idx + (aw >> 1)) * FS, 16); FENCE(); }
                accI = MFMA32(fa[k], fi[k], accI);
                SBAR();
            }
        }
        __builtin_amdgcn_s_setprio(3);
        STAMP(3);
        f32x2 hr[8], hi[8];
#pragma unroll
        for (int i = 0; i < 8; ++i) { hr[i] = f32x2{accR[2 * i], accR[2 * i + 1]}; hi[i] = f32x2{accI[2 * i], accI[2 * i + 1]}; }
#pragma unroll
        for (int i = 2; i < 8; ++i) {
            f32x2 nr = fma2(-P[0].y, hi[i - 2], fma2(P[0].x, hr[i - 2], hr[i]));
            f32x2 ni = fma2(P[0].y, hr[i - 2], fma2(P[0].x, hi[i - 2], hi[i]));
            hr[i] = nr; hi[i] = ni;
        }
        cf e0, e1, pre = {0.f, 0.f};
        {
            const f32x2 zr = fma2(-L8.y, hi[6], fma2(L8.x, hr[6], hr[7])), zi = fma2(L8.y, hr[6], fma2(L8.x, hi[6], hi[7]));
            const cf e16 = {__builtin_fmaf(-P[3].y, zi.x, __builtin_fmaf(P[3].x, zr.x, zr.y)), __builtin_fmaf(P[3].y, zr.x, __builtin_fmaf(P[3].x, zi.x, zi.y))};
            swap_halves(e16, e0, e1);
        }
        const cf e32 = cfma(e0, L16, e1);
        if (aw < 3) { lds_st<f32x2>(c1w, e32); FENCE(); }
        u32x4 g[2];
        if (aw < 3) {
#pragma unroll
            for (int k = 0; k < 2; ++k) g[k] = __builtin_amdgcn_raw_buffer_load_b128(ws, vo[k], 0, 16);
        }
        if (aw > 0) {
            cf w0, w1, w2;
#pragma unroll 1
            for (u32 spn = 0; spn < (1u << 22); ++spn) {
                asm volatile("ds_read_b64 %0, %3\n\tds_read_b64 %1, %3 offset:256\n\tds_read_b64 %2, %3 offset:512\n\ts_waitcnt lgkmcnt(0)" : "=&v"(w0), "=&v"(w1), "=&v"(w2) : "v"(c1r) : "memory");
                const u32 a = f2u(w0.x), b = f2u(w1.x), cc = f2u(w2.x);
                const u32 mx = a > b ? (a > cc ? a : cc) : (b > cc ? b : cc);
                if (__builtin_amdgcn_ballot_w64(mx == SENT) == 0) break;
            }
            const cf p2 = cfma(w0, L32, w1), p3 = cfma(p2, L32, w2);
            pre = aw == 1 ? w0 : (aw == 2 ? p2 : p3);
            if (aw == 3) {
                if (c < 15) { const cf E = cfma(p3, L32, e32); st128<0, true>(wss, u32x4{f2u(E.x), ta, f2u(E.y), tb}, evo, WS_E + (u32)(c * 512 + pb * 32) * 16); }
#pragma unroll
                for (int k = 0; k < 2; ++k) g[k] = __builtin_amdgcn_raw_buffer_load_b128(ws, vo[k], 0, 16);
            }
        }
        STAMP(4);
        const int th = aw & 1, kh = aw >> 1;
        const int t0 = tb64 * 64 + th * 32, dcol = db * 64 + kh * 32, t32 = tb64 * 2 + th;
        u32 xk[16];
        {
            const rsrc_t rx = mk_rsrc(p.in[0], 2048 * 1024);
#pragma unroll
            for (int r = 0; r < 16; ++r)
                xk[r] = __builtin_amdgcn_raw_buffer_load_b16(rx, (u32)hh * 4096 + (u32)g32 * 2 + (r & 3) * 1024, (u32)t0 * 1024 + (u32)dcol * 2 + (r >> 2) * 8192, 0);
        }
        const u32 dsk = __builtin_amdgcn_raw_buffer_load_b16(mk_rsrc(p.in[8], 1024), (u32)g32 * 2, (u32)dcol * 2, 0);
        SBAR();
        f32x2 cpr[2], cpi[2], cqr[2], cqi[2];
        {
            { const cf pre1 = cfma(pre, L16, e0); pre = hh ? pre1 : pre; }
            cf s[4], q[4];
            s[0] = pre; q[0] = Q;
            s[1] = cfma(s[0], P[3], cf{hr[6].x, hi[6].x}); s[2] = cfma(s[1], P[3], cf{hr[6].y, hi[6].y}); s[3] = cfma(s[2], P[3], cf{hr[7].x, hi[7].x});
#pragma unroll
            for (int a = 1; a < 4; ++a) q[a] = cmul(q[a - 1], P[3]);
#pragma unroll
            for (int a = 0; a < 4; ++a) s[a] = cmul(s[a], coef);
#pragma unroll
            for (int u = 0; u < 2; ++u) { cpr[u] = f32x2{s[2 * u].x, s[2 * u + 1].x}; cpi[u] = f32x2{s[2 * u].y, s[2 * u + 1].y}; cqr[u] = f32x2{q[2 * u].x, q[2 * u + 1].x}; cqi[u] = f32x2{q[2 * u].y, q[2 * u + 1].y};
                asm volatile("" : "+v"(cpr[u]), "+v"(cpi[u]), "+v"(cqr[u]), "+v"(cqi[u])); }
        }
#pragma unroll
        for (int i = 0; i < 8; ++i) {
            f32x2 nr = fma2(-coef.y, hi[i], coef.x * hr[i]);
            f32x2 ni = fma2(-coef.y, hr[i], -coef.x * hi[i]);
            hr[i] = nr; hi[i] = ni;
            asm volatile("" : "+v"(hr[i]), "+v"(hi[i]));
        }
        const u32 scr = lds0 + 7 * 16384 + aw * 4096;
        u32 wa0 = scr + (u32)(r32 ^ ((r32 >> 3) & 3)) * 64 + (u32)(((2 * hh) ^ ((r32 >> 1) & 3)) * 16), wa1 = wa0 ^ 16;
        u32 ra[4], pvo = lane < 8 ? WS_HFLAG + (u32)(t32 * 16 + kh * 8 + lane) * FS : OOB;
        { const int q = m16 >> 2, pp = lane & 3;
          const u32 b0 = scr + (u32)(8 * q + ((2 * hh) ^ q)) * 64 + (u32)(((2 * (q4 & 1) + (pp >> 1)) ^ hh) * 16) + (u32)(pp & 1) * 8;
          ra[0] = b0; ra[1] = b0 ^ 64; ra[2] = (b0 ^ 32) + 256; ra[3] = (b0 ^ 96) + 256; }
        asm volatile("" : "+v"(ra[0]), "+v"(ra[1]), "+v"(ra[2]), "+v"(ra[3]), "+v"(wa0), "+v"(wa1), "+v"(pvo));
        SBAR();
        cf ev[2] = {cf{0.f, 0.f}, cf{0.f, 0.f}};
#pragma unroll 1
        for (u32 spn = 0; spn < SPIN_CAP; ++spn) {
#pragma unroll
            for (int k = 0; k < 2; ++k) {
                const bool good = ((g[k].y ^ ta) | (g[k].w ^ tb)) == 0;
                ev[k].x = good ? u2f(g[k].x) : ev[k].x; ev[k].y = good ? u2f(g[k].z) : ev[k].y;
                vo[k] = good ? OOB : vo[k];
            }
            if (__builtin_amdgcn_ballot_w64(umin(vo[0], vo[1]) != OOB) == 0) break;
            FENCE();
#pragma unroll
            for (int k = 0; k < 2; ++k) g[k] = __builtin_amdgcn_raw_buffer_load_b128(ws, vo[k], 0, 16);
        }
        {
            cf G0, G1;
            swap_halves(cfma(ev[1], W1, cmul(ev[0], W0)), G0, G1);
            lds_st<f32x2>(c2w, G0 + G1);
            f32x4 h01, h23;
#pragma unroll 1
            for (u32 spn = 0; spn < (1u << 22); ++spn) {
                asm volatile("ds_read_b128 %0, %2\n\tds_read_b128 %1, %2 offset:16\n\ts_waitcnt lgkmcnt(0)" : "=&v"(h01), "=&v"(h23) : "v"(c2r) : "memory");
                const u32 a = f2u(h01[0]), b = f2u(h01[2]), cc = f2u(h23[0]), dd = f2u(h23[2]);
                const u32 m1 = a > b ? a : b, m2 = cc > dd ? cc : dd;
                if (__builtin_amdgcn_ballot_w64((m1 > m2 ? m1 : m2) == SENT) == 0) break;
            }
            STAMP(5);
            const f32x4 hs4 = h01 + h23;
            const cf H = {hs4[0] + hs4[2], hs4[1] + hs4[3]};
            f32x2 gr[2], gi[2];
#pragma unroll
            for (int u = 0; u < 2; ++u) { gr[u] = fma2(-H.y, cqi[u], fma2(H.x, cqr[u], cpr[u])); gi[u] = fma2(H.x, cqi[u], fma2(H.y, cqr[u], cpi[u])); }
#pragma unroll
            for (int i = 0; i < 8; ++i) hr[i] = fma2(-P[i >> 1].y, gi[i & 1], fma2(P[i >> 1].x, gr[i & 1], hr[i]));
            lds_st<u32x4>(wa0, u32x4{pkbf(hr[0].x, hr[2].x), pkbf(hr[4].x, hr[6].x), pkbf(hr[0].y, hr[2].y), pkbf(hr[4].y, hr[6].y)});
            lds_st<u32x4>(wa1, u32x4{pkbf(hr[1].x, hr[3].x), pkbf(hr[5].x, hr[7].x), pkbf(hr[1].y, hr[3].y), pkbf(hr[5].y, hr[7].y)});
            FENCE();
            u32x2 tv[8];
#pragma unroll
            for (int s = 0; s < 4; ++s) tv[s] = lds_tr(ra[s]);
            SBAR();
#pragma unroll
            for (int i = 0; i < 8; ++i) hi[i] = fma2(-P[i >> 1].y, gr[i & 1], fma2(-P[i >> 1].x, gi[i & 1], hi[i]));
            lds_st<u32x4>(wa0 + 2048, u32x4{pkbf(hi[0].x, hi[2].x), pkbf(hi[4].x, hi[6].x), pkbf(hi[0].y, hi[2].y), pkbf(hi[4].y, hi[6].y)});
            lds_st<u32x4>(wa1 + 2048, u32x4{pkbf(hi[1].x, hi[3].x), pkbf(hi[5].x, hi[7].x), pkbf(hi[1].y, hi[3].y), pkbf(hi[5].y, hi[7].y)});
            FENCE();
#pragma unroll
            for (int s = 0; s < 4; ++s) tv[4 + s] = lds_tr(ra[s] + 2048);
            const u32 hs = WS_H + (u32)((c * 4 + aw) * 16 + pb) * 4096;
            st128<0, true>(wss, u32x4{tv[0].x, tv[0].y, tv[1].x, tv[1].y}, l16, hs);
            st128<1024, true>(wss, u32x4{tv[2].x, tv[2].y, tv[3].x, tv[3].y}, l16, hs);
            st128<2048, true>(wss, u32x4{tv[4].x, tv[4].y, tv[5].x, tv[5].y}, l16, hs);
            st128<3072, true>(wss, u32x4{tv[6].x, tv[6].y, tv[7].x, tv[7].y}, l16, hs);
            sig(mysig, 9);
        }
        f32x16 y0, y1 = {};
        {
            const float dk = bf2f(dsk);
#pragma unroll
            for (int r = 0; r < 16; r += 2) { const u32 w = pkbf(bf2f(xk[r]) * dk, bf2f(xk[r + 1]) * dk); y0[r] = u2f(w << 16); y0[r + 1] = u2f(w & 0xFFFF0000u); }
            asm volatile("" : "+v"(y0));
        }
        WAITV(0);
        __builtin_amdgcn_raw_buffer_store_b64(tg, ws, fvo, WS_HFLAG + (u32)((c * 4 + aw) * 16 + pb) * FS, 16);
        FENCE();
        STAMP(6);
        u32 ca[4], ov[4];
#pragma unroll
        for (int k = 0; k < 4; ++k) { ca[k] = fo[k] + (u32)kh * 65536; ov[k] = (u32)hh * 16384 + (u32)g32 * 4 + k * 4096; asm volatile("" : "+v"(ca[k]), "+v"(ov[k])); }
        u32 xb = lds0 + (u32)(4 * kh + (db & 3)) * 16384 + (u32)th * 4096 + l16;
        asm volatile("" : "+v"(xb));
        wait4(syncL, 9);
        bf16x8 cb0[4], cb1[4];
        auto cad = [&](int f) { const int jj = f >> 3, a = (f >> 2) & 1, s = f & 3;
            return ca[2 * a + (s & 1)] + (u32)((jj + db) & 3) * 16384 + (s >> 1) * 8192; };
#pragma unroll
        for (int f = 0; f < 3; ++f) { const u32 ad = cad(f); cb0[f] = lds_ld<bf16x8>(ad); cb1[f] = lds_ld<bf16x8>(ad + 4096); }
        poll_flags(ws, pvo, ta, tb);
        STAMP(7);
        {
            u32x4 hf[16];
            auto ldh = [&](int f) { const u32 cg = (u32)(4 * kh + (((f >> 3) + db) & 3));
                return __builtin_amdgcn_raw_buffer_load_b128(ws, l16 + (f & 3) * 1024, WS_H + (u32)t32 * 65536 + cg * 8192 + ((f >> 2) & 1) * 4096, 16); };
#pragma unroll
            for (int f = 0; f < HWIN; ++f) { hf[f] = ldh(f); SBAR(); }
#pragma unroll
            for (int f = 0; f < 32; ++f) {
                bf16x8 av = __builtin_bit_cast(bf16x8, hf[f & 15]);
                y0 = MFMA32(av, cb0[f & 3], y0);
                SBAR();
                if (f + 3 < 32) { const u32 ad = cad(f + 3); cb0[(f + 3) & 3] = lds_ld<bf16x8>(ad); cb1[(f + 3) & 3] = lds_ld<bf16x8>(ad + 4096); }
                y1 = MFMA32(av, cb1[f & 3], y1);
                SBAR();
                if (f + HWIN < 32) { hf[(f + HWIN) & 15] = ldh(f + HWIN); SBAR(); }
                if (f == 8) { sig(mysig, 10); SBAR(); }
                if (f == 28) { wait4(syncA, 10); SBAR(); }
            }
        }
        STAMP(8);
        {
#pragma unroll
            for (int k = 0; k < 4; ++k) lds_st<f32x4>(xb + k * 1024, f32x4{y1[4 * k], y1[4 * k + 1], y1[4 * k + 2], y1[4 * k + 3]});
            sig(mysig, 11);
            wait1(syncA + (aw ^ 2) * 4, 11);
            const rsrc_t ro = mk_rsrc(p.out, 2048 * 4096);
            f32x4 o[4];
#pragma unroll
            for (int k = 0; k < 4; ++k) o[k] = lds_ld<f32x4>((xb ^ 65536) + k * 1024);
#pragma unroll
            for (int k = 0; k < 4; ++k) {
#pragma unroll
                for (int j = 0; j < 4; ++j)
                    __builtin_amdgcn_raw_buffer_store_b32(f2u(y0[4 * k + j] + o[k][j]), ro, ov[j], (u32)(t0 + 8 * k) * 4096 + (u32)dcol * 4, OAUX);
            }
            if (tb64 == 31 && db == 0 && aw == 0) __builtin_amdgcn_raw_buffer_store_b32(ta, ws, fvo, WS_W, 16);
        }
        STAMP(9);
    } else {
        const int bw = wv - 8, dsel = bw >> 1, half = bw & 1;
        if (ABL >= 1) return;
        __builtin_amdgcn_s_setprio(PRK);
        const int d = 64 * xcd + 2 * idx + dsel;
        const u32 s0 = lds0 + L_S0 + bw * S0_SZ, kc = lds0 + L_KC + dsel * KC_SZ, ub = lds0 + L_UB + dsel * UB_SZ;
        const u32 mysig = syncK + bw * 4, psig = syncK + (bw ^ 1) * 4;
        u32 pr[6];
#pragma unroll
        for (int i = 0; i < 6; ++i) pr[i] = __builtin_amdgcn_raw_buffer_load_b16(mk_rsrc(p.in[10 + i], 512 * 256), (u32)lane * 2, (u32)(d * 128 + half * 64) * 2, 0);
        const u32 q_ld = __builtin_amdgcn_raw_buffer_load_b16(mk_rsrc(p.in[16], 1024), 0, (u32)d * 2, 0);
        SBAR();
        u32 tch = 0, tch2 = 0;
        if (TOUCH) {
            const rsrc_t ro = mk_rsrc(p.out, 2048 * 4096);
            if (bw == 0) {
                tch = __builtin_amdgcn_raw_buffer_load_b32(ro, (u32)lane * 4096, (u32)tb64 * 262144, 0);
                tch2 = __builtin_amdgcn_raw_buffer_load_b32(ws, (u32)lane * 4096, WS_YT + (u32)xcd * 262144, 16);
            } else if (bw == 1) {
                const int sel = 2 * idx + (lane >> 5), i16 = lane & 15;
                const u32 row = (u32)(512 * (sel >> 4) + 64 * (sel & 3) + 4 * ((sel >> 2) & 3) + 256 * ((i16 >> 2) & 1) + 16 * (i16 >> 3) + (i16 & 3) + 32 * ((lane >> 4) & 1));
                tch = __builtin_amdgcn_raw_buffer_load_b32(ro, row * 4096, 0, 0);
                tch2 = __builtin_amdgcn_raw_buffer_load_b32(ws, (u32)lane * 4096, WS_UT + (u32)xcd * 262144, 16);
            } else if (bw == 2) {
                const u32 a = lane < 32 ? WS_H + (u32)tb64 * 131072 + (u32)lane * 4096 : lane < 36 ? WS_H + (u32)((c * 4 + lane - 32) * 16 + pb) * 4096 : lane < 52 ? WS_E + (u32)((lane - 36) * 512 + pb * 32) * 16
                            : lane < 54 ? WS_HFLAG + (u32)(lane == 52 ? tb64 * 2 : c * 4) * 2048 : lane < 56 ? WS_HFLAG + (u32)(c * 4 + 2) * 2048 : lane < 60 ? WS_UFLAG + (u32)xcd * 16384 + (u32)(lane - 56) * 4096 : WS_YFLAG + (u32)((lane >> 1) & 1) * 65536 + (u32)xcd * 8192 + (u32)(lane & 1) * 4096;
                tch = __builtin_amdgcn_raw_buffer_load_b32(ws, a, 0, 16);
                tch2 = __builtin_amdgcn_raw_buffer_load_b32(mk_rsrc(p.in[8], 1024), 0, 0, 0) + __builtin_amdgcn_raw_buffer_load_b32(mk_rsrc(p.in[17], 1024), 0, 0, 0);
            } else {
                tch = __builtin_amdgcn_raw_buffer_load_b32(mk_rsrc(p.in[5], 512 * 1024), (u32)(lane & 15) * 4096, (u32)db * 65536, 0);
                tch2 = __builtin_amdgcn_raw_buffer_load_b32(mk_rsrc(p.in[6], 512 * 1024), (u32)(lane & 15) * 4096, (u32)db * 65536, 0);
            }
        }
        SBAR();
        lds_st<u32x2>(ub + umin(lane, 59) * 8, u32x2{0u, 0u});
        u32 ga[19];
        {
            const int lag0 = 256 * rr + 128 * half + 127 - 18 * sp;
#pragma unroll
            for (int e = 0; e < 19; ++e) {
                const int lag = lag0 - e;
                const u32 bits = pkbf((float)lag, 0.f) & 0xFFFFu;
                const u32 ix = lag < 128 ? (u32)lag : bits - (0x4300u - 128u);
                ga[e] = lag < 0 ? ub : kc + 2 * ix;
                asm volatile("" : "+v"(ga[e]));
            }
        }
        asm volatile("" :: "v"(cpf));
        STAMP(1);
        f32x4 ka[3];
        {
            const float dt = rbf(__builtin_expf(bf2f(q_ld)));
            const float zr = dt * bf2f(pr[0]), zi = dt * bf2f(pr[1]);
            const cf db_ = {dt * bf2f(pr[2]), -dt * bf2f(pr[3])}, cc = {bf2f(pr[4]), -bf2f(pr[5])};
            const cf E1 = cexpk(zr, zi, 1.f), E8c = cexpk(zr, -zi, 8.f), E128 = cexpk(zr, zi, 128.f);
            const u32 wa = s0 + lane * 4, ra = s0 + (u32)m16 * 288 + (u32)q4 * 16;
            cf V[16], R[8], W[17];
            { const cf E16 = cmul(E8c, E8c), E32 = cmul(E16, E16), E64 = cmul(E32, E32);
              V[0] = cmul(cc, db_); V[1] = cmul(V[0], E8c);
#pragma unroll
              for (int j = 0; j < 2; ++j) V[2 + j] = cmul(V[j], E16);
#pragma unroll
              for (int j = 0; j < 4; ++j) V[4 + j] = cmul(V[j], E32);
#pragma unroll
              for (int j = 0; j < 8; ++j) V[8 + j] = cmul(V[j], E64); }
            R[0] = cf{1.f, 0.f}; R[1] = E1; R[2] = cmul(E1, E1); R[3] = cmul(R[1], R[2]); R[4] = cmul(R[2], R[2]);
#pragma unroll
            for (int r = 1; r < 4; ++r) R[4 + r] = cmul(R[r], R[4]);
            W[1] = E128; W[2] = cmul(W[1], W[1]); W[3] = cmul(W[1], W[2]); W[4] = cmul(W[2], W[2]);
#pragma unroll
            for (int k = 1; k <= 4; ++k) W[4 + k] = cmul(W[k], W[4]);
#pragma unroll
            for (int k = 1; k <= 8; ++k) W[8 + k] = cmul(W[k], W[8]);
#pragma unroll
            for (int j = 0; j < 16; ++j) lds_st<u32>(wa + j * 288, pkc(V[j]));
            FENCE();
            bf16x8 vf[4], uf[4];
#pragma unroll
            for (int s = 0; s < 4; ++s) vf[s] = lds_ld<bf16x8>(ra + s * 64);
            FENCE();
#pragma unroll
            for (int r = 0; r < 8; ++r) { lds_st<u32>(wa + r * 288, pkc(R[r])); lds_st<u32>(wa + (8 + r) * 288, pkc(r ? cmul(R[r], W[1]) : W[1])); }
            FENCE();
#pragma unroll
            for (int s = 0; s < 4; ++s) uf[s] = lds_ld<bf16x8>(ra + s * 64);
            FENCE();
#pragma unroll
            for (int k = 0; k < 2; ++k)
#pragma unroll
                for (int r = 0; r < 4; ++r) lds_st<u32>(wa + (4 * k + r) * 288, pkc(r ? cmul(R[2 * r], W[2 + k]) : W[2 + k]));
#pragma unroll
            for (int k = 0; k < 4; ++k) { lds_st<u32>(wa + (8 + 2 * k) * 288, pkc(W[4 + k])); lds_st<u32>(wa + (9 + 2 * k) * 288, pkc(cmul(W[4 + k], R[4]))); }
            FENCE();
            ka[0] = f32x4{};
#pragma unroll
            for (int s = 0; s < 4; ++s) ka[0] = MFMA16(uf[s], vf[s], ka[0]);
#pragma unroll
            for (int s = 0; s < 4; ++s) uf[s] = lds_ld<bf16x8>(ra + s * 64);
            FENCE();
#pragma unroll
            for (int k = 0; k < 9; ++k) lds_st<u32>(wa + k * 288, pkc(W[8 + k]));
            FENCE();
            ka[1] = f32x4{};
#pragma unroll
            for (int s = 0; s < 4; ++s) ka[1] = MFMA16(uf[s], vf[s], ka[1]);
#pragma unroll
            for (int s = 0; s < 4; ++s) uf[s] = lds_ld<bf16x8>(ra + s * 64);
            FENCE();
            ka[2] = f32x4{};
#pragma unroll
            for (int s = 0; s < 4; ++s) ka[2] = MFMA16(uf[s], vf[s], ka[2]);
        }
        asm volatile("" :: "v"(tch), "v"(tch2));
        STAMP(3);
        const u32 ta = get_tag(tagw), tb = tag2(ta);
        u32 uvo = WS_UFLAG + (u32)((d >> 5) * 64 + lane) * FS;
        u32x2 uf = __builtin_amdgcn_raw_buffer_load_b64(ws, uvo, 0, 16);
        SBAR();
#pragma unroll
        for (int t = 0; t < 3; ++t) lds_st<f32x4>(s0 + t * 1024 + lane * 16, ka[t]);
        sig(mysig, 1);
        wait1(psig, 1);
        u32x4 ur[2];
        {
            const u32 ps0 = lds0 + L_S0 + (bw ^ 1) * S0_SZ;
#pragma unroll
            for (int t = 0; t < 3; ++t) ka[t] += lds_ld<f32x4>(ps0 + t * 1024 + lane * 16);
            sig(mysig, 2);
            lds_st<u32x2>(kc + 2 * (128 * (q4 >> 1) + 4 * (q4 & 1) + 8 * m16), u32x2{pkbf(ka[0][0], ka[0][1]), pkbf(ka[0][2], ka[0][3])});
            const u32 a0 = q4 < 2 ? 256 + 64 * q4 + 4 * m16 : 384 + 64 * (q4 - 2) + 2 * m16, a1 = a0 + (q4 < 2 ? 2 : 32);
            lds_st<u32>(kc + 2 * a0, pkbf(ka[1][0], ka[1][1]));
            lds_st<u32>(kc + 2 * a1, pkbf(ka[1][2], ka[1][3]));
            const u32 w01 = pkbf(ka[2][0], ka[2][1]), w23 = pkbf(ka[2][2], ka[2][3]);
            const u32 a2 = kc + 2 * (512 + (q4 == 3 ? 144 : 64 * q4) + m16);
            lds_st<u16>(a2, (u16)w01); lds_st<u16>(a2 + 32, (u16)(w01 >> 16)); lds_st<u16>(a2 + 64, (u16)w23); lds_st<u16>(a2 + 96, (u16)(w23 >> 16));
            FENCE();
            uvo = ((uf.x ^ ta) | (uf.y ^ tb)) == 0 ? OOB : uvo;
            if (__builtin_amdgcn_ballot_w64(uvo != OOB) != 0) poll_flags(ws, uvo, ta, tb);
            FENCE();
#pragma unroll
            for (int k = 0; k < 2; ++k) ur[k] = __builtin_amdgcn_raw_buffer_load_b128(ws, (u32)(lane >> 1) * 64 + (u32)(lane & 1) * 16 + k * 2048, WS_UT + (u32)(16 * xcd + (idx >> 1)) * 16384 + (u32)(idx & 1) * 32 + (u32)bw * 4096, 16);
            SBAR();
            u32 gv[19];
#pragma unroll
            for (int e = 0; e < 19; ++e) gv[e] = lds_ld<u16>(ga[e]);
            wait1(psig, 2);
#pragma unroll
            for (int k = 0; k < 9; ++k) {
                lds_st<u32>(s0 + 36 * lane + 4 * k, gv[2 * k] | (gv[2 * k + 1] << 16));
                lds_st<u32>(s0 + 2368 + 36 * lane + 4 * k, gv[2 * k + 1] | (gv[2 * k + 2] << 16));
            }
            FENCE();
        }
        STAMP(4);
        wait4(syncA, 1);
#pragma unroll
        for (int k = 0; k < 2; ++k) lds_st<u32x4>(lds0 + L_UB + UB_PAD + (u32)(lane & 1) * UB_SZ + (u32)(lane >> 1) * 16 + (u32)bw * 1024 + k * 512, ur[k]);
        sig(mysig, 3);
        wait4(syncK, 3);
        __builtin_amdgcn_s_setprio(0);
        STAMP(5);
        f32x4 acc[8];
#pragma unroll
        for (int i = 0; i < 8; ++i) acc[i] = f32x4{};
        {
            const u32 bb = ub + 256 - 256 * half + 32 * m16 + 16 * q4;
            const u32 sgl = (u32)(1 - (m16 & 1));
            const u32 ab = s0 + 4 * (592 * sgl + ((111 - m16 - sgl) >> 1) + 4 * q4 - 48);
            bf16x8 bfr[2][8], afr[4];
            auto lda = [&](int n) { const int r = n >> 3, i = n & 7; const u32 o = ab + 4 * (72 * i + 16 * (3 - r));
                u32x4 av = {lds_ld<u32>(o), lds_ld<u32>(o + 4), lds_ld<u32>(o + 8), lds_ld<u32>(o + 12)}; return __builtin_bit_cast(bf16x8, av); };
            afr[0] = lda(0);
#pragma unroll
            for (int m = 0; m < 8; ++m) bfr[0][m] = lds_ld<bf16x8>(bb + 512 * m + 192);
            afr[1] = lda(1); afr[2] = lda(2);
            SBAR();
#pragma unroll
            for (int r = 0; r < 4; ++r) {
                const int cu = r & 1, nx = cu ^ 1;
#pragma unroll
                for (int i = 0; i < 8; ++i) {
                    const int n = 8 * r + i;
                    if (n + 3 < 32) afr[(n + 3) & 3] = lda(n + 3);
                    if (r < 3 && i < 4) { bfr[nx][2 * i] = lds_ld<bf16x8>(bb + 1024 * i + 64 * (2 - r)); bfr[nx][2 * i + 1] = lds_ld<bf16x8>(bb + 1024 * i + 512 + 64 * (2 - r)); }
#pragma unroll
                    for (int cb = i; cb < 8; ++cb) acc[cb] = MFMA16(afr[n & 3], bfr[cu][cb - i], acc[cb]);
                    SBAR();
                }
            }
        }
        FENCE();
        __builtin_amdgcn_s_setprio(3);
        STAMP(6);
        {
            const u32 ps0 = lds0 + L_S0 + (bw ^ 1) * S0_SZ;
            f32x4 kp[4];
            if (half) {
#pragma unroll
                for (int k = 0; k < 4; ++k) { lds_st<f32x4>(s0 + k * 1024 + lane * 16, acc[k]); kp[k] = acc[4 + k]; }
            } else {
#pragma unroll
                for (int k = 0; k < 4; ++k) { lds_st<f32x4>(s0 + k * 1024 + lane * 16, acc[4 + k]); kp[k] = acc[k]; }
            }
            sig(mysig, 4);
            wait1(psig, 4);
#pragma unroll
            for (int k = 0; k < 4; ++k) kp[k] += lds_ld<f32x4>(ps0 + k * 1024 + lane * 16);
            st128<0, true>(wss, u32x4{pkbf(kp[0][0], kp[0][1]), pkbf(kp[0][2], kp[0][3]), pkbf(kp[1][0], kp[1][1]), pkbf(kp[1][2], kp[1][3])}, (u32)lane * 16, WS_YT + (u32)d * 4096 + (u32)half * 2048);
            st128<1024, true>(wss, u32x4{pkbf(kp[2][0], kp[2][1]), pkbf(kp[2][2], kp[2][3]), pkbf(kp[3][0], kp[3][1]), pkbf(kp[3][2], kp[3][3])}, (u32)lane * 16, WS_YT + (u32)d * 4096 + (u32)half * 2048);
            WAITV(0);
            __builtin_amdgcn_raw_buffer_store_b64(u32x2{ta, tb}, ws, lane ? OOB : 0u, WS_YFLAG + (u32)(half * 512 + d) * FS, 16);
        }
        STAMP(7);
    }
    STAMP_OUT();
}

#if defined(__HIP_DEVICE_COMPILE__)
#pragma clang attribute pop
#endif

extern "C" void kernel_launch(void* const* d_in, const int* in_sizes, int n_in, void* d_out, int out_size, void* d_ws, size_t ws_size, hipStream_t stream) {
    Params p;
    for (int i = 0; i < 18; ++i) p.in[i] = d_in[i];
    p.out = (float*)d_out;
    p.ws = (char*)d_ws;
    (void)hipFuncSetAttribute((const void*)ssm_fused, hipFuncAttributeMaxDynamicSharedMemorySize, LDS_BYTES);
    ssm_fused<<<256, 768, LDS_BYTES, stream>>>(p);
}
```

```cpp
#define ENDT 1800
#include <hip/hip_runtime.h>
#include <stdint.h>

typedef unsigned short u16;
typedef unsigned u32;
typedef unsigned long long u64;
typedef short bf16x8 __attribute__((ext_vector_type(8)));
typedef short s16x4 __attribute__((ext_vector_type(4)));
typedef float f32x2 __attribute__((ext_vector_type(2)));
typedef float f32x4 __attribute__((ext_vector_type(4)));
typedef float f32x16 __attribute__((ext_vector_type(16)));
typedef unsigned u32x2 __attribute__((ext_vector_type(2)));
typedef unsigned u32x4 __attribute__((ext_vector_type(4)));
typedef __bf16 bf16x2_t __attribute__((ext_vector_type(2)));
#define LAS __attribute__((address_space(3)))
typedef __amdgpu_buffer_rsrc_t rsrc_t;

#define ABL 0
#define PRA 2
#define PRK 1
#define UM 7
#define DBG1 0
#define LSTAG 5
#define CTHR 0
#define PRL 0
#define WSL 0
#define LWIN 12
#define UFN 6
#define TOUCH 0
#define CPF 1
#define OAUX 0
#define STAMPS 0

constexpr u32 WS_W = 0;
constexpr u32 FS = 128;
constexpr u32 WS_UFLAG = 524288;
constexpr u32 WS_HFLAG = 131072;
constexpr u32 WS_YFLAG = 262144;
constexpr u32 WS_E = 393216;
constexpr u32 WS_UT = 1u << 20;
constexpr u32 WS_YT = 3u << 20;
constexpr u32 WS_H = 5u << 20;
constexpr u32 WS_BYTES = 9u << 20;
constexpr u32 WS_STAMP = 9u << 20;
constexpr u32 OOB = 0x7FFFFF00u;
constexpr u32 L_R2 = 131072;
constexpr u32 L_S0 = L_R2, S0_SZ = 4672;
constexpr u32 L_KC = L_R2 + 4 * S0_SZ, KC_SZ = 1440;
constexpr u32 L_UB = L_KC + 2 * KC_SZ, UB_PAD = 480, UB_SZ = UB_PAD + 4096;
constexpr u32 L_CB1 = L_UB + 2 * UB_SZ;
constexpr u32 L_CB2 = L_CB1 + 768;
constexpr u32 L_SYNC = L_CB2 + 1024;
constexpr u32 LDS_BYTES = 163840;
static_assert(L_SYNC + 64 <= LDS_BYTES, "lds");
constexpr int NWAVES = 12;
#define HWIN 10
#define PSLEEP 1
constexpr u32 SPIN_CAP = 1u << 15;
constexpr u32 CODE_BYTES = 24576;
constexpr u32 CODE_SLICES = 3;
constexpr u32 SENT = 0xFFFFFFFFu;

struct Params { const void* in[18]; float* out; char* ws; };

#if defined(__HIP_DEVICE_COMPILE__)
#pragma clang attribute push(__attribute__((target("no-packed-fp32-ops"))), apply_to = function)
#endif
template <int I> struct ic { static constexpr int v = I; };
template <int B, int E, typename F> __device__ __forceinline__ void sfor(F&& f) { if constexpr (B < E) { f(ic<B>{}); sfor<B + 1, E>(f); } }
#define INL __attribute__((always_inline))
__device__ __forceinline__ u32 umin(u32 a, u32 b) { return a < b ? a : b; }
__device__ __forceinline__ float u2f(u32 v) { return __builtin_bit_cast(float, v); }
__device__ __forceinline__ u32 f2u(float v) { return __builtin_bit_cast(u32, v); }

__device__ __forceinline__ float bf2f(u32 v) { return u2f(v << 16); }
__device__ __forceinline__ u32 pkbf(float lo, float hi) { f32x2 a = {lo, hi}; return __builtin_bit_cast(u32, __builtin_convertvector(a, bf16x2_t)); }
__device__ __forceinline__ float rbf(float x) { return u2f(pkbf(x, x) << 16); }
__device__ __forceinline__ u32x4 mk_srd(const void* p, u32 bytes) { u64 a = (u64)p; u32x4 r = {(u32)a, (u32)(a >> 32) & 0xFFFFu, bytes, 0x00020000u}; return r; }
__device__ __forceinline__ rsrc_t mk_rsrc(const void* p, u32 bytes) { return __builtin_amdgcn_make_buffer_rsrc((void*)p, 0, bytes, 0x00020000); }
template <int IMM, bool SC1> __device__ __forceinline__ void st128(u32x4 srd, u32x4 v, u32 voff, u32 soff) {
    if (SC1) asm volatile("buffer_store_dwordx4 %0, %1, %2, %3 offen offset:%4 sc1\n\ts_nop 1" :: "v"(v), "v"(voff), "s"(srd), "s"(soff), "i"(IMM) : "memory");
    else if (OAUX == 2) asm volatile("buffer_store_dwordx4 %0, %1, %2, %3 offen offset:%4 nt\n\ts_nop 1" :: "v"(v), "v"(voff), "s"(srd), "s"(soff), "i"(IMM) : "memory");
    else if (OAUX == 16) asm volatile("buffer_store_dwordx4 %0, %1, %2, %3 offen offset:%4 sc1\n\ts_nop 1" :: "v"(v), "v"(voff), "s"(srd), "s"(soff), "i"(IMM) : "memory");
    else asm volatile("buffer_store_dwordx4 %0, %1, %2, %3 offen offset:%4\n\ts_nop 1" :: "v"(v), "v"(voff), "s"(srd), "s"(soff), "i"(IMM) : "memory");
}
#define SBAR() __builtin_amdgcn_sched_barrier(0)
#define WAITV(N) asm volatile("s_waitcnt vmcnt(" #N ")" ::: "memory")
#define WAITL() asm volatile("s_waitcnt lgkmcnt(0)" ::: "memory")
#define FENCE() asm volatile("" ::: "memory")
template <typename T> __device__ __forceinline__ T lds_ld(u32 a) { return *(const LAS T*)(uintptr_t)a; }
template <typename T> __device__ __forceinline__ void lds_st(u32 a, T v) { *(LAS T*)(uintptr_t)a = v; }
__device__ __forceinline__ u32x2 lds_tr(u32 a) { return __builtin_bit_cast(u32x2, __builtin_amdgcn_ds_read_tr16_b64_v4i16((LAS s16x4*)(uintptr_t)a)); }
__device__ __forceinline__ void sig(u32 addr, u32 val) { asm volatile("ds_write_b32 %0, %1" :: "v"(addr), "v"(val) : "memory"); }
__device__ __forceinline__ u32 rfl(u32 v) { return (u32)__builtin_amdgcn_readfirstlane(v); }
__device__ __forceinline__ u32 peek4(u32 addr) { u32x4 v; asm volatile("ds_read_b128 %0, %1\n\ts_waitcnt lgkmcnt(0)" : "=v"(v) : "v"(addr) : "memory"); return rfl(umin(umin(v.x, v.y), umin(v.z, v.w))); }
__device__ __forceinline__ u32 peek3(u32 addr) { u32x4 v; asm volatile("ds_read_b128 %0, %1\n\ts_waitcnt lgkmcnt(0)" : "=v"(v) : "v"(addr) : "memory"); return rfl(umin(umin(v.x, v.y), v.z)); }
__device__ __forceinline__ u32 peek1(u32 addr) { u32 v; asm volatile("ds_read_b32 %0, %1\n\ts_waitcnt lgkmcnt(0)" : "=v"(v) : "v"(addr) : "memory"); return rfl(v); }
#define SPINW(cond, slp) do { _Pragma("unroll 1") for (u32 sp_ = 0; (cond) && sp_ < (1u << 22); ++sp_) { if (slp) __builtin_amdgcn_s_sleep(slp); } } while (0)
__device__ __forceinline__ void wait4(u32 addr, u32 val) { SPINW(peek4(addr) < val, WSL); }
__device__ __forceinline__ void wait4s(u32 addr, u32 val) { SPINW(peek4(addr) < val, 2); }
__device__ __forceinline__ void wait3(u32 addr, u32 val) { SPINW(peek3(addr) < val, WSL); }
__device__ __forceinline__ void wait1(u32 addr, u32 val) { SPINW(peek1(addr) < val, WSL); }
__device__ __forceinline__ void wait1s(u32 addr, u32 val) { SPINW(peek1(addr) < val, 2); }
__device__ __forceinline__ u32 get_tag(u32 addr) {
    u32x2 v = {0u, 0u};
#pragma unroll 1
    for (u32 s = 0; s < (1u << 22); ++s) {
        asm volatile("ds_read_b64 %0, %1\n\ts_waitcnt lgkmcnt(0)" : "=v"(v) : "v"(addr) : "memory");
        if (__builtin_amdgcn_readfirstlane(v.y) != 0) break;
    }
    return __builtin_amdgcn_readfirstlane(v.x);
}
__device__ __forceinline__ u32 tag2(u32 t) { return t * 2654435761u + 0x2545F491u; }
typedef f32x2 cf;
__device__ __forceinline__ cf cmul(cf a, cf b) { return cf{__builtin_fmaf(-a.y, b.y, a.x * b.x), __builtin_fmaf(a.y, b.x, a.x * b.y)}; }
__device__ __forceinline__ cf csq(cf a) { return cf{__builtin_fmaf(-a.y, a.y, a.x * a.x), (a.x + a.x) * a.y}; }
__device__ __forceinline__ cf cfma(cf a, cf b, cf c) { return cf{__builtin_fmaf(-a.y, b.y, __builtin_fmaf(a.x, b.x, c.x)), __builtin_fmaf(a.y, b.x, __builtin_fmaf(a.x, b.y, c.y))}; }
__device__ __forceinline__ f32x2 fma2(float a, f32x2 b, f32x2 c) { return f32x2{__builtin_fmaf(a, b.x, c.x), __builtin_fmaf(a, b.y, c.y)}; }
__device__ __forceinline__ u32 pkc(cf a) { return __builtin_bit_cast(u32, __builtin_convertvector(a, bf16x2_t)); }
__device__ __forceinline__ cf cexpk(float zr, float zi, float k) {
    float m = __builtin_amdgcn_exp2f(zr * (k * 1.4426950408889634f));
    float f = __builtin_amdgcn_fractf(zi * (k * 0.15915494309189535f));
    return cf{m * __builtin_amdgcn_cosf(f), m * __builtin_amdgcn_sinf(f)};
}
__device__ __forceinline__ void swap_halves(cf v, cf& lo, cf& hi) {
    auto a = __builtin_amdgcn_permlane32_swap(f2u(v.x), f2u(v.x), false, false);
    auto b = __builtin_amdgcn_permlane32_swap(f2u(v.y), f2u(v.y), false, false);
    lo = cf{u2f(a[0]), u2f(b[0])}; hi = cf{u2f(a[1]), u2f(b[1])};
}
__device__ __forceinline__ void poll_flags(rsrc_t ws, u32 voff, u32 ta, u32 tb) {
#pragma unroll 1
    for (u32 s = 0; s < SPIN_CAP; ++s) {
        FENCE();
        u32x2 v = __builtin_amdgcn_raw_buffer_load_b64(ws, voff, 0, 16);
        voff = ((v.x ^ ta) | (v.y ^ tb)) == 0 ? OOB : voff;
        if (__builtin_amdgcn_ballot_w64(voff != OOB) == 0) break;
        __builtin_amdgcn_s_sleep(PSLEEP);
    }
    FENCE();
}
__device__ __forceinline__ f32x4 sel4(bool c, f32x4 a, f32x4 b) { asm volatile("" : "+v"(a), "+v"(b)); return c ? a : b; }
#define MFMA32(a, b, c) __builtin_amdgcn_mfma_f32_32x32x16_bf16(a, b, c, 0, 0, 0)
#define MFMA16(a, b, c) __builtin_amdgcn_mfma_f32_16x16x32_bf16(a, b, c, 0, 0, 0)

#define STAMP(i) do {} while (0)
#define STAMP_OUT() do {} while (0)

__global__ __launch_bounds__(768, 3) void ssm_fused(Params p) {
    extern __shared__ __attribute__((aligned(16))) char smem[];
    const u32 lds0 = (u32)(uintptr_t)(LAS char*)smem;
    const u32 tid = __builtin_amdgcn_workitem_id_x();
    const int lane = tid & 63;
    const int wv = __builtin_amdgcn_readfirstlane(tid >> 6);
    const int xcd = __builtin_amdgcn_workgroup_id_x() & 7, idx = __builtin_amdgcn_workgroup_id_x() >> 3;
    STAMP(0);
    const u64 tstart = __builtin_amdgcn_s_memrealtime();
    u32 cpf = 0;
    if (CPF && wv == 8) {
        u64 pc0; asm volatile("s_getpc_b64 %0" : "=s"(pc0));
        cpf = __builtin_amdgcn_raw_buffer_load_b32(mk_rsrc((const void*)(pc0 & ~127ull), CODE_BYTES), (u32)lane * 128, (u32)(idx % CODE_SLICES) * 8192, 0);
    }
    sig(lds0 + L_CB1 + umin(tid, 463u) * 4, tid < 448 ? SENT : 0u);
    asm volatile("s_waitcnt lgkmcnt(0)\n\ts_barrier" ::: "memory");
    asm volatile("" :: "s"(p.in[0]), "s"(p.in[1]), "s"(p.in[2]), "s"(p.in[3]), "s"(p.in[4]), "s"(p.in[5]), "s"(p.in[6]), "s"(p.in[7]), "s"(p.in[8]), "s"(p.in[9]),
                 "s"(p.in[10]), "s"(p.in[11]), "s"(p.in[12]), "s"(p.in[13]), "s"(p.in[14]), "s"(p.in[15]), "s"(p.in[16]), "s"(p.in[17]), "s"(p.out), "s"(p.ws));
    const rsrc_t ws = mk_rsrc(p.ws, WS_BYTES);
    const u32x4 wss = mk_srd(p.ws, WS_BYTES);
    const u32 syncL = lds0 + L_SYNC, syncA = syncL + 16, syncK = syncL + 32, tagw = syncL + 48;
    const int rr = lane >> 3, sp = lane & 7;
    const int m16 = lane & 15, q4 = lane >> 4;
    const int ppos = (xcd < 4 ? xcd : (xcd ^ 2)) >> 1;
    const int c = 4 * ppos + (idx & 3), pb = 8 * (xcd & 1) + (idx >> 2);
    const int tb64 = 8 * ppos + 4 * (xcd & 1) + (idx & 3), db = idx >> 2;

    if (wv < 4) {
        const int uw = wv;
        const u32 mysig = syncL + uw * 4;
        {
            __builtin_amdgcn_s_setprio(PRL);
            const rsrc_t rx = mk_rsrc(p.in[0], 2048 * 1024), rbr = mk_rsrc(p.in[3], 512 * 1024), rbi = mk_rsrc(p.in[4], 512 * 1024);
            const rsrc_t rc = mk_rsrc((uw & 2) ? p.in[6] : p.in[5], 512 * 1024);
            const u32 vg = (u32)rr * 4096 + (u32)sp * 16;
            const u32 xs0 = (u32)c * 131072 + (u32)uw * 32768, bs0 = (u32)pb * 32768 + (u32)uw * 1024, cs0 = (u32)db * 65536 + (u32)(uw & 1) * 32768;
            const int r0 = (idx >> 2) + 2 * (idx & 3);
            auto ld = [&](auto ii) INL {
                constexpr int i = decltype(ii)::v;
                if constexpr (i < 48) { constexpr int s = i / 6, j = i % 6; const u32 ch = (u32)((r0 + s) & 7) * 128;
                    if constexpr (j == 0) return __builtin_amdgcn_raw_buffer_load_b128(rbr, vg, bs0 + ch, 0);
                    else if constexpr (j == 1) return __builtin_amdgcn_raw_buffer_load_b128(rbi, vg, bs0 + ch, 0);
                    else return __builtin_amdgcn_raw_buffer_load_b128(rx, vg + (j - 2) * 1024, xs0 + ch, 0); }
                else { constexpr int cg = (0x74321065u >> (4 * ((i - 48) >> 2))) & 15, b = (i - 48) & 3;
                    return __builtin_amdgcn_raw_buffer_load_b128(rc, vg + b * 1024 + cg * 128, cs0, 0); } };
            u32x4 buf[LWIN];
            sfor<0, LWIN>([&](auto ii) INL { buf[decltype(ii)::v] = ld(ii); SBAR(); });
            const u32 w0 = lds0 + (u32)rr * 128 + (u32)((sp ^ (rr >> 1)) * 16);
            u32 wx[2], wxh[2], wb, wbh, wch[2];
#pragma unroll
            for (int k = 0; k < 2; ++k) { wx[k] = (w0 ^ (k * 64)) + uw * 4096; wxh[k] = wx[k] + 65536; wch[k] = (wx[k] ^ 4096) + 65536; }
            wb = (w0 ^ ((uw & 1) * 64)) + uw * 1024; wbh = wb + 65536;
            u32 msg = mysig, vsa = syncA;
            asm volatile("" : "+v"(wx[0]), "+v"(wx[1]), "+v"(wxh[0]), "+v"(wxh[1]), "+v"(wch[0]), "+v"(wch[1]), "+v"(wb), "+v"(wbh), "+v"(msg), "+v"(vsa));
            STAMP(1);
            sfor<0, 80>([&](auto ii) INL {
                constexpr int i = decltype(ii)::v;
                u32 ad;
                if constexpr (i < 48) { constexpr int s = i / 6, j = i % 6, o = (s % 5) * 24576;
                    if constexpr (j == 0 && s >= 5) wait4(vsa, s - 4);
                    if constexpr (j < 2) { constexpr int ob = o + 16384 + j * 4096; ad = ob < 65536 ? wb + ob : wbh + (ob - 65536); }
                    else { constexpr int ox = o + (j - 2) * 1024; ad = ox < 65536 ? wx[j & 1] + ox : wxh[j & 1] + (ox - 65536); }
                } else { constexpr int q = (i - 48) >> 2, cg = (0x74321065u >> (4 * q)) & 15, need = (0x98877654u >> (4 * q)) & 15, pneed = (0x88776540u >> (4 * q)) & 15, b = (i - 48) & 3;
                    if constexpr (b == 0 && need != pneed) wait4s(vsa, need);
                    ad = (cg < 4 ? wx[b & 1] : wch[b & 1]) + (cg & 3) * 16384 + b * 1024;
                }
                lds_st<u32x4>(ad, buf[i % LWIN]);
                SBAR();
                if constexpr (i + LWIN < 80) { buf[i % LWIN] = ld(ic<i + LWIN>{}); SBAR(); }
                if constexpr (i < 48 && i % 6 == 5) { sig(msg, i / 6 + 1); SBAR(); }
                if constexpr (i == 5) STAMP(2);
                if constexpr (i == 47) { STAMP(3); __builtin_amdgcn_s_setprio(0); }
            });
            sig(mysig, 9);
            STAMP(5);
        }
    }
    int wv2 = wv;
    asm volatile("" : "+s"(wv2));
    if (wv2 < 4) {
        const int uw = wv;
        const rsrc_t ru = mk_rsrc(p.in[9], 2048 * 1024);
        __builtin_amdgcn_s_setprio(1);
        const int dsel = uw >> 1, half = uw & 1;
        const u32 im = lds0 + L_UB + dsel * UB_SZ + UB_PAD + half * 2048;
        const int dd = 64 * xcd + half * 32;
        const u32 ta = get_tag(tagw), tb = tag2(ta);
        const int sel = 2 * idx + dsel, prr = sel >> 4;
        const u32 trow = (u32)(256 * ((m16 >> 2) & 1) + 16 * (m16 >> 3) + (m16 & 3)), tbase = (u32)(512 * prr + 64 * (sel & 3) + 4 * ((sel >> 2) & 3));
        f32x4 sk[4];
        {
            u32x2 uu[4], dk[2];
#pragma unroll
            for (int k = 0; k < 4; ++k) uu[k] = __builtin_amdgcn_raw_buffer_load_b64(ru, trow * 1024 + (u32)q4 * 8 + (k & 1) * 32, (tbase + 32 * (k >> 1)) * 1024 + (u32)dd * 2, 0);
#pragma unroll
            for (int k = 0; k < 2; ++k) dk[k] = __builtin_amdgcn_raw_buffer_load_b64(mk_rsrc(p.in[17], 1024), (u32)q4 * 8 + k * 32, (u32)dd * 2, 0);
#pragma unroll
            for (int k = 0; k < 4; ++k) {
                const u32x2 a = uu[k], b = dk[k & 1];
                const u32 w0 = pkbf(u2f(a.x << 16) * u2f(b.x << 16), u2f(a.x & 0xFFFF0000u) * u2f(b.x & 0xFFFF0000u));
                const u32 w1 = pkbf(u2f(a.y << 16) * u2f(b.y << 16), u2f(a.y & 0xFFFF0000u) * u2f(b.y & 0xFFFF0000u));
                sk[k] = f32x4{u2f(w0 << 16), u2f(w0 & 0xFFFF0000u), u2f(w1 << 16), u2f(w1 & 0xFFFF0000u)};
                asm volatile("" : "+v"(sk[k]));
            }
        }
        STAMP(6);
        if (ABL == 0) {
            wait4s(syncK, 4);
            STAMP(7);
            poll_flags(ws, lane < 32 ? WS_YFLAG + (u32)((prr >> 1) * 512 + dd + lane) * FS : OOB, ta, tb);
            __builtin_amdgcn_s_setprio(3);
            STAMP(8);
            const u32 so = WS_YT + (u32)dd * 4096 + (u32)sel * 64;
            u32x4 a = __builtin_amdgcn_raw_buffer_load_b128(ws, (u32)(lane >> 2) * 4096 + (u32)(lane & 3) * 16, so, 16);
            u32x4 b = __builtin_amdgcn_raw_buffer_load_b128(ws, (u32)(lane >> 2) * 4096 + (u32)(lane & 3) * 16, so + 65536, 16);
            lds_st<u32x4>(im + lane * 16, a);
            lds_st<u32x4>(im + 1024 + lane * 16, b);
            FENCE();
            const u32x4 ro = mk_srd(p.out, 2048 * 4096);
            const u32 ra = im + (u32)(4 * q4 + (m16 >> 2)) * 64 + (u32)(lane & 3) * 8;
#pragma unroll
            for (int k = 0; k < 4; ++k) {
                u32x2 v = lds_tr(ra + (k & 1) * 1024 + (k >> 1) * 32);
                f32x4 f = f32x4{u2f(v.x << 16), u2f(v.x & 0xFFFF0000u), u2f(v.y << 16), u2f(v.y & 0xFFFF0000u)} + sk[k];
                const u32 w0 = pkbf(f[0], f[1]), w1 = pkbf(f[2], f[3]);
                st128<0, false>(ro, u32x4{w0 << 16, w0 & 0xFFFF0000u, w1 << 16, w1 & 0xFFFF0000u}, trow * 4096 + (u32)q4 * 16, (tbase + 32 * (k >> 1)) * 4096 + (u32)(512 + dd) * 4 + (k & 1) * 64);
            }
        }
        STAMP(9);
    } else if (__builtin_expect(wv < 8, 1)) {
        const int aw = wv - 4;
        __builtin_amdgcn_s_setprio(PRA);
        const int r32 = lane & 31, hh = lane >> 5;
        const int g32 = 4 * (r32 & 7) + (r32 >> 3);
        const u32 q_lr = __builtin_amdgcn_raw_buffer_load_b16(mk_rsrc(p.in[1], 1024), (u32)g32 * 2, (u32)pb * 64, 0);
        const u32 q_li = __builtin_amdgcn_raw_buffer_load_b16(mk_rsrc(p.in[2], 1024), (u32)g32 * 2, (u32)pb * 64, 0);
        const u32 q_ld = __builtin_amdgcn_raw_buffer_load_b16(mk_rsrc(p.in[7], 1024), (u32)g32 * 2, (u32)pb * 64, 0);
        const u32 wv0 = __builtin_amdgcn_raw_buffer_load_b32(ws, 0, WS_W, 16);
        u32x4 ut[2];
#pragma unroll
        for (int k = 0; k < 2; ++k) ut[k] = __builtin_amdgcn_raw_buffer_load_b128(mk_rsrc(p.in[9], 2048 * 1024), (u32)(lane >> 2) * 1024 + (u32)(lane & 3) * 16, (u32)(idx * 64 + (aw >> 1) * 32 + 16 * k) * 1024 + (u32)xcd * 128 + (u32)(aw & 1) * 64, 0);
        SBAR();
        u32 fo[4], xo[4], fh[4], xh[4];
#pragma unroll
        for (int k = 0; k < 4; ++k) { fo[k] = lds0 + (u32)r32 * 128 + (u32)(((2 * k + hh) ^ ((r32 >> 1) & 7)) * 16); xo[k] = fo[k] + aw * 4096; fh[k] = fo[k] + 65536; xh[k] = xo[k] + 65536; }
#pragma unroll
        for (int k = 0; k < 4; ++k) asm volatile("" : "+v"(fo[k]), "+v"(xo[k]), "+v"(fh[k]), "+v"(xh[k]));
        u32 mysig = syncA + aw * 4, vsl = syncL, c1r = lds0 + L_CB1 + r32 * 8, c2r = lds0 + L_CB2 + r32 * 32, l16 = (u32)lane * 16;
        u32 c1w = c1r + aw * 256, c2w = c2r + aw * 8, evo = hh ? OOB : (u32)r32 * 16;
        u32 vo[2];
        {
            const int cb0 = c - 1 - hh - 4 * aw;
            const u32 vo0 = WS_E + (u32)(cb0 * 512 + pb * 32 + r32) * 16;
            vo[0] = cb0 >= 0 ? vo0 : OOB; vo[1] = cb0 >= 2 ? vo0 - 16384 : OOB;
        }
        asm volatile("" : "+v"(mysig), "+v"(vsl), "+v"(c1r), "+v"(c2r), "+v"(c1w), "+v"(c2w), "+v"(evo), "+v"(vo[0]), "+v"(vo[1]), "+v"(l16));
        STAMP(1);
        cf P[4], L8, L16, L32, W0, W1, coef, Q;
        {
            const float a_lr = bf2f(q_lr), a_li = bf2f(q_li);
            const float dt = rbf(__builtin_expf(bf2f(q_ld)));
            P[0] = cexpk(dt * a_lr, dt * a_li, 1.f);
            P[1] = csq(P[0]); P[2] = cmul(P[0], P[1]); P[3] = csq(P[1]);
            L8 = csq(P[3]); L16 = csq(L8); L32 = csq(L16);
            const cf L64 = csq(L32), L128 = csq(L64), L256 = csq(L128), L512 = csq(L256);
            W0 = hh ? L128 : cf{1.f, 0.f};
            if (aw & 1) W0 = cmul(W0, L512);
            if (aw & 2) W0 = cmul(W0, csq(L512));
            W1 = cmul(W0, L256);
            const float nr = P[0].x - 1.f, ni = P[0].y, inv = 2.f / (a_lr * a_lr + a_li * a_li);
            coef = cf{(nr * a_lr + ni * a_li) * inv, (ni * a_lr - nr * a_li) * inv};
            Q = hh ? cmul(coef, L16) : coef;
            if (aw & 1) Q = cmul(Q, L32);
            if (aw & 2) Q = cmul(Q, L64);
            asm volatile("" : "+v"(P[0]), "+v"(P[1]), "+v"(P[2]), "+v"(P[3]), "+v"(L8), "+v"(L16), "+v"(L32), "+v"(W0), "+v"(W1), "+v"(coef), "+v"(Q));
        }
        const u32 ta = rfl(wv0) + 1u, tb = tag2(ta);
        {
            if (aw == 0) lds_st<u32x2>(tagw, u32x2{ta, 1u});
            const u32 im = lds0 + L_UB + UB_PAD + (aw >> 1) * UB_SZ + (aw & 1) * 2048;
            lds_st<u32x4>(im + l16, ut[0]);
            lds_st<u32x4>(im + 1024 + l16, ut[1]);
            FENCE();
            const u32 tra = im + (u32)(8 * (lane & 3) + (m16 >> 2)) * 64 + (u32)q4 * 8;
#pragma unroll
            for (int k = 0; k < 2; ++k) { const u32x2 x = lds_tr(tra + k * 32), y = lds_tr(tra + k * 32 + 256); ut[k] = u32x4{x.x, x.y, y.x, y.y}; }
#pragma unroll
            for (int k = 0; k < 2; ++k) st128<0, true>(wss, ut[k], (u32)q4 * 16384 + (u32)m16 * 16, WS_UT + (u32)(16 * xcd + 8 * (aw & 1) + 4 * k) * 16384 + (u32)(2 * idx + (aw >> 1)) * 256);
        }
        u32 fvo = lane ? OOB : 0u;
        u32x2 tg = {ta, tb};
        asm volatile("" : "+v"(fvo), "+v"(tg));
        STAMP(2);
        f32x16 accR = {}, accI = {};
        {
            bf16x8 fa[4], fr[4], fi[4];
            auto rd = [&](int n) { const int s = n >> 2, k = n & 3, o = (s % 5) * 24576, ob = o + 16384;
                fa[n & 3] = lds_ld<bf16x8>(o < 65536 ? xo[k] + o : xh[k] + (o - 65536));
                fr[n & 3] = lds_ld<bf16x8>(ob < 65536 - 4096 ? fo[k] + ob : fh[k] + (ob - 65536));
                fi[n & 3] = lds_ld<bf16x8>(ob < 65536 - 4096 ? fo[k] + ob + 4096 : fh[k] + (ob + 4096 - 65536)); };
            wait4(syncL, 1);
            STAMP(10);
            u32x4 fl = *(volatile const LAS u32x4*)(uintptr_t)vsl;
            rd(0); rd(1); rd(2);
            SBAR();
#pragma unroll
            for (int n = 0; n < 32; ++n) {
                const int s = n >> 2, k = n & 3;
                if (k == 1 && s < 7) {
                    if (rfl(umin(umin(fl.x, fl.y), umin(fl.z, fl.w))) < (u32)(s + 2)) wait4(syncL, s + 2);
                    FENCE();
                }
                if (k == 2 && s < 6) fl = *(volatile const LAS u32x4*)(uintptr_t)vsl;
                accR = MFMA32(fa[k], fr[k], accR);
                SBAR();
                if (n + 3 < 32) rd(n + 3);
                if (k == 0) sig(mysig, s + 1);
                if (n == UFN) { WAITV(0); __builtin_amdgcn_raw_buffer_store_b64(tg, ws, fvo, WS_UFLAG + (u32)((2 * xcd + (aw & 1)) * 64 + 2 * idx + (aw >> 1)) * FS, 16); FENCE(); }
                accI = MFMA32(fa[k], fi[k], accI);
                SBAR();
            }
        }
        __builtin_amdgcn_s_setprio(3);
        STAMP(3);
        f32x2 hr[8], hi[8];
#pragma unroll
        for (int i = 0; i < 8; ++i) { hr[i] = f32x2{accR[2 * i], accR[2 * i + 1]}; hi[i] = f32x2{accI[2 * i], accI[2 * i + 1]}; }
#pragma unroll
        for (int i = 2; i < 8; ++i) {
            f32x2 nr = fma2(-P[0].y, hi[i - 2], fma2(P[0].x, hr[i - 2], hr[i]));
            f32x2 ni = fma2(P[0].y, hr[i - 2], fma2(P[0].x, hi[i - 2], hi[i]));
            hr[i] = nr; hi[i] = ni;
        }
        cf e0, e1, pre = {0.f, 0.f};
        {
            const f32x2 zr = fma2(-L8.y, hi[6], fma2(L8.x, hr[6], hr[7])), zi = fma2(L8.y, hr[6], fma2(L8.x, hi[6], hi[7]));
            const cf e16 = {__builtin_fmaf(-P[3].y, zi.x, __builtin_fmaf(P[3].x, zr.x, zr.y)), __builtin_fmaf(P[3].y, zr.x, __builtin_fmaf(P[3].x, zi.x, zi.y))};
            swap_halves(e16, e0, e1);
        }
        const cf e32 = cfma(e0, L16, e1);
        if (aw < 3) { lds_st<f32x2>(c1w, e32); FENCE(); }
        u32x4 g[2];
        if (aw < 3) {
#pragma unroll
            for (int k = 0; k < 2; ++k) g[k] = __builtin_amdgcn_raw_buffer_load_b128(ws, vo[k], 0, 16);
        }
        if (aw > 0) {
            cf w0, w1, w2;
#pragma unroll 1
            for (u32 spn = 0; spn < (1u << 22); ++spn) {
                asm volatile("ds_read_b64 %0, %3\n\tds_read_b64 %1, %3 offset:256\n\tds_read_b64 %2, %3 offset:512\n\ts_waitcnt lgkmcnt(0)" : "=&v"(w0), "=&v"(w1), "=&v"(w2) : "v"(c1r) : "memory");
                const u32 a = f2u(w0.x), b = f2u(w1.x), cc = f2u(w2.x);
                const u32 mx = a > b ? (a > cc ? a : cc) : (b > cc ? b : cc);
                if (__builtin_amdgcn_ballot_w64(mx == SENT) == 0) break;
            }
            const cf p2 = cfma(w0, L32, w1), p3 = cfma(p2, L32, w2);
            pre = aw == 1 ? w0 : (aw == 2 ? p2 : p3);
            if (aw == 3) {
                if (c < 15) { const cf E = cfma(p3, L32, e32); st128<0, true>(wss, u32x4{f2u(E.x), ta, f2u(E.y), tb}, evo, WS_E + (u32)(c * 512 + pb * 32) * 16); }
#pragma unroll
                for (int k = 0; k < 2; ++k) g[k] = __builtin_amdgcn_raw_buffer_load_b128(ws, vo[k], 0, 16);
            }
        }
        STAMP(4);
        const int th = aw & 1, kh = aw >> 1;
        const int t0 = tb64 * 64 + th * 32, dcol = db * 64 + kh * 32, t32 = tb64 * 2 + th;
        u32 xk[16];
        {
            const rsrc_t rx = mk_rsrc(p.in[0], 2048 * 1024);
#pragma unroll
            for (int r = 0; r < 16; ++r)
                xk[r] = __builtin_amdgcn_raw_buffer_load_b16(rx, (u32)hh * 4096 + (u32)g32 * 2 + (r & 3) * 1024, (u32)t0 * 1024 + (u32)dcol * 2 + (r >> 2) * 8192, 0);
        }
        const u32 dsk = __builtin_amdgcn_raw_buffer_load_b16(mk_rsrc(p.in[8], 1024), (u32)g32 * 2, (u32)dcol * 2, 0);
        SBAR();
        f32x2 cpr[2], cpi[2], cqr[2], cqi[2];
        {
            { const cf pre1 = cfma(pre, L16, e0); pre = hh ? pre1 : pre; }
            cf s[4], q[4];
            s[0] = pre; q[0] = Q;
            s[1] = cfma(s[0], P[3], cf{hr[6].x, hi[6].x}); s[2] = cfma(s[1], P[3], cf{hr[6].y, hi[6].y}); s[3] = cfma(s[2], P[3], cf{hr[7].x, hi[7].x});
#pragma unroll
            for (int a = 1; a < 4; ++a) q[a] = cmul(q[a - 1], P[3]);
#pragma unroll
            for (int a = 0; a < 4; ++a) s[a] = cmul(s[a], coef);
#pragma unroll
            for (int u = 0; u < 2; ++u) { cpr[u] = f32x2{s[2 * u].x, s[2 * u + 1].x}; cpi[u] = f32x2{s[2 * u].y, s[2 * u + 1].y}; cqr[u] = f32x2{q[2 * u].x, q[2 * u + 1].x}; cqi[u] = f32x2{q[2 * u].y, q[2 * u + 1].y};
                asm volatile("" : "+v"(cpr[u]), "+v"(cpi[u]), "+v"(cqr[u]), "+v"(cqi[u])); }
        }
#pragma unroll
        for (int i = 0; i < 8; ++i) {
            f32x2 nr = fma2(-coef.y, hi[i], coef.x * hr[i]);
            f32x2 ni = fma2(-coef.y, hr[i], -coef.x * hi[i]);
            hr[i] = nr; hi[i] = ni;
            asm volatile("" : "+v"(hr[i]), "+v"(hi[i]));
        }
        const u32 scr = lds0 + 7 * 16384 + aw * 4096;
        u32 wa0 = scr + (u32)(r32 ^ ((r32 >> 3) & 3)) * 64 + (u32)(((2 * hh) ^ ((r32 >> 1) & 3)) * 16), wa1 = wa0 ^ 16;
        u32 ra[4], pvo = lane < 8 ? WS_HFLAG + (u32)(t32 * 16 + kh * 8 + lane) * FS : OOB;
        { const int q = m16 >> 2, pp = lane & 3;
          const u32 b0 = scr + (u32)(8 * q + ((2 * hh) ^ q)) * 64 + (u32)(((2 * (q4 & 1) + (pp >> 1)) ^ hh) * 16) + (u32)(pp & 1) * 8;
          ra[0] = b0; ra[1] = b0 ^ 64; ra[2] = (b0 ^ 32) + 256; ra[3] = (b0 ^ 96) + 256; }
        asm volatile("" : "+v"(ra[0]), "+v"(ra[1]), "+v"(ra[2]), "+v"(ra[3]), "+v"(wa0), "+v"(wa1), "+v"(pvo));
        SBAR();
        cf ev[2] = {cf{0.f, 0.f}, cf{0.f, 0.f}};
#pragma unroll 1
        for (u32 spn = 0; spn < SPIN_CAP; ++spn) {
#pragma unroll
            for (int k = 0; k < 2; ++k) {
                const bool good = ((g[k].y ^ ta) | (g[k].w ^ tb)) == 0;
                ev[k].x = good ? u2f(g[k].x) : ev[k].x; ev[k].y = good ? u2f(g[k].z) : ev[k].y;
                vo[k] = good ? OOB : vo[k];
            }
            if (__builtin_amdgcn_ballot_w64(umin(vo[0], vo[1]) != OOB) == 0) break;
            FENCE();
#pragma unroll
            for (int k = 0; k < 2; ++k) g[k] = __builtin_amdgcn_raw_buffer_load_b128(ws, vo[k], 0, 16);
        }
        {
            cf G0, G1;
            swap_halves(cfma(ev[1], W1, cmul(ev[0], W0)), G0, G1);
            lds_st<f32x2>(c2w, G0 + G1);
            f32x4 h01, h23;
#pragma unroll 1
            for (u32 spn = 0; spn < (1u << 22); ++spn) {
                asm volatile("ds_read_b128 %0, %2\n\tds_read_b128 %1, %2 offset:16\n\ts_waitcnt lgkmcnt(0)" : "=&v"(h01), "=&v"(h23) : "v"(c2r) : "memory");
                const u32 a = f2u(h01[0]), b = f2u(h01[2]), cc = f2u(h23[0]), dd = f2u(h23[2]);
                const u32 m1 = a > b ? a : b, m2 = cc > dd ? cc : dd;
                if (__builtin_amdgcn_ballot_w64((m1 > m2 ? m1 : m2) == SENT) == 0) break;
            }
            STAMP(5);
            const f32x4 hs4 = h01 + h23;
            const cf H = {hs4[0] + hs4[2], hs4[1] + hs4[3]};
            f32x2 gr[2], gi[2];
#pragma unroll
            for (int u = 0; u < 2; ++u) { gr[u] = fma2(-H.y, cqi[u], fma2(H.x, cqr[u], cpr[u])); gi[u] = fma2(H.x, cqi[u], fma2(H.y, cqr[u], cpi[u])); }
#pragma unroll
            for (int i = 0; i < 8; ++i) hr[i] = fma2(-P[i >> 1].y, gi[i & 1], fma2(P[i >> 1].x, gr[i & 1], hr[i]));
            lds_st<u32x4>(wa0, u32x4{pkbf(hr[0].x, hr[2].x), pkbf(hr[4].x, hr[6].x), pkbf(hr[0].y, hr[2].y), pkbf(hr[4].y, hr[6].y)});
            lds_st<u32x4>(wa1, u32x4{pkbf(hr[1].x, hr[3].x), pkbf(hr[5].x, hr[7].x), pkbf(hr[1].y, hr[3].y), pkbf(hr[5].y, hr[7].y)});
            FENCE();
            u32x2 tv[8];
#pragma unroll
            for (int s = 0; s < 4; ++s) tv[s] = lds_tr(ra[s]);
            SBAR();
#pragma unroll
            for (int i = 0; i < 8; ++i) hi[i] = fma2(-P[i >> 1].y, gr[i & 1], fma2(-P[i >> 1].x, gi[i & 1], hi[i]));
            lds_st<u32x4>(wa0 + 2048, u32x4{pkbf(hi[0].x, hi[2].x), pkbf(hi[4].x, hi[6].x), pkbf(hi[0].y, hi[2].y), pkbf(hi[4].y, hi[6].y)});
            lds_st<u32x4>(wa1 + 2048, u32x4{pkbf(hi[1].x, hi[3].x), pkbf(hi[5].x, hi[7].x), pkbf(hi[1].y, hi[3].y), pkbf(hi[5].y, hi[7].y)});
            FENCE();
#pragma unroll
            for (int s = 0; s < 4; ++s) tv[4 + s] = lds_tr(ra[s] + 2048);
            const u32 hs = WS_H + (u32)((c * 4 + aw) * 16 + pb) * 4096;
            st128<0, true>(wss, u32x4{tv[0].x, tv[0].y, tv[1].x, tv[1].y}, l16, hs);
            st128<1024, true>(wss, u32x4{tv[2].x, tv[2].y, tv[3].x, tv[3].y}, l16, hs);
            st128<2048, true>(wss, u32x4{tv[4].x, tv[4].y, tv[5].x, tv[5].y}, l16, hs);
            st128<3072, true>(wss, u32x4{tv[6].x, tv[6].y, tv[7].x, tv[7].y}, l16, hs);
            sig(mysig, 9);
        }
        f32x16 y0, y1 = {};
        {
            const float dk = bf2f(dsk);
#pragma unroll
            for (int r = 0; r < 16; r += 2) { const u32 w = pkbf(bf2f(xk[r]) * dk, bf2f(xk[r + 1]) * dk); y0[r] = u2f(w << 16); y0[r + 1] = u2f(w & 0xFFFF0000u); }
            asm volatile("" : "+v"(y0));
        }
        WAITV(0);
        __builtin_amdgcn_raw_buffer_store_b64(tg, ws, fvo, WS_HFLAG + (u32)((c * 4 + aw) * 16 + pb) * FS, 16);
        FENCE();
        STAMP(6);
        u32 ca[4], ov[4];
#pragma unroll
        for (int k = 0; k < 4; ++k) { ca[k] = fo[k] + (u32)kh * 65536; ov[k] = (u32)hh * 16384 + (u32)g32 * 4 + k * 4096; asm volatile("" : "+v"(ca[k]), "+v"(ov[k])); }
        u32 xb = lds0 + (u32)(4 * kh + (db & 3)) * 16384 + (u32)th * 4096 + l16;
        asm volatile("" : "+v"(xb));
        wait4(syncL, 9);
        bf16x8 cb0[4], cb1[4];
        auto cad = [&](int f) { const int jj = f >> 3, a = (f >> 2) & 1, s = f & 3;
            return ca[2 * a + (s & 1)] + (u32)((jj + db) & 3) * 16384 + (s >> 1) * 8192; };
#pragma unroll
        for (int f = 0; f < 3; ++f) { const u32 ad = cad(f); cb0[f] = lds_ld<bf16x8>(ad); cb1[f] = lds_ld<bf16x8>(ad + 4096); }
        poll_flags(ws, pvo, ta, tb);
        STAMP(7);
        {
            u32x4 hf[16];
            auto ldh = [&](int f) { const u32 cg = (u32)(4 * kh + (((f >> 3) + db) & 3));
                return __builtin_amdgcn_raw_buffer_load_b128(ws, l16 + (f & 3) * 1024, WS_H + (u32)t32 * 65536 + cg * 8192 + ((f >> 2) & 1) * 4096, 16); };
#pragma unroll
            for (int f = 0; f < HWIN; ++f) { hf[f] = ldh(f); SBAR(); }
#pragma unroll
            for (int f = 0; f < 32; ++f) {
                bf16x8 av = __builtin_bit_cast(bf16x8, hf[f & 15]);
                y0 = MFMA32(av, cb0[f & 3], y0);
                SBAR();
                if (f + 3 < 32) { const u32 ad = cad(f + 3); cb0[(f + 3) & 3] = lds_ld<bf16x8>(ad); cb1[(f + 3) & 3] = lds_ld<bf16x8>(ad + 4096); }
                y1 = MFMA32(av, cb1[f & 3], y1);
                SBAR();
                if (f + HWIN < 32) { hf[(f + HWIN) & 15] = ldh(f + HWIN); SBAR(); }
                if (f == 8) { sig(mysig, 10); SBAR(); }
                if (f == 28) { wait4(syncA, 10); SBAR(); }
            }
        }
        STAMP(8);
        {
#pragma unroll
            for (int k = 0; k < 4; ++k) lds_st<f32x4>(xb + k * 1024, f32x4{y1[4 * k], y1[4 * k + 1], y1[4 * k + 2], y1[4 * k + 3]});
            sig(mysig, 11);
            wait1(syncA + (aw ^ 2) * 4, 11);
            const rsrc_t ro = mk_rsrc(p.out, 2048 * 4096);
            f32x4 o[4];
#pragma unroll
            for (int k = 0; k < 4; ++k) o[k] = lds_ld<f32x4>((xb ^ 65536) + k * 1024);
#pragma unroll
            for (int k = 0; k < 4; ++k) {
#pragma unroll
                for (int j = 0; j < 4; ++j)
                    __builtin_amdgcn_raw_buffer_store_b32(f2u(y0[4 * k + j] + o[k][j]), ro, ov[j], (u32)(t0 + 8 * k) * 4096 + (u32)dcol * 4, OAUX);
            }
            if (tb64 == 31 && db == 0 && aw == 0) __builtin_amdgcn_raw_buffer_store_b32(ta, ws, fvo, WS_W, 16);
        }
        STAMP(9);
    } else {
        const int bw = wv - 8, dsel = bw >> 1, half = bw & 1;
        if (ABL >= 1) return;
        __builtin_amdgcn_s_setprio(PRK);
        const int d = 64 * xcd + 2 * idx + dsel;
        const u32 s0 = lds0 + L_S0 + bw * S0_SZ, kc = lds0 + L_KC + dsel * KC_SZ, ub = lds0 + L_UB + dsel * UB_SZ;
        const u32 mysig = syncK + bw * 4, psig = syncK + (bw ^ 1) * 4;
        u32 pr[6];
#pragma unroll
        for (int i = 0; i < 6; ++i) pr[i] = __builtin_amdgcn_raw_buffer_load_b16(mk_rsrc(p.in[10 + i], 512 * 256), (u32)lane * 2, (u32)(d * 128 + half * 64) * 2, 0);
        const u32 q_ld = __builtin_amdgcn_raw_buffer_load_b16(mk_rsrc(p.in[16], 1024), 0, (u32)d * 2, 0);
        SBAR();
        u32 tch = 0, tch2 = 0;
        if (TOUCH) {
            const rsrc_t ro = mk_rsrc(p.out, 2048 * 4096);
            if (bw == 0) {
                tch = __builtin_amdgcn_raw_buffer_load_b32(ro, (u32)lane * 4096, (u32)tb64 * 262144, 0);
                tch2 = __builtin_amdgcn_raw_buffer_load_b32(ws, (u32)lane * 4096, WS_YT + (u32)xcd * 262144, 16);
            } else if (bw == 1) {
                const int sel = 2 * idx + (lane >> 5), i16 = lane & 15;
                const u32 row = (u32)(512 * (sel >> 4) + 64 * (sel & 3) + 4 * ((sel >> 2) & 3) + 256 * ((i16 >> 2) & 1) + 16 * (i16 >> 3) + (i16 & 3) + 32 * ((lane >> 4) & 1));
                tch = __builtin_amdgcn_raw_buffer_load_b32(ro, row * 4096, 0, 0);
                tch2 = __builtin_amdgcn_raw_buffer_load_b32(ws, (u32)lane * 4096, WS_UT + (u32)xcd * 262144, 16);
            } else if (bw == 2) {
                const u32 a = lane < 32 ? WS_H + (u32)tb64 * 131072 + (u32)lane * 4096 : lane < 36 ? WS_H + (u32)((c * 4 + lane - 32) * 16 + pb) * 4096 : lane < 52 ? WS_E + (u32)((lane - 36) * 512 + pb * 32) * 16
                            : lane < 54 ? WS_HFLAG + (u32)(lane == 52 ? tb64 * 2 : c * 4) * 2048 : lane < 56 ? WS_HFLAG + (u32)(c * 4 + 2) * 2048 : lane < 60 ? WS_UFLAG + (u32)xcd * 16384 + (u32)(lane - 56) * 4096 : WS_YFLAG + (u32)((lane >> 1) & 1) * 65536 + (u32)xcd * 8192 + (u32)(lane & 1) * 4096;
                tch = __builtin_amdgcn_raw_buffer_load_b32(ws, a, 0, 16);
                tch2 = __builtin_amdgcn_raw_buffer_load_b32(mk_rsrc(p.in[8], 1024), 0, 0, 0) + __builtin_amdgcn_raw_buffer_load_b32(mk_rsrc(p.in[17], 1024), 0, 0, 0);
            } else {
                tch = __builtin_amdgcn_raw_buffer_load_b32(mk_rsrc(p.in[5], 512 * 1024), (u32)(lane & 15) * 4096, (u32)db * 65536, 0);
                tch2 = __builtin_amdgcn_raw_buffer_load_b32(mk_rsrc(p.in[6], 512 * 1024), (u32)(lane & 15) * 4096, (u32)db * 65536, 0);
            }
        }
        SBAR();
        lds_st<u32x2>(ub + umin(lane, 59) * 8, u32x2{0u, 0u});
        u32 ga[19];
        {
            const int lag0 = 256 * rr + 128 * half + 127 - 18 * sp;
#pragma unroll
            for (int e = 0; e < 19; ++e) {
                const int lag = lag0 - e;
                const u32 bits = pkbf((float)lag, 0.f) & 0xFFFFu;
                const u32 ix = lag < 128 ? (u32)lag : bits - (0x4300u - 128u);
                ga[e] = lag < 0 ? ub : kc + 2 * ix;
                asm volatile("" : "+v"(ga[e]));
            }
        }
        asm volatile("" :: "v"(cpf));
        STAMP(1);
        f32x4 ka[3];
        {
            const float dt = rbf(__builtin_expf(bf2f(q_ld)));
            const float zr = dt * bf2f(pr[0]), zi = dt * bf2f(pr[1]);
            const cf db_ = {dt * bf2f(pr[2]), -dt * bf2f(pr[3])}, cc = {bf2f(pr[4]), -bf2f(pr[5])};
            const cf E1 = cexpk(zr, zi, 1.f), E8c = cexpk(zr, -zi, 8.f), E128 = cexpk(zr, zi, 128.f);
            const u32 wa = s0 + lane * 4, ra = s0 + (u32)m16 * 288 + (u32)q4 * 16;
            cf V[16], R[8], W[17];
            { const cf E16 = cmul(E8c, E8c), E32 = cmul(E16, E16), E64 = cmul(E32, E32);
              V[0] = cmul(cc, db_); V[1] = cmul(V[0], E8c);
#pragma unroll
              for (int j = 0; j < 2; ++j) V[2 + j] = cmul(V[j], E16);
#pragma unroll
              for (int j = 0; j < 4; ++j) V[4 + j] = cmul(V[j], E32);
#pragma unroll
              for (int j = 0; j < 8; ++j) V[8 + j] = cmul(V[j], E64); }
            R[0] = cf{1.f, 0.f}; R[1] = E1; R[2] = cmul(E1, E1); R[3] = cmul(R[1], R[2]); R[4] = cmul(R[2], R[2]);
#pragma unroll
            for (int r = 1; r < 4; ++r) R[4 + r] = cmul(R[r], R[4]);
            W[1] = E128; W[2] = cmul(W[1], W[1]); W[3] = cmul(W[1], W[2]); W[4] = cmul(W[2], W[2]);
#pragma unroll
            for (int k = 1; k <= 4; ++k) W[4 + k] = cmul(W[k], W[4]);
#pragma unroll
            for (int k = 1; k <= 8; ++k) W[8 + k] = cmul(W[k], W[8]);
#pragma unroll
            for (int j = 0; j < 16; ++j) lds_st<u32>(wa + j * 288, pkc(V[j]));
            FENCE();
            bf16x8 vf[4], uf[4];
#pragma unroll
            for (int s = 0; s < 4; ++s) vf[s] = lds_ld<bf16x8>(ra + s * 64);
            FENCE();
#pragma unroll
            for (int r = 0; r < 8; ++r) { lds_st<u32>(wa + r * 288, pkc(R[r])); lds_st<u32>(wa + (8 + r) * 288, pkc(r ? cmul(R[r], W[1]) : W[1])); }
            FENCE();
#pragma unroll
            for (int s = 0; s < 4; ++s) uf[s] = lds_ld<bf16x8>(ra + s * 64);
            FENCE();
#pragma unroll
            for (int k = 0; k < 2; ++k)
#pragma unroll
                for (int r = 0; r < 4; ++r) lds_st<u32>(wa + (4 * k + r) * 288, pkc(r ? cmul(R[2 * r], W[2 + k]) : W[2 + k]));
#pragma unroll
            for (int k = 0; k < 4; ++k) { lds_st<u32>(wa + (8 + 2 * k) * 288, pkc(W[4 + k])); lds_st<u32>(wa + (9 + 2 * k) * 288, pkc(cmul(W[4 + k], R[4]))); }
            FENCE();
            ka[0] = f32x4{};
#pragma unroll
            for (int s = 0; s < 4; ++s) ka[0] = MFMA16(uf[s], vf[s], ka[0]);
#pragma unroll
            for (int s = 0; s < 4; ++s) uf[s] = lds_ld<bf16x8>(ra + s * 64);
            FENCE();
#pragma unroll
            for (int k = 0; k < 9; ++k) lds_st<u32>(wa + k * 288, pkc(W[8 + k]));
            FENCE();
            ka[1] = f32x4{};
#pragma unroll
            for (int s = 0; s < 4; ++s) ka[1] = MFMA16(uf[s], vf[s], ka[1]);
#pragma unroll
            for (int s = 0; s < 4; ++s) uf[s] = lds_ld<bf16x8>(ra + s * 64);
            FENCE();
            ka[2] = f32x4{};
#pragma unroll
            for (int s = 0; s < 4; ++s) ka[2] = MFMA16(uf[s], vf[s], ka[2]);
        }
        asm volatile("" :: "v"(tch), "v"(tch2));
        STAMP(3);
        const u32 ta = get_tag(tagw), tb = tag2(ta);
        u32 uvo = WS_UFLAG + (u32)((d >> 5) * 64 + lane) * FS;
        u32x2 uf = __builtin_amdgcn_raw_buffer_load_b64(ws, uvo, 0, 16);
        SBAR();
#pragma unroll
        for (int t = 0; t < 3; ++t) lds_st<f32x4>(s0 + t * 1024 + lane * 16, ka[t]);
        sig(mysig, 1);
        wait1(psig, 1);
        u32x4 ur[2];
        {
            const u32 ps0 = lds0 + L_S0 + (bw ^ 1) * S0_SZ;
#pragma unroll
            for (int t = 0; t < 3; ++t) ka[t] += lds_ld<f32x4>(ps0 + t * 1024 + lane * 16);
            sig(mysig, 2);
            lds_st<u32x2>(kc + 2 * (128 * (q4 >> 1) + 4 * (q4 & 1) + 8 * m16), u32x2{pkbf(ka[0][0], ka[0][1]), pkbf(ka[0][2], ka[0][3])});
            const u32 a0 = q4 < 2 ? 256 + 64 * q4 + 4 * m16 : 384 + 64 * (q4 - 2) + 2 * m16, a1 = a0 + (q4 < 2 ? 2 : 32);
            lds_st<u32>(kc + 2 * a0, pkbf(ka[1][0], ka[1][1]));
            lds_st<u32>(kc + 2 * a1, pkbf(ka[1][2], ka[1][3]));
            const u32 w01 = pkbf(ka[2][0], ka[2][1]), w23 = pkbf(ka[2][2], ka[2][3]);
            const u32 a2 = kc + 2 * (512 + (q4 == 3 ? 144 : 64 * q4) + m16);
            lds_st<u16>(a2, (u16)w01); lds_st<u16>(a2 + 32, (u16)(w01 >> 16)); lds_st<u16>(a2 + 64, (u16)w23); lds_st<u16>(a2 + 96, (u16)(w23 >> 16));
            FENCE();
            uvo = ((uf.x ^ ta) | (uf.y ^ tb)) == 0 ? OOB : uvo;
            if (__builtin_amdgcn_ballot_w64(uvo != OOB) != 0) poll_flags(ws, uvo, ta, tb);
            FENCE();
#pragma unroll
            for (int k = 0; k < 2; ++k) ur[k] = __builtin_amdgcn_raw_buffer_load_b128(ws, (u32)(lane >> 1) * 64 + (u32)(lane & 1) * 16 + k * 2048, WS_UT + (u32)(16 * xcd + (idx >> 1)) * 16384 + (u32)(idx & 1) * 32 + (u32)bw * 4096, 16);
            SBAR();
            u32 gv[19];
#pragma unroll
            for (int e = 0; e < 19; ++e) gv[e] = lds_ld<u16>(ga[e]);
            wait1(psig, 2);
#pragma unroll
            for (int k = 0; k < 9; ++k) {
                lds_st<u32>(s0 + 36 * lane + 4 * k, gv[2 * k] | (gv[2 * k + 1] << 16));
                lds_st<u32>(s0 + 2368 + 36 * lane + 4 * k, gv[2 * k + 1] | (gv[2 * k + 2] << 16));
            }
            FENCE();
        }
        STAMP(4);
        wait4(syncA, 1);
#pragma unroll
        for (int k = 0; k < 2; ++k) lds_st<u32x4>(lds0 + L_UB + UB_PAD + (u32)(lane & 1) * UB_SZ + (u32)(lane >> 1) * 16 + (u32)bw * 1024 + k * 512, ur[k]);
        sig(mysig, 3);
        wait4(syncK, 3);
        __builtin_amdgcn_s_setprio(0);
        STAMP(5);
        f32x4 acc[8];
#pragma unroll
        for (int i = 0; i < 8; ++i) acc[i] = f32x4{};
        {
            const u32 bb = ub + 256 - 256 * half + 32 * m16 + 16 * q4;
            const u32 sgl = (u32)(1 - (m16 & 1));
            const u32 ab = s0 + 4 * (592 * sgl + ((111 - m16 - sgl) >> 1) + 4 * q4 - 48);
            bf16x8 bfr[2][8], afr[4];
            auto lda = [&](int n) { const int r = n >> 3, i = n & 7; const u32 o = ab + 4 * (72 * i + 16 * (3 - r));
                u32x4 av = {lds_ld<u32>(o), lds_ld<u32>(o + 4), lds_ld<u32>(o + 8), lds_ld<u32>(o + 12)}; return __builtin_bit_cast(bf16x8, av); };
            afr[0] = lda(0);
#pragma unroll
            for (int m = 0; m < 8; ++m) bfr[0][m] = lds_ld<bf16x8>(bb + 512 * m + 192);
            afr[1] = lda(1); afr[2] = lda(2);
            SBAR();
#pragma unroll
            for (int r = 0; r < 4; ++r) {
                const int cu = r & 1, nx = cu ^ 1;
#pragma unroll
                for (int i = 0; i < 8; ++i) {
                    const int n = 8 * r + i;
                    if (n + 3 < 32) afr[(n + 3) & 3] = lda(n + 3);
                    if (r < 3 && i < 4) { bfr[nx][2 * i] = lds_ld<bf16x8>(bb + 1024 * i + 64 * (2 - r)); bfr[nx][2 * i + 1] = lds_ld<bf16x8>(bb + 1024 * i + 512 + 64 * (2 - r)); }
#pragma unroll
                    for (int cb = i; cb < 8; ++cb) acc[cb] = MFMA16(afr[n & 3], bfr[cu][cb - i], acc[cb]);
                    SBAR();
                }
            }
        }
        FENCE();
        __builtin_amdgcn_s_setprio(3);
        STAMP(6);
        {
            const u32 ps0 = lds0 + L_S0 + (bw ^ 1) * S0_SZ;
            f32x4 kp[4];
            if (half) {
#pragma unroll
                for (int k = 0; k < 4; ++k) { lds_st<f32x4>(s0 + k * 1024 + lane * 16, acc[k]); kp[k] = acc[4 + k]; }
            } else {
#pragma unroll
                for (int k = 0; k < 4; ++k) { lds_st<f32x4>(s0 + k * 1024 + lane * 16, acc[4 + k]); kp[k] = acc[k]; }
            }
            sig(mysig, 4);
            wait1(psig, 4);
#pragma unroll
            for (int k = 0; k < 4; ++k) kp[k] += lds_ld<f32x4>(ps0 + k * 1024 + lane * 16);
            st128<0, true>(wss, u32x4{pkbf(kp[0][0], kp[0][1]), pkbf(kp[0][2], kp[0][3]), pkbf(kp[1][0], kp[1][1]), pkbf(kp[1][2], kp[1][3])}, (u32)lane * 16, WS_YT + (u32)d * 4096 + (u32)half * 2048);
            st128<1024, true>(wss, u32x4{pkbf(kp[2][0], kp[2][1]), pkbf(kp[2][2], kp[2][3]), pkbf(kp[3][0], kp[3][1]), pkbf(kp[3][2], kp[3][3])}, (u32)lane * 16, WS_YT + (u32)d * 4096 + (u32)half * 2048);
            WAITV(0);
            __builtin_amdgcn_raw_buffer_store_b64(u32x2{ta, tb}, ws, lane ? OOB : 0u, WS_YFLAG + (u32)(half * 512 + d) * FS, 16);
        }
        STAMP(7);
    }
    STAMP_OUT();
    while (__builtin_amdgcn_s_memrealtime() - tstart < ENDT) __builtin_amdgcn_s_sleep(4);
}

#if defined(__HIP_DEVICE_COMPILE__)
#pragma clang attribute pop
#endif

extern "C" void kernel_launch(void* const* d_in, const int* in_sizes, int n_in, void* d_out, int out_size, void* d_ws, size_t ws_size, hipStream_t stream) {
    Params p;
    for (int i = 0; i < 18; ++i) p.in[i] = d_in[i];
    p.out = (float*)d_out;
    p.ws = (char*)d_ws;
    (void)hipFuncSetAttribute((const void*)ssm_fused, hipFuncAttributeMaxDynamicSharedMemorySize, LDS_BYTES);
    ssm_fused<<<256, 768, LDS_BYTES, stream>>>(p);
}
```

```cpp
#define ENDT 3000
#define WHO -1
#define OAUX 16
#include <hip/hip_runtime.h>
#include <stdint.h>

typedef unsigned short u16;
typedef unsigned u32;
typedef unsigned long long u64;
typedef short bf16x8 __attribute__((ext_vector_type(8)));
typedef short s16x4 __attribute__((ext_vector_type(4)));
typedef float f32x2 __attribute__((ext_vector_type(2)));
typedef float f32x4 __attribute__((ext_vector_type(4)));
typedef float f32x16 __attribute__((ext_vector_type(16)));
typedef unsigned u32x2 __attribute__((ext_vector_type(2)));
typedef unsigned u32x4 __attribute__((ext_vector_type(4)));
typedef __bf16 bf16x2_t __attribute__((ext_vector_type(2)));
#define LAS __attribute__((address_space(3)))
typedef __amdgpu_buffer_rsrc_t rsrc_t;

#define ABL 0
#define PRA 2
#define PRK 1
#define UM 7
#define DBG1 0
#define LSTAG 5
#define CTHR 0
#define PRL 0
#define WSL 0
#define LWIN 12
#define UFN 6
#define TOUCH 0
#define CPF 1
#define STAMPS 0

constexpr u32 WS_W = 0;
constexpr u32 FS = 128;
constexpr u32 WS_UFLAG = 524288;
constexpr u32 WS_HFLAG = 131072;
constexpr u32 WS_YFLAG = 262144;
constexpr u32 WS_E = 393216;
constexpr u32 WS_UT = 1u << 20;
constexpr u32 WS_YT = 3u << 20;
constexpr u32 WS_H = 5u << 20;
constexpr u32 WS_BYTES = 9u << 20;
constexpr u32 WS_STAMP = 9u << 20;
constexpr u32 OOB = 0x7FFFFF00u;
constexpr u32 L_R2 = 131072;
constexpr u32 L_S0 = L_R2, S0_SZ = 4672;
constexpr u32 L_KC = L_R2 + 4 * S0_SZ, KC_SZ = 1440;
constexpr u32 L_UB = L_KC + 2 * KC_SZ, UB_PAD = 480, UB_SZ = UB_PAD + 4096;
constexpr u32 L_CB1 = L_UB + 2 * UB_SZ;
constexpr u32 L_CB2 = L_CB1 + 768;
constexpr u32 L_SYNC = L_CB2 + 1024;
constexpr u32 LDS_BYTES = 163840;
static_assert(L_SYNC + 64 <= LDS_BYTES, "lds");
constexpr int NWAVES = 12;
#define HWIN 10
#define PSLEEP 1
constexpr u32 SPIN_CAP = 1u << 15;
constexpr u32 CODE_BYTES = 24576;
constexpr u32 CODE_SLICES = 3;
constexpr u32 SENT = 0xFFFFFFFFu;

struct Params { const void* in[18]; float* out; char* ws; };

#if defined(__HIP_DEVICE_COMPILE__)
#pragma clang attribute push(__attribute__((target("no-packed-fp32-ops"))), apply_to = function)
#endif
template <int I> struct ic { static constexpr int v = I; };
template <int B, int E, typename F> __device__ __forceinline__ void sfor(F&& f) { if constexpr (B < E) { f(ic<B>{}); sfor<B + 1, E>(f); } }
#define INL __attribute__((always_inline))
__device__ __forceinline__ u32 umin(u32 a, u32 b) { return a < b ? a : b; }
__device__ __forceinline__ float u2f(u32 v) { return __builtin_bit_cast(float, v); }
__device__ __forceinline__ u32 f2u(float v) { return __builtin_bit_cast(u32, v); }

__device__ __forceinline__ float bf2f(u32 v) { return u2f(v << 16); }
__device__ __forceinline__ u32 pkbf(float lo, float hi) { f32x2 a = {lo, hi}; return __builtin_bit_cast(u32, __builtin_convertvector(a, bf16x2_t)); }
__device__ __forceinline__ float rbf(float x) { return u2f(pkbf(x, x) << 16); }
__device__ __forceinline__ u32x4 mk_srd(const void* p, u32 bytes) { u64 a = (u64)p; u32x4 r = {(u32)a, (u32)(a >> 32) & 0xFFFFu, bytes, 0x00020000u}; return r; }
__device__ __forceinline__ rsrc_t mk_rsrc(const void* p, u32 bytes) { return __builtin_amdgcn_make_buffer_rsrc((void*)p, 0, bytes, 0x00020000); }
template <int IMM, bool SC1> __device__ __forceinline__ void st128(u32x4 srd, u32x4 v, u32 voff, u32 soff) {
    if (SC1) asm volatile("buffer_store_dwordx4 %0, %1, %2, %3 offen offset:%4 sc1\n\ts_nop 1" :: "v"(v), "v"(voff), "s"(srd), "s"(soff), "i"(IMM) : "memory");
    else if (OAUX == 2) asm volatile("buffer_store_dwordx4 %0, %1, %2, %3 offen offset:%4 nt\n\ts_nop 1" :: "v"(v), "v"(voff), "s"(srd), "s"(soff), "i"(IMM) : "memory");
    else if (OAUX == 16) asm volatile("buffer_store_dwordx4 %0, %1, %2, %3 offen offset:%4 sc1\n\ts_nop 1" :: "v"(v), "v"(voff), "s"(srd), "s"(soff), "i"(IMM) : "memory");
    else asm volatile("buffer_store_dwordx4 %0, %1, %2, %3 offen offset:%4\n\ts_nop 1" :: "v"(v), "v"(voff), "s"(srd), "s"(soff), "i"(IMM) : "memory");
}
#define SBAR() __builtin_amdgcn_sched_barrier(0)
#define WAITV(N) asm volatile("s_waitcnt vmcnt(" #N ")" ::: "memory")
#define WAITL() asm volatile("s_waitcnt lgkmcnt(0)" ::: "memory")
#define FENCE() asm volatile("" ::: "memory")
template <typename T> __device__ __forceinline__ T lds_ld(u32 a) { return *(const LAS T*)(uintptr_t)a; }
template <typename T> __device__ __forceinline__ void lds_st(u32 a, T v) { *(LAS T*)(uintptr_t)a = v; }
__device__ __forceinline__ u32x2 lds_tr(u32 a) { return __builtin_bit_cast(u32x2, __builtin_amdgcn_ds_read_tr16_b64_v4i16((LAS s16x4*)(uintptr_t)a)); }
__device__ __forceinline__ void sig(u32 addr, u32 val) { asm volatile("ds_write_b32 %0, %1" :: "v"(addr), "v"(val) : "memory"); }
__device__ __forceinline__ u32 rfl(u32 v) { return (u32)__builtin_amdgcn_readfirstlane(v); }
__device__ __forceinline__ u32 peek4(u32 addr) { u32x4 v; asm volatile("ds_read_b128 %0, %1\n\ts_waitcnt lgkmcnt(0)" : "=v"(v) : "v"(addr) : "memory"); return rfl(umin(umin(v.x, v.y), umin(v.z, v.w))); }
__device__ __forceinline__ u32 peek3(u32 addr) { u32x4 v; asm volatile("ds_read_b128 %0, %1\n\ts_waitcnt lgkmcnt(0)" : "=v"(v) : "v"(addr) : "memory"); return rfl(umin(umin(v.x, v.y), v.z)); }
__device__ __forceinline__ u32 peek1(u32 addr) { u32 v; asm volatile("ds_read_b32 %0, %1\n\ts_waitcnt lgkmcnt(0)" : "=v"(v) : "v"(addr) : "memory"); return rfl(v); }
#define SPINW(cond, slp) do { _Pragma("unroll 1") for (u32 sp_ = 0; (cond) && sp_ < (1u << 22); ++sp_) { if (slp) __builtin_amdgcn_s_sleep(slp); } } while (0)
__device__ __forceinline__ void wait4(u32 addr, u32 val) { SPINW(peek4(addr) < val, WSL); }
__device__ __forceinline__ void wait4s(u32 addr, u32 val) { SPINW(peek4(addr) < val, 2); }
__device__ __forceinline__ void wait3(u32 addr, u32 val) { SPINW(peek3(addr) < val, WSL); }
__device__ __forceinline__ void wait1(u32 addr, u32 val) { SPINW(peek1(addr) < val, WSL); }
__device__ __forceinline__ void wait1s(u32 addr, u32 val) { SPINW(peek1(addr) < val, 2); }
__device__ __forceinline__ u32 get_tag(u32 addr) {
    u32x2 v = {0u, 0u};
#pragma unroll 1
    for (u32 s = 0; s < (1u << 22); ++s) {
        asm volatile("ds_read_b64 %0, %1\n\ts_waitcnt lgkmcnt(0)" : "=v"(v) : "v"(addr) : "memory");
        if (__builtin_amdgcn_readfirstlane(v.y) != 0) break;
    }
    return __builtin_amdgcn_readfirstlane(v.x);
}
__device__ __forceinline__ u32 tag2(u32 t) { return t * 2654435761u + 0x2545F491u; }
typedef f32x2 cf;
__device__ __forceinline__ cf cmul(cf a, cf b) { return cf{__builtin_fmaf(-a.y, b.y, a.x * b.x), __builtin_fmaf(a.y, b.x, a.x * b.y)}; }
__device__ __forceinline__ cf csq(cf a) { return cf{__builtin_fmaf(-a.y, a.y, a.x * a.x), (a.x + a.x) * a.y}; }
__device__ __forceinline__ cf cfma(cf a, cf b, cf c) { return cf{__builtin_fmaf(-a.y, b.y, __builtin_fmaf(a.x, b.x, c.x)), __builtin_fmaf(a.y, b.x, __builtin_fmaf(a.x, b.y, c.y))}; }
__device__ __forceinline__ f32x2 fma2(float a, f32x2 b, f32x2 c) { return f32x2{__builtin_fmaf(a, b.x, c.x), __builtin_fmaf(a, b.y, c.y)}; }
__device__ __forceinline__ u32 pkc(cf a) { return __builtin_bit_cast(u32, __builtin_convertvector(a, bf16x2_t)); }
__device__ __forceinline__ cf cexpk(float zr, float zi, float k) {
    float m = __builtin_amdgcn_exp2f(zr * (k * 1.4426950408889634f));
    float f = __builtin_amdgcn_fractf(zi * (k * 0.15915494309189535f));
    return cf{m * __builtin_amdgcn_cosf(f), m * __builtin_amdgcn_sinf(f)};
}
__device__ __forceinline__ void swap_halves(cf v, cf& lo, cf& hi) {
    auto a = __builtin_amdgcn_permlane32_swap(f2u(v.x), f2u(v.x), false, false);
    auto b = __builtin_amdgcn_permlane32_swap(f2u(v.y), f2u(v.y), false, false);
    lo = cf{u2f(a[0]), u2f(b[0])}; hi = cf{u2f(a[1]), u2f(b[1])};
}
__device__ __forceinline__ void poll_flags(rsrc_t ws, u32 voff, u32 ta, u32 tb) {
#pragma unroll 1
    for (u32 s = 0; s < SPIN_CAP; ++s) {
        FENCE();
        u32x2 v = __builtin_amdgcn_raw_buffer_load_b64(ws, voff, 0, 16);
        voff = ((v.x ^ ta) | (v.y ^ tb)) == 0 ? OOB : voff;
        if (__builtin_amdgcn_ballot_w64(voff != OOB) == 0) break;
        __builtin_amdgcn_s_sleep(PSLEEP);
    }
    FENCE();
}
__device__ __forceinline__ f32x4 sel4(bool c, f32x4 a, f32x4 b) { asm volatile("" : "+v"(a), "+v"(b)); return c ? a : b; }
#define MFMA32(a, b, c) __builtin_amdgcn_mfma_f32_32x32x16_bf16(a, b, c, 0, 0, 0)
#define MFMA16(a, b, c) __builtin_amdgcn_mfma_f32_16x16x32_bf16(a, b, c, 0, 0, 0)

#define STAMP(i) do {} while (0)
#define STAMP_OUT() do {} while (0)

__global__ __launch_bounds__(768, 3) void ssm_fused(Params p) {
    extern __shared__ __attribute__((aligned(16))) char smem[];
    const u32 lds0 = (u32)(uintptr_t)(LAS char*)smem;
    const u32 tid = __builtin_amdgcn_workitem_id_x();
    const int lane = tid & 63;
    const int wv = __builtin_amdgcn_readfirstlane(tid >> 6);
    const int xcd = __builtin_amdgcn_workgroup_id_x() & 7, idx = __builtin_amdgcn_workgroup_id_x() >> 3;
    STAMP(0);
    const u64 tstart = __builtin_amdgcn_s_memrealtime();
    u32 cpf = 0;
    if (CPF && wv == 8) {
        u64 pc0; asm volatile("s_getpc_b64 %0" : "=s"(pc0));
        cpf = __builtin_amdgcn_raw_buffer_load_b32(mk_rsrc((const void*)(pc0 & ~127ull), CODE_BYTES), (u32)lane * 128, (u32)(idx % CODE_SLICES) * 8192, 0);
    }
    sig(lds0 + L_CB1 + umin(tid, 463u) * 4, tid < 448 ? SENT : 0u);
    asm volatile("s_waitcnt lgkmcnt(0)\n\ts_barrier" ::: "memory");
    asm volatile("" :: "s"(p.in[0]), "s"(p.in[1]), "s"(p.in[2]), "s"(p.in[3]), "s"(p.in[4]), "s"(p.in[5]), "s"(p.in[6]), "s"(p.in[7]), "s"(p.in[8]), "s"(p.in[9]),
                 "s"(p.in[10]), "s"(p.in[11]), "s"(p.in[12]), "s"(p.in[13]), "s"(p.in[14]), "s"(p.in[15]), "s"(p.in[16]), "s"(p.in[17]), "s"(p.out), "s"(p.ws));
    const rsrc_t ws = mk_rsrc(p.ws, WS_BYTES);
    const u32x4 wss = mk_srd(p.ws, WS_BYTES);
    const u32 syncL = lds0 + L_SYNC, syncA = syncL + 16, syncK = syncL + 32, tagw = syncL + 48;
    const int rr = lane >> 3, sp = lane & 7;
    const int m16 = lane & 15, q4 = lane >> 4;
    const int ppos = (xcd < 4 ? xcd : (xcd ^ 2)) >> 1;
    const int c = 4 * ppos + (idx & 3), pb = 8 * (xcd & 1) + (idx >> 2);
    const int tb64 = 8 * ppos + 4 * (xcd & 1) + (idx & 3), db = idx >> 2;

    if (wv < 4) {
        const int uw = wv;
        const u32 mysig = syncL + uw * 4;
        {
            __builtin_amdgcn_s_setprio(PRL);
            const rsrc_t rx = mk_rsrc(p.in[0], 2048 * 1024), rbr = mk_rsrc(p.in[3], 512 * 1024), rbi = mk_rsrc(p.in[4], 512 * 1024);
            const rsrc_t rc = mk_rsrc((uw & 2) ? p.in[6] : p.in[5], 512 * 1024);
            const u32 vg = (u32)rr * 4096 + (u32)sp * 16;
            const u32 xs0 = (u32)c * 131072 + (u32)uw * 32768, bs0 = (u32)pb * 32768 + (u32)uw * 1024, cs0 = (u32)db * 65536 + (u32)(uw & 1) * 32768;
            const int r0 = (idx >> 2) + 2 * (idx & 3);
            auto ld = [&](auto ii) INL {
                constexpr int i = decltype(ii)::v;
                if constexpr (i < 48) { constexpr int s = i / 6, j = i % 6; const u32 ch = (u32)((r0 + s) & 7) * 128;
                    if constexpr (j == 0) return __builtin_amdgcn_raw_buffer_load_b128(rbr, vg, bs0 + ch, 0);
                    else if constexpr (j == 1) return __builtin_amdgcn_raw_buffer_load_b128(rbi, vg, bs0 + ch, 0);
                    else return __builtin_amdgcn_raw_buffer_load_b128(rx, vg + (j - 2) * 1024, xs0 + ch, 0); }
                else { constexpr int cg = (0x74321065u >> (4 * ((i - 48) >> 2))) & 15, b = (i - 48) & 3;
                    return __builtin_amdgcn_raw_buffer_load_b128(rc, vg + b * 1024 + cg * 128, cs0, 0); } };
            u32x4 buf[LWIN];
            sfor<0, LWIN>([&](auto ii) INL { buf[decltype(ii)::v] = ld(ii); SBAR(); });
            const u32 w0 = lds0 + (u32)rr * 128 + (u32)((sp ^ (rr >> 1)) * 16);
            u32 wx[2], wxh[2], wb, wbh, wch[2];
#pragma unroll
            for (int k = 0; k < 2; ++k) { wx[k] = (w0 ^ (k * 64)) + uw * 4096; wxh[k] = wx[k] + 65536; wch[k] = (wx[k] ^ 4096) + 65536; }
            wb = (w0 ^ ((uw & 1) * 64)) + uw * 1024; wbh = wb + 65536;
            u32 msg = mysig, vsa = syncA;
            asm volatile("" : "+v"(wx[0]), "+v"(wx[1]), "+v"(wxh[0]), "+v"(wxh[1]), "+v"(wch[0]), "+v"(wch[1]), "+v"(wb), "+v"(wbh), "+v"(msg), "+v"(vsa));
            STAMP(1);
            sfor<0, 80>([&](auto ii) INL {
                constexpr int i = decltype(ii)::v;
                u32 ad;
                if constexpr (i < 48) { constexpr int s = i / 6, j = i % 6, o = (s % 5) * 24576;
                    if constexpr (j == 0 && s >= 5) wait4(vsa, s - 4);
                    if constexpr (j < 2) { constexpr int ob = o + 16384 + j * 4096; ad = ob < 65536 ? wb + ob : wbh + (ob - 65536); }
                    else { constexpr int ox = o + (j - 2) * 1024; ad = ox < 65536 ? wx[j & 1] + ox : wxh[j & 1] + (ox - 65536); }
                } else { constexpr int q = (i - 48) >> 2, cg = (0x74321065u >> (4 * q)) & 15, need = (0x98877654u >> (4 * q)) & 15, pneed = (0x88776540u >> (4 * q)) & 15, b = (i - 48) & 3;
                    if constexpr (b == 0 && need != pneed) wait4s(vsa, need);
                    ad = (cg < 4 ? wx[b & 1] : wch[b & 1]) + (cg & 3) * 16384 + b * 1024;
                }
                lds_st<u32x4>(ad, buf[i % LWIN]);
                SBAR();
                if constexpr (i + LWIN < 80) { buf[i % LWIN] = ld(ic<i + LWIN>{}); SBAR(); }
                if constexpr (i < 48 && i % 6 == 5) { sig(msg, i / 6 + 1); SBAR(); }
                if constexpr (i == 5) STAMP(2);
                if constexpr (i == 47) { STAMP(3); __builtin_amdgcn_s_setprio(0); }
            });
            sig(mysig, 9);
            STAMP(5);
        }
    }
    int wv2 = wv;
    asm volatile("" : "+s"(wv2));
    if (wv2 < 4) {
        const int uw = wv;
        const rsrc_t ru = mk_rsrc(p.in[9], 2048 * 1024);
        __builtin_amdgcn_s_setprio(1);
        const int dsel = uw >> 1, half = uw & 1;
        const u32 im = lds0 + L_UB + dsel * UB_SZ + UB_PAD + half * 2048;
        const int dd = 64 * xcd + half * 32;
        const u32 ta = get_tag(tagw), tb = tag2(ta);
        const int sel = 2 * idx + dsel, prr = sel >> 4;
        const u32 trow = (u32)(256 * ((m16 >> 2) & 1) + 16 * (m16 >> 3) + (m16 & 3)), tbase = (u32)(512 * prr + 64 * (sel & 3) + 4 * ((sel >> 2) & 3));
        f32x4 sk[4];
        {
            u32x2 uu[4], dk[2];
#pragma unroll
            for (int k = 0; k < 4; ++k) uu[k] = __builtin_amdgcn_raw_buffer_load_b64(ru, trow * 1024 + (u32)q4 * 8 + (k & 1) * 32, (tbase + 32 * (k >> 1)) * 1024 + (u32)dd * 2, 0);
#pragma unroll
            for (int k = 0; k < 2; ++k) dk[k] = __builtin_amdgcn_raw_buffer_load_b64(mk_rsrc(p.in[17], 1024), (u32)q4 * 8 + k * 32, (u32)dd * 2, 0);
#pragma unroll
            for (int k = 0; k < 4; ++k) {
                const u32x2 a = uu[k], b = dk[k & 1];
                const u32 w0 = pkbf(u2f(a.x << 16) * u2f(b.x << 16), u2f(a.x & 0xFFFF0000u) * u2f(b.x & 0xFFFF0000u));
                const u32 w1 = pkbf(u2f(a.y << 16) * u2f(b.y << 16), u2f(a.y & 0xFFFF0000u) * u2f(b.y & 0xFFFF0000u));
                sk[k] = f32x4{u2f(w0 << 16), u2f(w0 & 0xFFFF0000u), u2f(w1 << 16), u2f(w1 & 0xFFFF0000u)};
                asm volatile("" : "+v"(sk[k]));
            }
        }
        STAMP(6);
        if (ABL == 0) {
            wait4s(syncK, 4);
            STAMP(7);
            poll_flags(ws, lane < 32 ? WS_YFLAG + (u32)((prr >> 1) * 512 + dd + lane) * FS : OOB, ta, tb);
            __builtin_amdgcn_s_setprio(3);
            STAMP(8);
            const u32 so = WS_YT + (u32)dd * 4096 + (u32)sel * 64;
            u32x4 a = __builtin_amdgcn_raw_buffer_load_b128(ws, (u32)(lane >> 2) * 4096 + (u32)(lane & 3) * 16, so, 16);
            u32x4 b = __builtin_amdgcn_raw_buffer_load_b128(ws, (u32)(lane >> 2) * 4096 + (u32)(lane & 3) * 16, so + 65536, 16);
            lds_st<u32x4>(im + lane * 16, a);
            lds_st<u32x4>(im + 1024 + lane * 16, b);
            FENCE();
            const u32x4 ro = mk_srd(p.out, 2048 * 4096);
            const u32 ra = im + (u32)(4 * q4 + (m16 >> 2)) * 64 + (u32)(lane & 3) * 8;
#pragma unroll
            for (int k = 0; k < 4; ++k) {
                u32x2 v = lds_tr(ra + (k & 1) * 1024 + (k >> 1) * 32);
                f32x4 f = f32x4{u2f(v.x << 16), u2f(v.x & 0xFFFF0000u), u2f(v.y << 16), u2f(v.y & 0xFFFF0000u)} + sk[k];
                const u32 w0 = pkbf(f[0], f[1]), w1 = pkbf(f[2], f[3]);
                st128<0, false>(ro, u32x4{w0 << 16, w0 & 0xFFFF0000u, w1 << 16, w1 & 0xFFFF0000u}, trow * 4096 + (u32)q4 * 16, (tbase + 32 * (k >> 1)) * 4096 + (u32)(512 + dd) * 4 + (k & 1) * 64);
            }
        }
        STAMP(9);
    } else if (__builtin_expect(wv < 8, 1)) {
        const int aw = wv - 4;
        __builtin_amdgcn_s_setprio(PRA);
        const int r32 = lane & 31, hh = lane >> 5;
        const int g32 = 4 * (r32 & 7) + (r32 >> 3);
        const u32 q_lr = __builtin_amdgcn_raw_buffer_load_b16(mk_rsrc(p.in[1], 1024), (u32)g32 * 2, (u32)pb * 64, 0);
        const u32 q_li = __builtin_amdgcn_raw_buffer_load_b16(mk_rsrc(p.in[2], 1024), (u32)g32 * 2, (u32)pb * 64, 0);
        const u32 q_ld = __builtin_amdgcn_raw_buffer_load_b16(mk_rsrc(p.in[7], 1024), (u32)g32 * 2, (u32)pb * 64, 0);
        const u32 wv0 = __builtin_amdgcn_raw_buffer_load_b32(ws, 0, WS_W, 16);
        u32x4 ut[2];
#pragma unroll
        for (int k = 0; k < 2; ++k) ut[k] = __builtin_amdgcn_raw_buffer_load_b128(mk_rsrc(p.in[9], 2048 * 1024), (u32)(lane >> 2) * 1024 + (u32)(lane & 3) * 16, (u32)(idx * 64 + (aw >> 1) * 32 + 16 * k) * 1024 + (u32)xcd * 128 + (u32)(aw & 1) * 64, 0);
        SBAR();
        u32 fo[4], xo[4], fh[4], xh[4];
#pragma unroll
        for (int k = 0; k < 4; ++k) { fo[k] = lds0 + (u32)r32 * 128 + (u32)(((2 * k + hh) ^ ((r32 >> 1) & 7)) * 16); xo[k] = fo[k] + aw * 4096; fh[k] = fo[k] + 65536; xh[k] = xo[k] + 65536; }
#pragma unroll
        for (int k = 0; k < 4; ++k) asm volatile("" : "+v"(fo[k]), "+v"(xo[k]), "+v"(fh[k]), "+v"(xh[k]));
        u32 mysig = syncA + aw * 4, vsl = syncL, c1r = lds0 + L_CB1 + r32 * 8, c2r = lds0 + L_CB2 + r32 * 32, l16 = (u32)lane * 16;
        u32 c1w = c1r + aw * 256, c2w = c2r + aw * 8, evo = hh ? OOB : (u32)r32 * 16;
        u32 vo[2];
        {
            const int cb0 = c - 1 - hh - 4 * aw;
            const u32 vo0 = WS_E + (u32)(cb0 * 512 + pb * 32 + r32) * 16;
            vo[0] = cb0 >= 0 ? vo0 : OOB; vo[1] = cb0 >= 2 ? vo0 - 16384 : OOB;
        }
        asm volatile("" : "+v"(mysig), "+v"(vsl), "+v"(c1r), "+v"(c2r), "+v"(c1w), "+v"(c2w), "+v"(evo), "+v"(vo[0]), "+v"(vo[1]), "+v"(l16));
        STAMP(1);
        cf P[4], L8, L16, L32, W0, W1, coef, Q;
        {
            const float a_lr = bf2f(q_lr), a_li = bf2f(q_li);
            const float dt = rbf(__builtin_expf(bf2f(q_ld)));
            P[0] = cexpk(dt * a_lr, dt * a_li, 1.f);
            P[1] = csq(P[0]); P[2] = cmul(P[0], P[1]); P[3] = csq(P[1]);
            L8 = csq(P[3]); L16 = csq(L8); L32 = csq(L16);
            const cf L64 = csq(L32), L128 = csq(L64), L256 = csq(L128), L512 = csq(L256);
            W0 = hh ? L128 : cf{1.f, 0.f};
            if (aw & 1) W0 = cmul(W0, L512);
            if (aw & 2) W0 = cmul(W0, csq(L512));
            W1 = cmul(W0, L256);
            const float nr = P[0].x - 1.f, ni = P[0].y, inv = 2.f / (a_lr * a_lr + a_li * a_li);
            coef = cf{(nr * a_lr + ni * a_li) * inv, (ni * a_lr - nr * a_li) * inv};
            Q = hh ? cmul(coef, L16) : coef;
            if (aw & 1) Q = cmul(Q, L32);
            if (aw & 2) Q = cmul(Q, L64);
            asm volatile("" : "+v"(P[0]), "+v"(P[1]), "+v"(P[2]), "+v"(P[3]), "+v"(L8), "+v"(L16), "+v"(L32), "+v"(W0), "+v"(W1), "+v"(coef), "+v"(Q));
        }
        const u32 ta = rfl(wv0) + 1u, tb = tag2(ta);
        {
            if (aw == 0) lds_st<u32x2>(tagw, u32x2{ta, 1u});
            const u32 im = lds0 + L_UB + UB_PAD + (aw >> 1) * UB_SZ + (aw & 1) * 2048;
            lds_st<u32x4>(im + l16, ut[0]);
            lds_st<u32x4>(im + 1024 + l16, ut[1]);
            FENCE();
            const u32 tra = im + (u32)(8 * (lane & 3) + (m16 >> 2)) * 64 + (u32)q4 * 8;
#pragma unroll
            for (int k = 0; k < 2; ++k) { const u32x2 x = lds_tr(tra + k * 32), y = lds_tr(tra + k * 32 + 256); ut[k] = u32x4{x.x, x.y, y.x, y.y}; }
#pragma unroll
            for (int k = 0; k < 2; ++k) st128<0, true>(wss, ut[k], (u32)q4 * 16384 + (u32)m16 * 16, WS_UT + (u32)(16 * xcd + 8 * (aw & 1) + 4 * k) * 16384 + (u32)(2 * idx + (aw >> 1)) * 256);
        }
        u32 fvo = lane ? OOB : 0u;
        u32x2 tg = {ta, tb};
        asm volatile("" : "+v"(fvo), "+v"(tg));
        STAMP(2);
        f32x16 accR = {}, accI = {};
        {
            bf16x8 fa[4], fr[4], fi[4];
            auto rd = [&](int n) { const int s = n >> 2, k = n & 3, o = (s % 5) * 24576, ob = o + 16384;
                fa[n & 3] = lds_ld<bf16x8>(o < 65536 ? xo[k] + o : xh[k] + (o - 65536));
                fr[n & 3] = lds_ld<bf16x8>(ob < 65536 - 4096 ? fo[k] + ob : fh[k] + (ob - 65536));
                fi[n & 3] = lds_ld<bf16x8>(ob < 65536 - 4096 ? fo[k] + ob + 4096 : fh[k] + (ob + 4096 - 65536)); };
            wait4(syncL, 1);
            STAMP(10);
            u32x4 fl = *(volatile const LAS u32x4*)(uintptr_t)vsl;
            rd(0); rd(1); rd(2);
            SBAR();
#pragma unroll
            for (int n = 0; n < 32; ++n) {
                const int s = n >> 2, k = n & 3;
                if (k == 1 && s < 7) {
                    if (rfl(umin(umin(fl.x, fl.y), umin(fl.z, fl.w))) < (u32)(s + 2)) wait4(syncL, s + 2);
                    FENCE();
                }
                if (k == 2 && s < 6) fl = *(volatile const LAS u32x4*)(uintptr_t)vsl;
                accR = MFMA32(fa[k], fr[k], accR);
                SBAR();
                if (n + 3 < 32) rd(n + 3);
                if (k == 0) sig(mysig, s + 1);
                if (n == UFN) { WAITV(0); __builtin_amdgcn_raw_buffer_store_b64(tg, ws, fvo, WS_UFLAG + (u32)((2 * xcd + (aw & 1)) * 64 + 2 * idx + (aw >> 1)) * FS, 16); FENCE(); }
                accI = MFMA32(fa[k], fi[k], accI);
                SBAR();
            }
        }
        __builtin_amdgcn_s_setprio(3);
        STAMP(3);
        f32x2 hr[8], hi[8];
#pragma unroll
        for (int i = 0; i < 8; ++i) { hr[i] = f32x2{accR[2 * i], accR[2 * i + 1]}; hi[i] = f32x2{accI[2 * i], accI[2 * i + 1]}; }
#pragma unroll
        for (int i = 2; i < 8; ++i) {
            f32x2 nr = fma2(-P[0].y, hi[i - 2], fma2(P[0].x, hr[i - 2], hr[i]));
            f32x2 ni = fma2(P[0].y, hr[i - 2], fma2(P[0].x, hi[i - 2], hi[i]));
            hr[i] = nr; hi[i] = ni;
        }
        cf e0, e1, pre = {0.f, 0.f};
        {
            const f32x2 zr = fma2(-L8.y, hi[6], fma2(L8.x, hr[6], hr[7])), zi = fma2(L8.y, hr[6], fma2(L8.x, hi[6], hi[7]));
            const cf e16 = {__builtin_fmaf(-P[3].y, zi.x, __builtin_fmaf(P[3].x, zr.x, zr.y)), __builtin_fmaf(P[3].y, zr.x, __builtin_fmaf(P[3].x, zi.x, zi.y))};
            swap_halves(e16, e0, e1);
        }
        const cf e32 = cfma(e0, L16, e1);
        if (aw < 3) { lds_st<f32x2>(c1w, e32); FENCE(); }
        u32x4 g[2];
        if (aw < 3) {
#pragma unroll
            for (int k = 0; k < 2; ++k) g[k] = __builtin_amdgcn_raw_buffer_load_b128(ws, vo[k], 0, 16);
        }
        if (aw > 0) {
            cf w0, w1, w2;
#pragma unroll 1
            for (u32 spn = 0; spn < (1u << 22); ++spn) {
                asm volatile("ds_read_b64 %0, %3\n\tds_read_b64 %1, %3 offset:256\n\tds_read_b64 %2, %3 offset:512\n\ts_waitcnt lgkmcnt(0)" : "=&v"(w0), "=&v"(w1), "=&v"(w2) : "v"(c1r) : "memory");
                const u32 a = f2u(w0.x), b = f2u(w1.x), cc = f2u(w2.x);
                const u32 mx = a > b ? (a > cc ? a : cc) : (b > cc ? b : cc);
                if (__builtin_amdgcn_ballot_w64(mx == SENT) == 0) break;
            }
            const cf p2 = cfma(w0, L32, w1), p3 = cfma(p2, L32, w2);
            pre = aw == 1 ? w0 : (aw == 2 ? p2 : p3);
            if (aw == 3) {
                if (c < 15) { const cf E = cfma(p3, L32, e32); st128<0, true>(wss, u32x4{f2u(E.x), ta, f2u(E.y), tb}, evo, WS_E + (u32)(c * 512 + pb * 32) * 16); }
#pragma unroll
                for (int k = 0; k < 2; ++k) g[k] = __builtin_amdgcn_raw_buffer_load_b128(ws, vo[k], 0, 16);
            }
        }
        STAMP(4);
        const int th = aw & 1, kh = aw >> 1;
        const int t0 = tb64 * 64 + th * 32, dcol = db * 64 + kh * 32, t32 = tb64 * 2 + th;
        u32 xk[16];
        {
            const rsrc_t rx = mk_rsrc(p.in[0], 2048 * 1024);
#pragma unroll
            for (int r = 0; r < 16; ++r)
                xk[r] = __builtin_amdgcn_raw_buffer_load_b16(rx, (u32)hh * 4096 + (u32)g32 * 2 + (r & 3) * 1024, (u32)t0 * 1024 + (u32)dcol * 2 + (r >> 2) * 8192, 0);
        }
        const u32 dsk = __builtin_amdgcn_raw_buffer_load_b16(mk_rsrc(p.in[8], 1024), (u32)g32 * 2, (u32)dcol * 2, 0);
        SBAR();
        f32x2 cpr[2], cpi[2], cqr[2], cqi[2];
        {
            { const cf pre1 = cfma(pre, L16, e0); pre = hh ? pre1 : pre; }
            cf s[4], q[4];
            s[0] = pre; q[0] = Q;
            s[1] = cfma(s[0], P[3], cf{hr[6].x, hi[6].x}); s[2] = cfma(s[1], P[3], cf{hr[6].y, hi[6].y}); s[3] = cfma(s[2], P[3], cf{hr[7].x, hi[7].x});
#pragma unroll
            for (int a = 1; a < 4; ++a) q[a] = cmul(q[a - 1], P[3]);
#pragma unroll
            for (int a = 0; a < 4; ++a) s[a] = cmul(s[a], coef);
#pragma unroll
            for (int u = 0; u < 2; ++u) { cpr[u] = f32x2{s[2 * u].x, s[2 * u + 1].x}; cpi[u] = f32x2{s[2 * u].y, s[2 * u + 1].y}; cqr[u] = f32x2{q[2 * u].x, q[2 * u + 1].x}; cqi[u] = f32x2{q[2 * u].y, q[2 * u + 1].y};
                asm volatile("" : "+v"(cpr[u]), "+v"(cpi[u]), "+v"(cqr[u]), "+v"(cqi[u])); }
        }
#pragma unroll
        for (int i = 0; i < 8; ++i) {
            f32x2 nr = fma2(-coef.y, hi[i], coef.x * hr[i]);
            f32x2 ni = fma2(-coef.y, hr[i], -coef.x * hi[i]);
            hr[i] = nr; hi[i] = ni;
            asm volatile("" : "+v"(hr[i]), "+v"(hi[i]));
        }
        const u32 scr = lds0 + 7 * 16384 + aw * 4096;
        u32 wa0 = scr + (u32)(r32 ^ ((r32 >> 3) & 3)) * 64 + (u32)(((2 * hh) ^ ((r32 >> 1) & 3)) * 16), wa1 = wa0 ^ 16;
        u32 ra[4], pvo = lane < 8 ? WS_HFLAG + (u32)(t32 * 16 + kh * 8 + lane) * FS : OOB;
        { const int q = m16 >> 2, pp = lane & 3;
          const u32 b0 = scr + (u32)(8 * q + ((2 * hh) ^ q)) * 64 + (u32)(((2 * (q4 & 1) + (pp >> 1)) ^ hh) * 16) + (u32)(pp & 1) * 8;
          ra[0] = b0; ra[1] = b0 ^ 64; ra[2] = (b0 ^ 32) + 256; ra[3] = (b0 ^ 96) + 256; }
        asm volatile("" : "+v"(ra[0]), "+v"(ra[1]), "+v"(ra[2]), "+v"(ra[3]), "+v"(wa0), "+v"(wa1), "+v"(pvo));
        SBAR();
        cf ev[2] = {cf{0.f, 0.f}, cf{0.f, 0.f}};
#pragma unroll 1
        for (u32 spn = 0; spn < SPIN_CAP; ++spn) {
#pragma unroll
            for (int k = 0; k < 2; ++k) {
                const bool good = ((g[k].y ^ ta) | (g[k].w ^ tb)) == 0;
                ev[k].x = good ? u2f(g[k].x) : ev[k].x; ev[k].y = good ? u2f(g[k].z) : ev[k].y;
                vo[k] = good ? OOB : vo[k];
            }
            if (__builtin_amdgcn_ballot_w64(umin(vo[0], vo[1]) != OOB) == 0) break;
            FENCE();
#pragma unroll
            for (int k = 0; k < 2; ++k) g[k] = __builtin_amdgcn_raw_buffer_load_b128(ws, vo[k], 0, 16);
        }
        {
            cf G0, G1;
            swap_halves(cfma(ev[1], W1, cmul(ev[0], W0)), G0, G1);
            lds_st<f32x2>(c2w, G0 + G1);
            f32x4 h01, h23;
#pragma unroll 1
            for (u32 spn = 0; spn < (1u << 22); ++spn) {
                asm volatile("ds_read_b128 %0, %2\n\tds_read_b128 %1, %2 offset:16\n\ts_waitcnt lgkmcnt(0)" : "=&v"(h01), "=&v"(h23) : "v"(c2r) : "memory");
                const u32 a = f2u(h01[0]), b = f2u(h01[2]), cc = f2u(h23[0]), dd = f2u(h23[2]);
                const u32 m1 = a > b ? a : b, m2 = cc > dd ? cc : dd;
                if (__builtin_amdgcn_ballot_w64((m1 > m2 ? m1 : m2) == SENT) == 0) break;
            }
            STAMP(5);
            const f32x4 hs4 = h01 + h23;
            const cf H = {hs4[0] + hs4[2], hs4[1] + hs4[3]};
            f32x2 gr[2], gi[2];
#pragma unroll
            for (int u = 0; u < 2; ++u) { gr[u] = fma2(-H.y, cqi[u], fma2(H.x, cqr[u], cpr[u])); gi[u] = fma2(H.x, cqi[u], fma2(H.y, cqr[u], cpi[u])); }
#pragma unroll
            for (int i = 0; i < 8; ++i) hr[i] = fma2(-P[i >> 1].y, gi[i & 1], fma2(P[i >> 1].x, gr[i & 1], hr[i]));
            lds_st<u32x4>(wa0, u32x4{pkbf(hr[0].x, hr[2].x), pkbf(hr[4].x, hr[6].x), pkbf(hr[0].y, hr[2].y), pkbf(hr[4].y, hr[6].y)});
            lds_st<u32x4>(wa1, u32x4{pkbf(hr[1].x, hr[3].x), pkbf(hr[5].x, hr[7].x), pkbf(hr[1].y, hr[3].y), pkbf(hr[5].y, hr[7].y)});
            FENCE();
            u32x2 tv[8];
#pragma unroll
            for (int s = 0; s < 4; ++s) tv[s] = lds_tr(ra[s]);
            SBAR();
#pragma unroll
            for (int i = 0; i < 8; ++i) hi[i] = fma2(-P[i >> 1].y, gr[i & 1], fma2(-P[i >> 1].x, gi[i & 1], hi[i]));
            lds_st<u32x4>(wa0 + 2048, u32x4{pkbf(hi[0].x, hi[2].x), pkbf(hi[4].x, hi[6].x), pkbf(hi[0].y, hi[2].y), pkbf(hi[4].y, hi[6].y)});
            lds_st<u32x4>(wa1 + 2048, u32x4{pkbf(hi[1].x, hi[3].x), pkbf(hi[5].x, hi[7].x), pkbf(hi[1].y, hi[3].y), pkbf(hi[5].y, hi[7].y)});
            FENCE();
#pragma unroll
            for (int s = 0; s < 4; ++s) tv[4 + s] = lds_tr(ra[s] + 2048);
            const u32 hs = WS_H + (u32)((c * 4 + aw) * 16 + pb) * 4096;
            st128<0, true>(wss, u32x4{tv[0].x, tv[0].y, tv[1].x, tv[1].y}, l16, hs);
            st128<1024, true>(wss, u32x4{tv[2].x, tv[2].y, tv[3].x, tv[3].y}, l16, hs);
            st128<2048, true>(wss, u32x4{tv[4].x, tv[4].y, tv[5].x, tv[5].y}, l16, hs);
            st128<3072, true>(wss, u32x4{tv[6].x, tv[6].y, tv[7].x, tv[7].y}, l16, hs);
            sig(mysig, 9);
        }
        f32x16 y0, y1 = {};
        {
            const float dk = bf2f(dsk);
#pragma unroll
            for (int r = 0; r < 16; r += 2) { const u32 w = pkbf(bf2f(xk[r]) * dk, bf2f(xk[r + 1]) * dk); y0[r] = u2f(w << 16); y0[r + 1] = u2f(w & 0xFFFF0000u); }
            asm volatile("" : "+v"(y0));
        }
        WAITV(0);
        __builtin_amdgcn_raw_buffer_store_b64(tg, ws, fvo, WS_HFLAG + (u32)((c * 4 + aw) * 16 + pb) * FS, 16);
        FENCE();
        STAMP(6);
        u32 ca[4], ov[4];
#pragma unroll
        for (int k = 0; k < 4; ++k) { ca[k] = fo[k] + (u32)kh * 65536; ov[k] = (u32)hh * 16384 + (u32)g32 * 4 + k * 4096; asm volatile("" : "+v"(ca[k]), "+v"(ov[k])); }
        u32 xb = lds0 + (u32)(4 * kh + (db & 3)) * 16384 + (u32)th * 4096 + l16;
        asm volatile("" : "+v"(xb));
        wait4(syncL, 9);
        bf16x8 cb0[4], cb1[4];
        auto cad = [&](int f) { const int jj = f >> 3, a = (f >> 2) & 1, s = f & 3;
            return ca[2 * a + (s & 1)] + (u32)((jj + db) & 3) * 16384 + (s >> 1) * 8192; };
#pragma unroll
        for (int f = 0; f < 3; ++f) { const u32 ad = cad(f); cb0[f] = lds_ld<bf16x8>(ad); cb1[f] = lds_ld<bf16x8>(ad + 4096); }
        poll_flags(ws, pvo, ta, tb);
        STAMP(7);
        {
            u32x4 hf[16];
            auto ldh = [&](int f) { const u32 cg = (u32)(4 * kh + (((f >> 3) + db) & 3));
                return __builtin_amdgcn_raw_buffer_load_b128(ws, l16 + (f & 3) * 1024, WS_H + (u32)t32 * 65536 + cg * 8192 + ((f >> 2) & 1) * 4096, 16); };
#pragma unroll
            for (int f = 0; f < HWIN; ++f) { hf[f] = ldh(f); SBAR(); }
#pragma unroll
            for (int f = 0; f < 32; ++f) {
                bf16x8 av = __builtin_bit_cast(bf16x8, hf[f & 15]);
                y0 = MFMA32(av, cb0[f & 3], y0);
                SBAR();
                if (f + 3 < 32) { const u32 ad = cad(f + 3); cb0[(f + 3) & 3] = lds_ld<bf16x8>(ad); cb1[(f + 3) & 3] = lds_ld<bf16x8>(ad + 4096); }
                y1 = MFMA32(av, cb1[f & 3], y1);
                SBAR();
                if (f + HWIN < 32) { hf[(f + HWIN) & 15] = ldh(f + HWIN); SBAR(); }
                if (f == 8) { sig(mysig, 10); SBAR(); }
                if (f == 28) { wait4(syncA, 10); SBAR(); }
            }
        }
        STAMP(8);
        {
#pragma unroll
            for (int k = 0; k < 4; ++k) lds_st<f32x4>(xb + k * 1024, f32x4{y1[4 * k], y1[4 * k + 1], y1[4 * k + 2], y1[4 * k + 3]});
            sig(mysig, 11);
            wait1(syncA + (aw ^ 2) * 4, 11);
            const rsrc_t ro = mk_rsrc(p.out, 2048 * 4096);
            f32x4 o[4];
#pragma unroll
            for (int k = 0; k < 4; ++k) o[k] = lds_ld<f32x4>((xb ^ 65536) + k * 1024);
#pragma unroll
            for (int k = 0; k < 4; ++k) {
#pragma unroll
                for (int j = 0; j < 4; ++j)
                    __builtin_amdgcn_raw_buffer_store_b32(f2u(y0[4 * k + j] + o[k][j]), ro, ov[j], (u32)(t0 + 8 * k) * 4096 + (u32)dcol * 4, OAUX);
            }
            if (tb64 == 31 && db == 0 && aw == 0) __builtin_amdgcn_raw_buffer_store_b32(ta, ws, fvo, WS_W, 16);
        }
        STAMP(9);
    } else {
        const int bw = wv - 8, dsel = bw >> 1, half = bw & 1;
        if (ABL >= 1) return;
        __builtin_amdgcn_s_setprio(PRK);
        const int d = 64 * xcd + 2 * idx + dsel;
        const u32 s0 = lds0 + L_S0 + bw * S0_SZ, kc = lds0 + L_KC + dsel * KC_SZ, ub = lds0 + L_UB + dsel * UB_SZ;
        const u32 mysig = syncK + bw * 4, psig = syncK + (bw ^ 1) * 4;
        u32 pr[6];
#pragma unroll
        for (int i = 0; i < 6; ++i) pr[i] = __builtin_amdgcn_raw_buffer_load_b16(mk_rsrc(p.in[10 + i], 512 * 256), (u32)lane * 2, (u32)(d * 128 + half * 64) * 2, 0);
        const u32 q_ld = __builtin_amdgcn_raw_buffer_load_b16(mk_rsrc(p.in[16], 1024), 0, (u32)d * 2, 0);
        SBAR();
        u32 tch = 0, tch2 = 0;
        if (TOUCH) {
            const rsrc_t ro = mk_rsrc(p.out, 2048 * 4096);
            if (bw == 0) {
                tch = __builtin_amdgcn_raw_buffer_load_b32(ro, (u32)lane * 4096, (u32)tb64 * 262144, 0);
                tch2 = __builtin_amdgcn_raw_buffer_load_b32(ws, (u32)lane * 4096, WS_YT + (u32)xcd * 262144, 16);
            } else if (bw == 1) {
                const int sel = 2 * idx + (lane >> 5), i16 = lane & 15;
                const u32 row = (u32)(512 * (sel >> 4) + 64 * (sel & 3) + 4 * ((sel >> 2) & 3) + 256 * ((i16 >> 2) & 1) + 16 * (i16 >> 3) + (i16 & 3) + 32 * ((lane >> 4) & 1));
                tch = __builtin_amdgcn_raw_buffer_load_b32(ro, row * 4096, 0, 0);
                tch2 = __builtin_amdgcn_raw_buffer_load_b32(ws, (u32)lane * 4096, WS_UT + (u32)xcd * 262144, 16);
            } else if (bw == 2) {
                const u32 a = lane < 32 ? WS_H + (u32)tb64 * 131072 + (u32)lane * 4096 : lane < 36 ? WS_H + (u32)((c * 4 + lane - 32) * 16 + pb) * 4096 : lane < 52 ? WS_E + (u32)((lane - 36) * 512 + pb * 32) * 16
                            : lane < 54 ? WS_HFLAG + (u32)(lane == 52 ? tb64 * 2 : c * 4) * 2048 : lane < 56 ? WS_HFLAG + (u32)(c * 4 + 2) * 2048 : lane < 60 ? WS_UFLAG + (u32)xcd * 16384 + (u32)(lane - 56) * 4096 : WS_YFLAG + (u32)((lane >> 1) & 1) * 65536 + (u32)xcd * 8192 + (u32)(lane & 1) * 4096;
                tch = __builtin_amdgcn_raw_buffer_load_b32(ws, a, 0, 16);
                tch2 = __builtin_amdgcn_raw_buffer_load_b32(mk_rsrc(p.in[8], 1024), 0, 0, 0) + __builtin_amdgcn_raw_buffer_load_b32(mk_rsrc(p.in[17], 1024), 0, 0, 0);
            } else {
                tch = __builtin_amdgcn_raw_buffer_load_b32(mk_rsrc(p.in[5], 512 * 1024), (u32)(lane & 15) * 4096, (u32)db * 65536, 0);
                tch2 = __builtin_amdgcn_raw_buffer_load_b32(mk_rsrc(p.in[6], 512 * 1024), (u32)(lane & 15) * 4096, (u32)db * 65536, 0);
            }
        }
        SBAR();
        lds_st<u32x2>(ub + umin(lane, 59) * 8, u32x2{0u, 0u});
        u32 ga[19];
        {
            const int lag0 = 256 * rr + 128 * half + 127 - 18 * sp;
#pragma unroll
            for (int e = 0; e < 19; ++e) {
                const int lag = lag0 - e;
                const u32 bits = pkbf((float)lag, 0.f) & 0xFFFFu;
                const u32 ix = lag < 128 ? (u32)lag : bits - (0x4300u - 128u);
                ga[e] = lag < 0 ? ub : kc + 2 * ix;
                asm volatile("" : "+v"(ga[e]));
            }
        }
        asm volatile("" :: "v"(cpf));
        STAMP(1);
        f32x4 ka[3];
        {
            const float dt = rbf(__builtin_expf(bf2f(q_ld)));
            const float zr = dt * bf2f(pr[0]), zi = dt * bf2f(pr[1]);
            const cf db_ = {dt * bf2f(pr[2]), -dt * bf2f(pr[3])}, cc = {bf2f(pr[4]), -bf2f(pr[5])};
            const cf E1 = cexpk(zr, zi, 1.f), E8c = cexpk(zr, -zi, 8.f), E128 = cexpk(zr, zi, 128.f);
            const u32 wa = s0 + lane * 4, ra = s0 + (u32)m16 * 288 + (u32)q4 * 16;
            cf V[16], R[8], W[17];
            { const cf E16 = cmul(E8c, E8c), E32 = cmul(E16, E16), E64 = cmul(E32, E32);
              V[0] = cmul(cc, db_); V[1] = cmul(V[0], E8c);
#pragma unroll
              for (int j = 0; j < 2; ++j) V[2 + j] = cmul(V[j], E16);
#pragma unroll
              for (int j = 0; j < 4; ++j) V[4 + j] = cmul(V[j], E32);
#pragma unroll
              for (int j = 0; j < 8; ++j) V[8 + j] = cmul(V[j], E64); }
            R[0] = cf{1.f, 0.f}; R[1] = E1; R[2] = cmul(E1, E1); R[3] = cmul(R[1], R[2]); R[4] = cmul(R[2], R[2]);
#pragma unroll
            for (int r = 1; r < 4; ++r) R[4 + r] = cmul(R[r], R[4]);
            W[1] = E128; W[2] = cmul(W[1], W[1]); W[3] = cmul(W[1], W[2]); W[4] = cmul(W[2], W[2]);
#pragma unroll
            for (int k = 1; k <= 4; ++k) W[4 + k] = cmul(W[k], W[4]);
#pragma unroll
            for (int k = 1; k <= 8; ++k) W[8 + k] = cmul(W[k], W[8]);
#pragma unroll
            for (int j = 0; j < 16; ++j) lds_st<u32>(wa + j * 288, pkc(V[j]));
            FENCE();
            bf16x8 vf[4], uf[4];
#pragma unroll
            for (int s = 0; s < 4; ++s) vf[s] = lds_ld<bf16x8>(ra + s * 64);
            FENCE();
#pragma unroll
            for (int r = 0; r < 8; ++r) { lds_st<u32>(wa + r * 288, pkc(R[r])); lds_st<u32>(wa + (8 + r) * 288, pkc(r ? cmul(R[r], W[1]) : W[1])); }
            FENCE();
#pragma unroll
            for (int s = 0; s < 4; ++s) uf[s] = lds_ld<bf16x8>(ra + s * 64);
            FENCE();
#pragma unroll
            for (int k = 0; k < 2; ++k)
#pragma unroll
                for (int r = 0; r < 4; ++r) lds_st<u32>(wa + (4 * k + r) * 288, pkc(r ? cmul(R[2 * r], W[2 + k]) : W[2 + k]));
#pragma unroll
            for (int k = 0; k < 4; ++k) { lds_st<u32>(wa + (8 + 2 * k) * 288, pkc(W[4 + k])); lds_st<u32>(wa + (9 + 2 * k) * 288, pkc(cmul(W[4 + k], R[4]))); }
            FENCE();
            ka[0] = f32x4{};
#pragma unroll
            for (int s = 0; s < 4; ++s) ka[0] = MFMA16(uf[s], vf[s], ka[0]);
#pragma unroll
            for (int s = 0; s < 4; ++s) uf[s] = lds_ld<bf16x8>(ra + s * 64);
            FENCE();
#pragma unroll
            for (int k = 0; k < 9; ++k) lds_st<u32>(wa + k * 288, pkc(W[8 + k]));
            FENCE();
            ka[1] = f32x4{};
#pragma unroll
            for (int s = 0; s < 4; ++s) ka[1] = MFMA16(uf[s], vf[s], ka[1]);
#pragma unroll
            for (int s = 0; s < 4; ++s) uf[s] = lds_ld<bf16x8>(ra + s * 64);
            FENCE();
            ka[2] = f32x4{};
#pragma unroll
            for (int s = 0; s < 4; ++s) ka[2] = MFMA16(uf[s], vf[s], ka[2]);
        }
        asm volatile("" :: "v"(tch), "v"(tch2));
        STAMP(3);
        const u32 ta = get_tag(tagw), tb = tag2(ta);
        u32 uvo = WS_UFLAG + (u32)((d >> 5) * 64 + lane) * FS;
        u32x2 uf = __builtin_amdgcn_raw_buffer_load_b64(ws, uvo, 0, 16);
        SBAR();
#pragma unroll
        for (int t = 0; t < 3; ++t) lds_st<f32x4>(s0 + t * 1024 + lane * 16, ka[t]);
        sig(mysig, 1);
        wait1(psig, 1);
        u32x4 ur[2];
        {
            const u32 ps0 = lds0 + L_S0 + (bw ^ 1) * S0_SZ;
#pragma unroll
            for (int t = 0; t < 3; ++t) ka[t] += lds_ld<f32x4>(ps0 + t * 1024 + lane * 16);
            sig(mysig, 2);
            lds_st<u32x2>(kc + 2 * (128 * (q4 >> 1) + 4 * (q4 & 1) + 8 * m16), u32x2{pkbf(ka[0][0], ka[0][1]), pkbf(ka[0][2], ka[0][3])});
            const u32 a0 = q4 < 2 ? 256 + 64 * q4 + 4 * m16 : 384 + 64 * (q4 - 2) + 2 * m16, a1 = a0 + (q4 < 2 ? 2 : 32);
            lds_st<u32>(kc + 2 * a0, pkbf(ka[1][0], ka[1][1]));
            lds_st<u32>(kc + 2 * a1, pkbf(ka[1][2], ka[1][3]));
            const u32 w01 = pkbf(ka[2][0], ka[2][1]), w23 = pkbf(ka[2][2], ka[2][3]);
            const u32 a2 = kc + 2 * (512 + (q4 == 3 ? 144 : 64 * q4) + m16);
            lds_st<u16>(a2, (u16)w01); lds_st<u16>(a2 + 32, (u16)(w01 >> 16)); lds_st<u16>(a2 + 64, (u16)w23); lds_st<u16>(a2 + 96, (u16)(w23 >> 16));
            FENCE();
            uvo = ((uf.x ^ ta) | (uf.y ^ tb)) == 0 ? OOB : uvo;
            if (__builtin_amdgcn_ballot_w64(uvo != OOB) != 0) poll_flags(ws, uvo, ta, tb);
            FENCE();
#pragma unroll
            for (int k = 0; k < 2; ++k) ur[k] = __builtin_amdgcn_raw_buffer_load_b128(ws, (u32)(lane >> 1) * 64 + (u32)(lane & 1) * 16 + k * 2048, WS_UT + (u32)(16 * xcd + (idx >> 1)) * 16384 + (u32)(idx & 1) * 32 + (u32)bw * 4096, 16);
            SBAR();
            u32 gv[19];
#pragma unroll
            for (int e = 0; e < 19; ++e) gv[e] = lds_ld<u16>(ga[e]);
            wait1(psig, 2);
#pragma unroll
            for (int k = 0; k < 9; ++k) {
                lds_st<u32>(s0 + 36 * lane + 4 * k, gv[2 * k] | (gv[2 * k + 1] << 16));
                lds_st<u32>(s0 + 2368 + 36 * lane + 4 * k, gv[2 * k + 1] | (gv[2 * k + 2] << 16));
            }
            FENCE();
        }
        STAMP(4);
        wait4(syncA, 1);
#pragma unroll
        for (int k = 0; k < 2; ++k) lds_st<u32x4>(lds0 + L_UB + UB_PAD + (u32)(lane & 1) * UB_SZ + (u32)(lane >> 1) * 16 + (u32)bw * 1024 + k * 512, ur[k]);
        sig(mysig, 3);
        wait4(syncK, 3);
        __builtin_amdgcn_s_setprio(0);
        STAMP(5);
        f32x4 acc[8];
#pragma unroll
        for (int i = 0; i < 8; ++i) acc[i] = f32x4{};
        {
            const u32 bb = ub + 256 - 256 * half + 32 * m16 + 16 * q4;
            const u32 sgl = (u32)(1 - (m16 & 1));
            const u32 ab = s0 + 4 * (592 * sgl + ((111 - m16 - sgl) >> 1) + 4 * q4 - 48);
            bf16x8 bfr[2][8], afr[4];
            auto lda = [&](int n) { const int r = n >> 3, i = n & 7; const u32 o = ab + 4 * (72 * i + 16 * (3 - r));
                u32x4 av = {lds_ld<u32>(o), lds_ld<u32>(o + 4), lds_ld<u32>(o + 8), lds_ld<u32>(o + 12)}; return __builtin_bit_cast(bf16x8, av); };
            afr[0] = lda(0);
#pragma unroll
            for (int m = 0; m < 8; ++m) bfr[0][m] = lds_ld<bf16x8>(bb + 512 * m + 192);
            afr[1] = lda(1); afr[2] = lda(2);
            SBAR();
#pragma unroll
            for (int r = 0; r < 4; ++r) {
                const int cu = r & 1, nx = cu ^ 1;
#pragma unroll
                for (int i = 0; i < 8; ++i) {
                    const int n = 8 * r + i;
                    if (n + 3 < 32) afr[(n + 3) & 3] = lda(n + 3);
                    if (r < 3 && i < 4) { bfr[nx][2 * i] = lds_ld<bf16x8>(bb + 1024 * i + 64 * (2 - r)); bfr[nx][2 * i + 1] = lds_ld<bf16x8>(bb + 1024 * i + 512 + 64 * (2 - r)); }
#pragma unroll
                    for (int cb = i; cb < 8; ++cb) acc[cb] = MFMA16(afr[n & 3], bfr[cu][cb - i], acc[cb]);
                    SBAR();
                }
            }
        }
        FENCE();
        __builtin_amdgcn_s_setprio(3);
        STAMP(6);
        {
            const u32 ps0 = lds0 + L_S0 + (bw ^ 1) * S0_SZ;
            f32x4 kp[4];
            if (half) {
#pragma unroll
                for (int k = 0; k < 4; ++k) { lds_st<f32x4>(s0 + k * 1024 + lane * 16, acc[k]); kp[k] = acc[4 + k]; }
            } else {
#pragma unroll
                for (int k = 0; k < 4; ++k) { lds_st<f32x4>(s0 + k * 1024 + lane * 16, acc[4 + k]); kp[k] = acc[k]; }
            }
            sig(mysig, 4);
            wait1(psig, 4);
#pragma unroll
            for (int k = 0; k < 4; ++k) kp[k] += lds_ld<f32x4>(ps0 + k * 1024 + lane * 16);
            st128<0, true>(wss, u32x4{pkbf(kp[0][0], kp[0][1]), pkbf(kp[0][2], kp[0][3]), pkbf(kp[1][0], kp[1][1]), pkbf(kp[1][2], kp[1][3])}, (u32)lane * 16, WS_YT + (u32)d * 4096 + (u32)half * 2048);
            st128<1024, true>(wss, u32x4{pkbf(kp[2][0], kp[2][1]), pkbf(kp[2][2], kp[2][3]), pkbf(kp[3][0], kp[3][1]), pkbf(kp[3][2], kp[3][3])}, (u32)lane * 16, WS_YT + (u32)d * 4096 + (u32)half * 2048);
            WAITV(0);
            __builtin_amdgcn_raw_buffer_store_b64(u32x2{ta, tb}, ws, lane ? OOB : 0u, WS_YFLAG + (u32)(half * 512 + d) * FS, 16);
        }
        STAMP(7);
    }
    STAMP_OUT();
    if (WHO < 0 || (int)__builtin_amdgcn_workgroup_id_x() == WHO) while (__builtin_amdgcn_s_memrealtime() - tstart < ENDT) __builtin_amdgcn_s_sleep(4);
}

#if defined(__HIP_DEVICE_COMPILE__)
#pragma clang attribute pop
#endif

extern "C" void kernel_launch(void* const* d_in, const int* in_sizes, int n_in, void* d_out, int out_size, void* d_ws, size_t ws_size, hipStream_t stream) {
    Params p;
    for (int i = 0; i < 18; ++i) p.in[i] = d_in[i];
    p.out = (float*)d_out;
    p.ws = (char*)d_ws;
    (void)hipFuncSetAttribute((const void*)ssm_fused, hipFuncAttributeMaxDynamicSharedMemorySize, LDS_BYTES);
    ssm_fused<<<256, 768, LDS_BYTES, stream>>>(p);
}
```

```cpp
#include <hip/hip_runtime.h>
#include <hip/hip_bf16.h>
#include <stdint.h>
#include <stdio.h>

typedef unsigned short u16;
__device__ __forceinline__ float bf2f(u16 v) { return __uint_as_float(((unsigned)v) << 16); }
__device__ __forceinline__ u16 f2bf(float f) {
    unsigned u = __float_as_uint(f);
    return (u16)((u + 0x7FFFu + ((u >> 16) & 1u)) >> 16);
}
constexpr int L = 2048, D = 512, P = 512, N = 128;

__global__ void a1(const u16* x, const u16* Lre, const u16* Lim, const u16* Bre, const u16* Bim, const u16* ldt, float2* Bu) {
    int p = blockIdx.x * blockDim.x + threadIdx.x;
    int t = blockIdx.y;
    float sr = 0, si = 0;
    for (int d = 0; d < D; ++d) { float xv = bf2f(x[t * D + d]); sr += xv * bf2f(Bre[p * D + d]); si += xv * bf2f(Bim[p * D + d]); }
    float dt = bf2f(f2bf(expf(bf2f(ldt[p]))));
    float lr = bf2f(Lre[p]), li = bf2f(Lim[p]);
    float m = expf(dt * lr); float br = m * cosf(dt * li), bi = m * sinf(dt * li);
    float nr = br - 1.f, ni = bi; float den = lr * lr + li * li;
    float cr = (nr * lr + ni * li) / den, ci = (ni * lr - nr * li) / den;
    Bu[t * P + p] = make_float2(cr * sr - ci * si, cr * si + ci * sr);
}
__global__ void a2(const u16* Lre, const u16* Lim, const u16* ldt, float2* Bu) {
    int p = blockIdx.x * blockDim.x + threadIdx.x;
    float dt = bf2f(f2bf(expf(bf2f(ldt[p]))));
    float lr = bf2f(Lre[p]), li = bf2f(Lim[p]);
    float m = expf(dt * lr); float br = m * cosf(dt * li), bi = m * sinf(dt * li);
    float hr = 0, hi = 0;
    for (int t = 0; t < L; ++t) { float2 b = Bu[t * P + p]; float nr = br * hr - bi * hi + b.x, ni = br * hi + bi * hr + b.y; hr = nr; hi = ni; Bu[t * P + p] = make_float2(hr, hi); }
}
__global__ void a3(const u16* x, const u16* Cre, const u16* Cim, const u16* Dk, const float2* h, float* out) {
    int d = blockIdx.x * blockDim.x + threadIdx.x;
    int t = blockIdx.y;
    float s = 0;
    for (int p = 0; p < P; ++p) { float2 hv = h[t * P + p]; s += hv.x * bf2f(Cre[d * P + p]) - hv.y * bf2f(Cim[d * P + p]); }
    out[t * 1024 + d] = 2.f * s + bf2f(f2bf(bf2f(x[t * D + d]) * bf2f(Dk[d])));
}
__global__ void b1(const u16* Are, const u16* Aim, const u16* Bre, const u16* Bim, const u16* Cre, const u16* Cim, const u16* ldt, float* K) {
    int l = blockIdx.x * blockDim.x + threadIdx.x;
    int d = blockIdx.y;
    float dt = bf2f(f2bf(expf(bf2f(ldt[d]))));
    float lf = bf2f(f2bf((float)l));
    float s = 0;
    for (int n = 0; n < N; ++n) {
        int i = d * N + n;
        float zr = dt * bf2f(Are[i]) * lf, zi = dt * bf2f(Aim[i]) * lf;
        float br = dt * bf2f(Bre[i]), bi = dt * bf2f(Bim[i]);
        float cr = bf2f(Cre[i]), ci = bf2f(Cim[i]);
        float wr = cr * br - ci * bi, wi = cr * bi + ci * br;
        float m = expf(zr);
        s += m * (wr * cosf(zi) - wi * sinf(zi));
    }
    K[d * L + l] = s;
}
__global__ void b2(const u16* u, const float* K, const u16* Dk, float* out) {
    int d = blockIdx.x * blockDim.x + threadIdx.x;
    int t = blockIdx.y;
    float s = 0;
    for (int l = 0; l <= t; ++l) s += K[d * L + l] * bf2f(u[(t - l) * D + d]);
    float y = bf2f(f2bf(s));
    float sk = bf2f(f2bf(bf2f(u[t * D + d]) * bf2f(Dk[d])));
    out[t * 1024 + 512 + d] = bf2f(f2bf(y + sk));
}

extern "C" void kernel_launch(void* const* d_in, const int* in_sizes, int n_in, void* d_out, int out_size, void* d_ws, size_t ws_size, hipStream_t stream) {
    const u16* const* in = (const u16* const*)d_in;
    float* out = (float*)d_out;
    for (int i = 0; i < 18; ++i) fprintf(stderr, "in%d %p ", i, d_in[i]);
    fprintf(stderr, "out %p ws %p\n", d_out, d_ws);
    float2* Bu = (float2*)d_ws;
    float* K = (float*)((char*)d_ws + (size_t)L * P * 8);
    a1<<<dim3(P / 64, L), 64, 0, stream>>>(in[0], in[1], in[2], in[3], in[4], in[7], Bu);
    a2<<<dim3(P / 64), 64, 0, stream>>>(in[1], in[2], in[7], Bu);
    a3<<<dim3(D / 64, L), 64, 0, stream>>>(in[0], in[5], in[6], in[8], Bu, out);
    b1<<<dim3(L / 64, D), 64, 0, stream>>>(in[10], in[11], in[12], in[13], in[14], in[15], in[16], K);
    b2<<<dim3(D / 64, L), 64, 0, stream>>>(in[9], K, in[17], out);
}
```

```cpp
#define MW 0
#define MS 0
#define FG 1
#define LG 1
#define ENDT 0
#include <hip/hip_runtime.h>
#include <stdint.h>

typedef unsigned short u16;
typedef unsigned u32;
typedef unsigned long long u64;
typedef short bf16x8 __attribute__((ext_vector_type(8)));
typedef short s16x4 __attribute__((ext_vector_type(4)));
typedef float f32x2 __attribute__((ext_vector_type(2)));
typedef float f32x4 __attribute__((ext_vector_type(4)));
typedef float f32x16 __attribute__((ext_vector_type(16)));
typedef unsigned u32x2 __attribute__((ext_vector_type(2)));
typedef unsigned u32x4 __attribute__((ext_vector_type(4)));
typedef unsigned u32x8 __attribute__((ext_vector_type(8)));
typedef unsigned u32x16 __attribute__((ext_vector_type(16)));
typedef __bf16 bf16x2_t __attribute__((ext_vector_type(2)));
#define LAS __attribute__((address_space(3)))
#define GAS __attribute__((address_space(1)))
typedef __amdgpu_buffer_rsrc_t rsrc_t;

#define ABL 0
#define PRA 3
#define PRK 1
#define UM 7
#define DBG1 0
#define LSTAG 5
#define CTHR 0
#define PRL 3
#define WSL 0
#define LWIN 12
#define UFN 6
#define TOUCH 0
#define CPF 2
#define OAUX 16
#define KGATE 2
#define CGATE 0
#define CVN 2
#define UDLY 0
#define UPOLL 7
#define CWIN 8
#define MATHN 12
#define NOZ 0
#define NOU 0
#define L0P 1
#define CB1N 3
#define EDLY 8
#define LDLY 3
#define CSTART 1
#define YDLY 1
#define YSL 8
#define REMAP 1
#define XW 0
#define NOKP 0
#define UFIRST 0
#define CPAUSE 7
#define FIRSTW 0
#define NOT1 0
#define NOC 0
#define NOTCH 0
#define NOSK 0
#define HSL 0
#define PRSH 0
#define PRCV 2
#define SPF 0
#define SPFW 0
#define YGATE 5
#define SIG5 8
#define PDLY 0
#define YSG 0
#define PPOS 1
#define CW 2
#define PAUX 16
#define HDLY 28
#define SPEC 1
#define CWI 1
#define ROTG 8
#define PK 0
#define KCG 0
#define SPF2 0
#define PACE1 0
#define PACE2 0
#define PACE3 0
#define OAUXB 16
#define DBG2 0
#define STAMPS 0

constexpr u32 WS_W = 0;
constexpr u32 FS = 128;
constexpr u32 WS_UFLAG = 524288;
constexpr u32 WS_HFLAG = 131072;
constexpr u32 WS_YFLAG = 262144;
constexpr u32 WS_E = 393216;
constexpr u32 WS_UT = 1u << 20;
constexpr u32 WS_YT = 3u << 20;
constexpr u32 WS_H = 5u << 20;
constexpr u32 WS_CS = 9u << 20;
constexpr u32 WS_BYTES = 10u << 20;
constexpr u32 WS_STAMP = 10u << 20;
constexpr u32 OOB = 0x7FFFFF00u;
constexpr u32 L_R2 = 131072;
constexpr u32 L_S0 = L_R2, S0_SZ = 4672;
constexpr u32 L_KC = L_R2 + 4 * S0_SZ + 48, KC_SZ = 1488;
constexpr u32 L_CB1 = L_KC + 2 * KC_SZ - 48;
constexpr u32 L_CB2 = L_CB1 + 768;
constexpr u32 L_ZL = L_CB2 + 256;
constexpr u32 L_ZA = L_ZL + 128;
constexpr u32 L_SYNC = L_ZA + 128;
constexpr u32 L_UB = L_SYNC + 64, UB_PAD = 960;
constexpr u32 L_W = L_UB + UB_PAD + 8192;
constexpr u32 L_PAR = 65536;
constexpr u32 LDS_BYTES = 163840;
static_assert(L_W + 16 <= LDS_BYTES, "lds");
constexpr int NWAVES = 12;
#define HWIN 9
#define PSLEEP 1
constexpr u32 SPIN_CAP = 1u << 15;
constexpr u32 CODE_BYTES = ABL >= 2 ? 6144 : ABL ? 17408 : 28416;
constexpr u32 CODE_SLICES = 3;
constexpr u32 SENT = 0xFFFFFFFFu;

struct Params { const void* in[18]; float* out; char* ws; };

#if defined(__HIP_DEVICE_COMPILE__) && !PK
#pragma clang attribute push(__attribute__((target("no-packed-fp32-ops"))), apply_to = function)
#endif
template <int I> struct ic { static constexpr int v = I; };
template <int B, int E, typename F> __device__ __forceinline__ void sfor(F&& f) { if constexpr (B < E) { f(ic<B>{}); sfor<B + 1, E>(f); } }
#define INL __attribute__((always_inline))
__device__ __forceinline__ u32 umin(u32 a, u32 b) { return a < b ? a : b; }
__device__ __forceinline__ u32 umax(u32 a, u32 b) { return a > b ? a : b; }
__device__ __forceinline__ u32 umax3(u32 a, u32 b, u32 c) { return umax(umax(a, b), c); }
__device__ __forceinline__ float u2f(u32 v) { return __builtin_bit_cast(float, v); }
__device__ __forceinline__ u32 f2u(float v) { return __builtin_bit_cast(u32, v); }

__device__ __forceinline__ float bf2f(u32 v) { return u2f(v << 16); }
__device__ __forceinline__ u32 pkbf(float lo, float hi) { f32x2 a = {lo, hi}; return __builtin_bit_cast(u32, __builtin_convertvector(a, bf16x2_t)); }
__device__ __forceinline__ float rbf(float x) { return u2f(pkbf(x, x) << 16); }
__device__ __forceinline__ u32x4 mk_srd(const void* p, u32 bytes) { u64 a = (u64)p; u32x4 r = {(u32)a, (u32)(a >> 32) & 0xFFFFu, bytes, 0x00020000u}; return r; }
__device__ __forceinline__ rsrc_t mk_rsrc(const void* p, u32 bytes) { return __builtin_amdgcn_make_buffer_rsrc((void*)p, 0, bytes, 0x00020000); }
__device__ __forceinline__ void dma4(u32x4 srd, u32 v0, u32 v1, u32 soff, u32 ldst) {
    asm volatile("s_mov_b32 m0, %0\n\ts_nop 0\n\t"
                 "buffer_load_dwordx4 %1, %3, %4 offen lds\n\t"
                 "buffer_load_dwordx4 %2, %3, %4 offen offset:1024 lds\n\t"
                 "buffer_load_dwordx4 %1, %3, %4 offen offset:2048 lds\n\t"
                 "buffer_load_dwordx4 %2, %3, %4 offen offset:3072 lds"
                 :: "s"(ldst), "v"(v0), "v"(v1), "s"(srd), "s"(soff) : "m0", "memory");
}
__device__ __forceinline__ void dma4n(u32x4 srd, const u32 (&v)[4], u32 soff, u32 ldst) {
    asm volatile("s_mov_b32 m0, %0\n\ts_nop 0\n\t"
                 "buffer_load_dwordx4 %1, %5, %6 offen lds\n\t"
                 "buffer_load_dwordx4 %2, %5, %6 offen offset:1024 lds\n\t"
                 "buffer_load_dwordx4 %3, %5, %6 offen offset:2048 lds\n\t"
                 "buffer_load_dwordx4 %4, %5, %6 offen offset:3072 lds"
                 :: "s"(ldst), "v"(v[0]), "v"(v[1]), "v"(v[2]), "v"(v[3]), "s"(srd), "s"(soff) : "m0", "memory");
}
__device__ __forceinline__ void dma1(u32x4 srd, u32 v0, u32 soff, u32 ldst) {
    asm volatile("s_mov_b32 m0, %0\n\ts_nop 0\n\tbuffer_load_dwordx4 %1, %2, %3 offen lds" :: "s"(ldst), "v"(v0), "s"(srd), "s"(soff) : "m0", "memory");
}
__device__ __forceinline__ void zsig(u32x4 srd, u32 voob, u32 ldst) {
    asm volatile("s_mov_b32 m0, %0\n\ts_mov_b64 exec, 1\n\tbuffer_load_dword %1, %2, 0 offen lds\n\ts_mov_b64 exec, -1" :: "s"(ldst), "v"(voob), "s"(srd) : "m0", "memory");
}
template <int IMM, bool SC1> __device__ __forceinline__ void st128(u32x4 srd, u32x4 v, u32 voff, u32 soff) {
    if (SC1) asm volatile("buffer_store_dwordx4 %0, %1, %2, %3 offen offset:%4 sc1\n\ts_nop 1" :: "v"(v), "v"(voff), "s"(srd), "s"(soff), "i"(IMM) : "memory");
    else if (OAUX == 2) asm volatile("buffer_store_dwordx4 %0, %1, %2, %3 offen offset:%4 nt\n\ts_nop 1" :: "v"(v), "v"(voff), "s"(srd), "s"(soff), "i"(IMM) : "memory");
    else if (OAUX == 16) asm volatile("buffer_store_dwordx4 %0, %1, %2, %3 offen offset:%4 sc1\n\ts_nop 1" :: "v"(v), "v"(voff), "s"(srd), "s"(soff), "i"(IMM) : "memory");
    else asm volatile("buffer_store_dwordx4 %0, %1, %2, %3 offen offset:%4\n\ts_nop 1" :: "v"(v), "v"(voff), "s"(srd), "s"(soff), "i"(IMM) : "memory");
}
#define SBAR() __builtin_amdgcn_sched_barrier(0)
#define WAITV(N) asm volatile("s_waitcnt vmcnt(%0)" :: "i"(N) : "memory")
#define WAITL() asm volatile("s_waitcnt lgkmcnt(0)" ::: "memory")
#define FENCE() asm volatile("" ::: "memory")
template <typename T> __device__ __forceinline__ T lds_ld(u32 a) { return *(const LAS T*)(uintptr_t)a; }
template <typename T> __device__ __forceinline__ void lds_st(u32 a, T v) { *(LAS T*)(uintptr_t)a = v; }
__device__ __forceinline__ u32x2 lds_tr(u32 a) { return __builtin_bit_cast(u32x2, __builtin_amdgcn_ds_read_tr16_b64_v4i16((LAS s16x4*)(uintptr_t)a)); }
__device__ __forceinline__ void sig(u32 addr, u32 val) { asm volatile("ds_write_b32 %0, %1" :: "v"(addr), "v"(val) : "memory"); }
__device__ __forceinline__ u32 rfl(u32 v) { return (u32)__builtin_amdgcn_readfirstlane(v); }
__device__ __forceinline__ u32 peek4(u32 addr) { u32x4 v; asm volatile("ds_read_b128 %0, %1\n\ts_waitcnt lgkmcnt(0)" : "=v"(v) : "v"(addr) : "memory"); return rfl(umin(umin(v.x, v.y), umin(v.z, v.w))); }
__device__ __forceinline__ u32 peek3(u32 addr) { u32x4 v; asm volatile("ds_read_b128 %0, %1\n\ts_waitcnt lgkmcnt(0)" : "=v"(v) : "v"(addr) : "memory"); return rfl(umin(umin(v.y, v.z), v.w)); }
__device__ __forceinline__ u32 peek1(u32 addr) { u32 v; asm volatile("ds_read_b32 %0, %1\n\ts_waitcnt lgkmcnt(0)" : "=v"(v) : "v"(addr) : "memory"); return rfl(v); }
__device__ __forceinline__ u32 peekz2(u32 addr) { u32x2 v; asm volatile("ds_read_b64 %0, %1\n\ts_waitcnt lgkmcnt(0)" : "=v"(v) : "v"(addr) : "memory"); return rfl(v.x | v.y); }
__device__ __forceinline__ u32 peekz3(u32 addr) { u32x4 v; asm volatile("ds_read_b128 %0, %1\n\ts_waitcnt lgkmcnt(0)" : "=v"(v) : "v"(addr) : "memory"); return rfl(v.x | v.y | v.z); }
__device__ __forceinline__ u32 peekz4(u32 addr) { u32x4 v; asm volatile("ds_read_b128 %0, %1\n\ts_waitcnt lgkmcnt(0)" : "=v"(v) : "v"(addr) : "memory"); return rfl(v.x | v.y | v.z | v.w); }
#define SPINW(cond, slp) do { _Pragma("unroll 1") for (u32 sp_ = 0; (cond) && sp_ < (1u << 22); ++sp_) { if (slp) __builtin_amdgcn_s_sleep(slp); } } while (0)
__device__ __forceinline__ void waitz3(u32 addr) { SPINW(peekz3(addr) != 0, WSL); }
__device__ __forceinline__ void waitz3s(u32 addr) { SPINW(peekz3(addr) != 0, 2); }
__device__ __forceinline__ void waitz4(u32 addr) { SPINW(peekz4(addr) != 0, WSL); }
#define BAR() asm volatile("s_waitcnt lgkmcnt(0)\n\ts_barrier" ::: "memory")
__device__ __forceinline__ void wait4(u32 addr, u32 val) { SPINW(peek4(addr) < val, WSL); }
__device__ __forceinline__ void wait4s(u32 addr, u32 val) { SPINW(peek4(addr) < val, 2); }
__device__ __forceinline__ void wait3(u32 addr, u32 val) { SPINW(peek3(addr) < val, WSL); }
__device__ __forceinline__ void wait1(u32 addr, u32 val) { SPINW(peek1(addr) < val, WSL); }
__device__ __forceinline__ void wait1s(u32 addr, u32 val) { SPINW(peek1(addr) < val, 2); }
__device__ __forceinline__ u32 get_tag_old(u32 addr) {
    u32x2 v = {0u, 0u};
#pragma unroll 1
    for (u32 s = 0; s < (1u << 22); ++s) {
        asm volatile("ds_read_b64 %0, %1\n\ts_waitcnt lgkmcnt(0)" : "=v"(v) : "v"(addr) : "memory");
        if (__builtin_amdgcn_readfirstlane(v.y) != 0) break;
    }
    return __builtin_amdgcn_readfirstlane(v.x);
}
__device__ __forceinline__ u32 tag2(u32 t) { return t * 2654435761u + 0x2545F491u; }
typedef f32x2 cf;
__device__ __forceinline__ cf cmul(cf a, cf b) { return cf{__builtin_fmaf(-a.y, b.y, a.x * b.x), __builtin_fmaf(a.y, b.x, a.x * b.y)}; }
__device__ __forceinline__ cf csq(cf a) { return cf{__builtin_fmaf(-a.y, a.y, a.x * a.x), (a.x + a.x) * a.y}; }
__device__ __forceinline__ cf cfma(cf a, cf b, cf c) { return cf{__builtin_fmaf(-a.y, b.y, __builtin_fmaf(a.x, b.x, c.x)), __builtin_fmaf(a.y, b.x, __builtin_fmaf(a.x, b.y, c.y))}; }
__device__ __forceinline__ f32x2 fma2(float a, f32x2 b, f32x2 c) { return f32x2{__builtin_fmaf(a, b.x, c.x), __builtin_fmaf(a, b.y, c.y)}; }
__device__ __forceinline__ u32 pkc(cf a) { return __builtin_bit_cast(u32, __builtin_convertvector(a, bf16x2_t)); }
__device__ __forceinline__ cf cexpk(float zr, float zi, float k) {
    float m = __builtin_amdgcn_exp2f(zr * (k * 1.4426950408889634f));
    float f = __builtin_amdgcn_fractf(zi * (k * 0.15915494309189535f));
    return cf{m * __builtin_amdgcn_cosf(f), m * __builtin_amdgcn_sinf(f)};
}
__device__ __forceinline__ void swap_halves(cf v, cf& lo, cf& hi) {
    auto a = __builtin_amdgcn_permlane32_swap(f2u(v.x), f2u(v.x), false, false);
    auto b = __builtin_amdgcn_permlane32_swap(f2u(v.y), f2u(v.y), false, false);
    lo = cf{u2f(a[0]), u2f(b[0])}; hi = cf{u2f(a[1]), u2f(b[1])};
}
template <int SL = PSLEEP> __device__ __forceinline__ void poll_flags(rsrc_t ws, u32 voff, u32 ta, u32 tb) {
#pragma unroll 1
    for (u32 s = 0; s < SPIN_CAP; ++s) {
        FENCE();
        u32x2 v = __builtin_amdgcn_raw_buffer_load_b64(ws, voff, 0, PAUX);
        voff = ((v.x ^ ta) | (v.y ^ tb)) == 0 ? OOB : voff;
        if (__builtin_amdgcn_ballot_w64(voff != OOB) == 0) break;
        if (SL) __builtin_amdgcn_s_sleep(SL);
    }
    FENCE();
}
__device__ __forceinline__ f32x4 sel4(bool c, f32x4 a, f32x4 b) { asm volatile("" : "+v"(a), "+v"(b)); return c ? a : b; }
#define ADD4(s, v) (((s) + (v).x + (v).y) + (v).z + (v).w)
#define MFMA32(a, b, c) __builtin_amdgcn_mfma_f32_32x32x16_bf16(a, b, c, 0, 0, 0)
#define MFMA16(a, b, c) __builtin_amdgcn_mfma_f32_16x16x32_bf16(a, b, c, 0, 0, 0)

#define STAMP(i) do {} while (0)
#define STAMP_OUT() do {} while (0)

__global__ __launch_bounds__(768, 3) void ssm_fused(Params) {
    STAMP(0);
    const u64 tstart = __builtin_amdgcn_s_memrealtime();
    const auto kseg = __builtin_amdgcn_kernarg_segment_ptr();
    extern __shared__ __attribute__((aligned(16))) char smem[];
    const u32 lds0 = (u32)(uintptr_t)(LAS char*)smem;
    const u32 tid = __builtin_amdgcn_workitem_id_x();
    const int lane = tid & 63;
    const int hwv = __builtin_amdgcn_readfirstlane(tid >> 6);
    const u32 bid = __builtin_amdgcn_workgroup_id_x();
    u32x16 k0, k1; u32x8 k2; u32x4 cpf;
    if (hwv >= 8) {
        asm volatile("s_load_dwordx16 %0, %3, 0x0\n\ts_load_dwordx16 %1, %3, 0x40\n\ts_load_dwordx8 %2, %3, 0x80\n\ts_setprio 3\n\ts_barrier" : "=&s"(k0), "=&s"(k1), "=&s"(k2) : "s"(kseg) : "memory");
    } else {
        u64 pc0; asm volatile("s_getpc_b64 %0" : "=s"(pc0));
        cpf = __builtin_amdgcn_raw_buffer_load_b128(mk_rsrc((const void*)(pc0 & ~255ull), hwv < CPF ? CODE_BYTES : 0u), (u32)lane * 16, ((bid >> 3) ^ ((u32)hwv * 16)) * 1024, 0);
        const u32 iv = tid < 160 ? SENT : 0u;
        lds_st<u32x2>(lds0 + L_CB1 + umin(tid, 287u) * 8, u32x2{iv, iv});
        asm volatile("s_waitcnt lgkmcnt(0)\n\ts_barrier" ::: "memory");
    }
    const int wv = (int)((0x0213BA987654ull >> (4 * hwv)) & 15);
    const int xcd = bid & 7, idx = bid >> 3;
    const u32 syncU = lds0 + L_SYNC, syncA = syncU + 16, syncK = syncU + 32, tagw = syncU + 48;
    const int rr = lane >> 3, sp = lane & 7;
    const int m16 = lane & 15, q4 = lane >> 4;
    const int r32 = lane & 31, hh = lane >> 5;
    const int g32 = 4 * (r32 & 7) + (r32 >> 3);
    const int ppos = (xcd < 4 ? xcd : (xcd ^ 2)) >> 1;
    const int c = 4 * ppos + (idx & 3), pb = 8 * (xcd & 1) + (idx >> 2);
    const int tb64 = 8 * ppos + 4 * (xcd & 1) + (idx & 3), db = idx >> 2;
    const int dK = 64 * xcd + 2 * idx + ((wv >> 1) & 1);
    u32 voob = OOB;
#define LATE_P() auto kl_ = kseg; asm volatile("" : "+s"(kl_)); const __attribute__((address_space(4))) Params& p = *(const __attribute__((address_space(4))) Params*)kl_
#define KPTR(k, i) ((const GAS char*)(((u64)k[2 * (i) + 1] << 32) | k[2 * (i)]))
    u32 pr[7], wv0;
    const u32 zpar = lds0 + L_ZL + 84, zu = lds0 + L_ZL + 80;
#define get_tag(x) (peek1(lds0 + L_W) + 1u)
    asm volatile("" : "+v"(voob));
    if (hwv == 9) {
        u32 vq0 = (u32)(64 * xcd + 2 * idx) * 256 + (u32)lane * 16, vq1 = (u32)pb * 64 + (u32)lane * 16, vq2 = (u32)(64 * xcd + 2 * idx) * 2;
        asm volatile("s_waitcnt lgkmcnt(0)\n\t"
                     "s_mov_b32 m0, %0\n\ts_mov_b32 exec_hi, 0\n\tglobal_load_lds_dwordx4 %1, %[b0]\n\t"
                     "s_add_u32 m0, m0, 512\n\ts_nop 0\n\tglobal_load_lds_dwordx4 %1, %[b1]\n\t"
                     "s_add_u32 m0, m0, 512\n\ts_nop 0\n\tglobal_load_lds_dwordx4 %1, %[b2]\n\t"
                     "s_add_u32 m0, m0, 512\n\ts_nop 0\n\tglobal_load_lds_dwordx4 %1, %[b3]\n\t"
                     "s_add_u32 m0, m0, 512\n\ts_nop 0\n\tglobal_load_lds_dwordx4 %1, %[b4]\n\t"
                     "s_add_u32 m0, m0, 512\n\ts_nop 0\n\tglobal_load_lds_dwordx4 %1, %[b5]\n\t"
                     "s_add_u32 m0, m0, 512\n\ts_mov_b32 exec_lo, 15\n\tglobal_load_lds_dwordx4 %2, %[a0]\n\t"
                     "s_add_u32 m0, m0, 64\n\ts_nop 0\n\tglobal_load_lds_dwordx4 %2, %[a1]\n\t"
                     "s_add_u32 m0, m0, 64\n\ts_nop 0\n\tglobal_load_lds_dwordx4 %2, %[a2]\n\t"
                     "s_add_u32 m0, m0, 64\n\ts_mov_b32 exec_lo, 1\n\tglobal_load_lds_dword %3, %[c0]\n\t"
                     "s_add_u32 m0, m0, 4\n\ts_nop 0\n\tglobal_load_lds_dword %3, %[c1]\n\t"
                     "s_mov_b32 m0, %4\n\ts_nop 0\n\tglobal_load_lds_dword %5, %[w] sc1\n\ts_mov_b64 exec, -1"
                     :: "s"(lds0 + L_PAR), "v"(vq0), "v"(vq1), "v"(vq2), "s"(lds0 + L_W), "v"(0u),
                        [b0] "s"(k1.s45), [b1] "s"(k1.s67), [b2] "s"(k1.s89), [b3] "s"(k1.sab), [b4] "s"(k1.scd), [b5] "s"(k1.sef),
                        [a0] "s"(k0.s23), [a1] "s"(k0.s45), [a2] "s"(k0.sef), [c0] "s"(k2.s01), [c1] "s"(k2.s23), [w] "s"(k2.s67), "s"(k0), "s"(k1), "s"(k2) : "m0", "scc", "memory");
        zsig(u32x4{0u, 0u, 0u, 0x00020000u}, voob, zpar);
        if (DBG1) { STAMP(1); WAITV(7); STAMP(2); WAITV(4); STAMP(3); WAITV(2); STAMP(4); WAITV(1); STAMP(10); WAITV(0); STAMP(11); }
    } else if (hwv == 8) {
        u32 vu = (u32)(idx * 64 + rr) * 1024 + (u32)xcd * 128 + (u32)((sp ^ ((rr >> 1) & 1)) * 16);
        if (UDLY) __builtin_amdgcn_s_sleep(UDLY);
        asm volatile("s_waitcnt lgkmcnt(0)\n\t"
                     "s_mov_b32 m0, %0\n\ts_nop 0\n\tglobal_load_lds_dwordx4 %1, %9\n\t"
                     "s_add_u32 m0, m0, 1024\n\ts_nop 0\n\tglobal_load_lds_dwordx4 %2, %9\n\t"
                     "s_add_u32 m0, m0, 1024\n\ts_nop 0\n\tglobal_load_lds_dwordx4 %3, %9\n\t"
                     "s_add_u32 m0, m0, 1024\n\ts_nop 0\n\tglobal_load_lds_dwordx4 %4, %9\n\t"
                     "s_add_u32 m0, m0, 1024\n\ts_nop 0\n\tglobal_load_lds_dwordx4 %5, %9\n\t"
                     "s_add_u32 m0, m0, 1024\n\ts_nop 0\n\tglobal_load_lds_dwordx4 %6, %9\n\t"
                     "s_add_u32 m0, m0, 1024\n\ts_nop 0\n\tglobal_load_lds_dwordx4 %7, %9\n\t"
                     "s_add_u32 m0, m0, 1024\n\ts_nop 0\n\tglobal_load_lds_dwordx4 %8, %9"
                     :: "s"(lds0 + L_UB + UB_PAD), "v"(vu), "v"((vu ^ 32) + 8192), "v"((vu ^ 64) + 16384), "v"((vu ^ 96) + 24576), "v"(vu + 32768), "v"((vu ^ 32) + 40960), "v"((vu ^ 64) + 49152), "v"((vu ^ 96) + 57344),
                        "s"(k1.s23), "s"(k0), "s"(k1), "s"(k2) : "m0", "scc", "memory");
    } else if (hwv >= 10) {
        const u32 lw = (u32)hwv & 1u ^ 1u;
        u32 vd0 = (u32)rr * 4096 + (u32)((sp ^ (rr >> 1)) * 16), vd1 = vd0 ^ 64, za = lds0 + L_ZA;
        u32 xs = (u32)c * 131072, bs = (u32)pb * 32768, r0 = ((u32)(ROTG == 8 ? (idx >> 2) + 2 * (idx & 3) : ROTG == 4 ? 2 * ((idx >> 2) + idx) + (xcd & 1) : 4 * ((idx >> 2) + idx) + (xcd & 3)) & 7u) * 128;
        asm volatile("" : "+s"(xs), "+s"(bs));
        u32 o0 = lw ? xs + 32768 : bs, o1 = lw ? xs + 65536 : bs, o2 = xs + lw * 98304, nrec = lw ? 2048 * 1024 : 512 * 1024;
        u32 l0 = lds0 + (lw ? 4096 : 81920 + 8192), lstep = lw ? 16384 : 8192, lwrap = lw ? 81920 : 49152, adv = lw ? 0 : 128;
        u32 lx = lds0 + lw * 12288, zl = lds0 + L_ZL + lw * 4;
        asm volatile("" : "+v"(vd0), "+v"(vd1), "+v"(za));
        asm volatile("" : "+s"(o0), "+s"(o1), "+s"(o2), "+s"(r0), "+s"(nrec), "+s"(l0), "+s"(lstep), "+s"(lwrap), "+s"(adv), "+s"(lx), "+s"(zl));
        u32x2 px, pa, pb_;
        asm volatile("s_waitcnt lgkmcnt(0)\n\ts_cmp_lg_u32 %3, 0\n\ts_mov_b64 %0, %4\n\ts_cselect_b64 %1, %4, %5\n\ts_cselect_b64 %2, %4, %6"
                     : "=&s"(px), "=&s"(pa), "=&s"(pb_) : "s"(lw), "s"(k0.s01), "s"(k0.s67), "s"(k0.s89), "s"(k0), "s"(k1), "s"(k2) : "scc", "memory");
        STAMP(11);
        if (LDLY) __builtin_amdgcn_s_sleep(LDLY);
        const u32x4 sx = {px.x, px.y & 0xFFFFu, 2048 * 1024, 0x00020000u}, s0 = {pa.x, pa.y & 0xFFFFu, nrec, 0x00020000u}, s1 = {pb_.x, pb_.y & 0xFFFFu, nrec, 0x00020000u};
        if (lw == 0) { dma4(s0, vd0, vd1, bs + r0, lds0 + 81920); dma4(s1, vd0, vd1, bs + r0, lds0 + 81920 + 4096); }
        u32x4 fl = {0u, 0u, 0u, 0u};
#pragma unroll 1
        for (int s = 0; s < 8; ++s) {
            const u32 ra = (r0 + adv) & 0x380u;
            if (s == 4 && lw == 0 && ABL < 2) { wait4(syncA, 1); if (ABL == 0) wait4(syncK, 1); wait1(syncU + 4, 1); }
            if (s >= 5 && ABL != 2) { if (rfl(fl.x | fl.y | fl.z | fl.w) != 0) waitz4(za); za += 16; if (s == 5) { lx -= 81920; l0 -= lwrap; } }
            if (s < 7 || lw) { dma4(s0, vd0, vd1, o0 + ra, l0); dma4(s1, vd0, vd1, o1 + ra, l0 + 4096); }
            fl = *(volatile const LAS u32x4*)(uintptr_t)za;
            dma4(sx, vd0, vd1, o2 + r0, lx);
            zsig(sx, voob, zl);
            r0 = (r0 + 128) & 0x380u; lx += 16384; l0 += lstep; zl += 8;
        }
        STAMP(3);
        if (ABL >= 2) { WAITV(0); STAMP(4); LATE_P(); STAMP_OUT(); return; }
        {
            LATE_P();
            const void* cp = lw ? p.in[6] : p.in[5];
            const u32x4 sc = mk_srd(cp, NOC ? 0 : 512 * 1024);
            u32 vc[4];
#pragma unroll
            for (int b = 0; b < 4; ++b) vc[b] = (((u32)rr * 1024 + (u32)((sp ^ (rr >> 1)) * 16)) ^ ((b & 1) * 64)) + b * 7168;
            waitz4(lds0 + L_ZA + 4 * 16);
#pragma unroll 1
            for (u32 i = 0; i < 8; ++i) {
                const u32 j = (0x52107463u >> (4 * i)) & 15u;
                if (i == CWI) { STAMP(4); waitz4(lds0 + L_ZA + 7 * 16); if (CW == 1) WAITV(0); if (CW == 2) SPINW(peek1(lds0 + L_CB2) == SENT, 1); if (CW == 3) wait4s(syncA, 4); if (CW == 4) wait4s(syncA, 3); STAMP(1); }
                const u32 so = (u32)db * 65536 + ((j & 4) | ((j + (u32)idx) & 3)) * 128, ld = lds0 + j * 16384 + lw * 8192 + (j >> 2) * 4096;
                dma4n(sc, vc, so, ld);
                dma4n(sc, vc, so + 32768, ld ^ 4096);
            }
            zsig(sc, voob, lds0 + L_ZL + 64 + lw * 4);
        }
        STAMP(5);
    }
    LATE_P();
    const rsrc_t ws = mk_rsrc(p.ws, WS_BYTES);
    const u32x4 wss = mk_srd(p.ws, WS_BYTES);
    if (ABL == 2 || (ABL == 3 && (wv < 4 || wv >= 8))) { WAITV(0); STAMP(1); STAMP_OUT(); return; }
    int wv2 = wv;
    asm volatile("" : "+s"(wv2));
    if (wv2 < 4) {
        const int uw = wv;
        if (uw == 3) {
            const u32 im0 = lds0 + L_UB + UB_PAD;
            __builtin_amdgcn_s_setprio(3);
            const u32 ra = (u32)(8 * (lane & 3) + (m16 >> 2)) * 128 + (u32)(((q4 >> 1) ^ (2 * (lane & 3) + ((m16 >> 3) & 1))) * 16) + (u32)(q4 & 1) * 8;
            WAITV(0);
            SPINW(peek1(zpar) != 0, 1);
            const u32 ta = get_tag(0), tb = tag2(ta);
            STAMP(1);
            u32x4 ut[8];
#pragma unroll
            for (int k = 0; k < 8; ++k) { const u32 a = (ra ^ ((k & 3) * 32)) + im0 + (k >> 2) * 4096; const u32x2 x = lds_tr(a), y = lds_tr(a + 512); ut[k] = u32x4{x.x, x.y, y.x, y.y}; }
#pragma unroll
            for (int k = 0; k < 8; ++k) { st128<0, true>(wss, ut[k], (u32)q4 * 16384 + (u32)m16 * 16, WS_UT + (u32)(16 * xcd + 4 * (k & 3)) * 16384 + (u32)(2 * idx + (k >> 2)) * 256); if (PACE1) __builtin_amdgcn_s_sleep(PACE1); }
            WAITV(0);
            __builtin_amdgcn_raw_buffer_store_b64(u32x2{ta, tb}, ws, lane ? OOB : 0u, WS_UFLAG + (u32)(xcd * 32 + idx) * FS, 16);
            FENCE();
            STAMP(2);
            SPINW(peekz2(lds0 + L_ZL + UPOLL * 8) != 0, 2);
            poll_flags(ws, lane < 32 ? WS_UFLAG + (u32)(xcd * 32 + lane) * FS : OOB, ta, tb);
            STAMP(3);
            {
                const u32 vt = (u32)(lane >> 1) * 64 + (u32)(lane & 1) * 16, so = WS_UT + (u32)(16 * xcd + (idx >> 1)) * 16384 + (u32)(idx & 1) * 32;
#pragma unroll
                for (int k = 0; k < 8; ++k)
                    asm volatile("s_mov_b32 m0, %0\n\ts_nop 0\n\tbuffer_load_dwordx4 %1, %2, %3 offen sc1 lds" :: "s"(im0 + k * 1024), "v"(vt), "s"(wss), "s"(so + k * 2048) : "m0", "memory");
                zsig(wss, voob, lds0 + L_ZL + 72);
            }
            STAMP(4);
        } else if (uw == 1) {
            u32 tw = 0, to = 0, tc = 0;
            if (TOUCH) {
                tw = __builtin_amdgcn_raw_buffer_load_b32(ws, lane < 9 * (1048576 / TOUCH) ? (u32)lane * TOUCH + 4096 : OOB, 0, 16);
                to = __builtin_amdgcn_raw_buffer_load_b32(mk_rsrc(p.out, 2048 * 4096), (u32)lane * TOUCH, 0, 16);
                tc = __builtin_amdgcn_raw_buffer_load_b32(mk_rsrc(p.in[5], 4), 0, 0, 0) + __builtin_amdgcn_raw_buffer_load_b32(mk_rsrc(p.in[6], 4), 0, 0, 0) + __builtin_amdgcn_raw_buffer_load_b32(mk_rsrc(p.in[8], 4), 0, 0, 0);
            }
            SPINW(peek1(zpar) != 0, 1);
#pragma unroll
            for (int i = 0; i < 3; ++i) pr[i] = lds_ld<u16>(lds0 + L_PAR + 3072 + i * 64 + g32 * 2);
            sig(syncU + 4, 1);
            cf L128, L256;
            {
                const float dt = rbf(__builtin_expf(bf2f(pr[2])));
                L128 = cexpk(dt * bf2f(pr[0]), dt * bf2f(pr[1]), 1.f);
#pragma unroll
                for (int i = 0; i < 7; ++i) L128 = csq(L128);
                L256 = csq(L128);
            }
            u32 vo[8];
            {
                const int cb0 = c - 1 - hh;
                const u32 vo0 = WS_E + (u32)(cb0 * 512 + pb * 32 + r32) * 16;
#pragma unroll
                for (int k = 0; k < 8; ++k) { vo[k] = 2 * k <= cb0 ? vo0 - (u32)k * 16384 : OOB; asm volatile("" : "+v"(vo[k])); }
            }
            asm volatile("" : "+v"(L128), "+v"(L256) : "v"(tw), "v"(to), "v"(tc));
            const u32 ta = get_tag(tagw), tb = tag2(ta);
            if (SPF) {
                SPINW(peekz2(lds0 + L_ZL + SPFW * 8) != 0, 2);
                const u32 off = (u32)db * 65536 + (u32)(idx & 3) * 16384 + 8192;
                u64 a0 = (u64)p.in[5] + off, a1 = (u64)p.in[6] + off;
                u32 dmy = 0;
#pragma unroll 1
                for (int i = 0; i < 16; ++i) {
                    asm volatile("s_load_dword %0, %1, 0x0\n\ts_load_dword %0, %1, 0x40\n\ts_load_dword %0, %1, 0x80\n\ts_load_dword %0, %1, 0xc0\n\ts_load_dword %0, %1, 0x100\n\ts_load_dword %0, %1, 0x140\n\ts_load_dword %0, %1, 0x180\n\ts_load_dword %0, %1, 0x1c0\n\t"
                                 "s_load_dword %0, %2, 0x0\n\ts_load_dword %0, %2, 0x40\n\ts_load_dword %0, %2, 0x80\n\ts_load_dword %0, %2, 0xc0\n\ts_load_dword %0, %2, 0x100\n\ts_load_dword %0, %2, 0x140\n\ts_load_dword %0, %2, 0x180\n\ts_load_dword %0, %2, 0x1c0"
                                 : "+s"(dmy) : "s"(a0), "s"(a1));
                    a0 += 512; a1 += 512;
                }
                asm volatile("s_waitcnt lgkmcnt(0)" : "+s"(dmy));
                STAMP(4);
            }
            u32x4 ut[8];
            SPINW(peek1(lds0 + L_CB1 + 512) == SENT, 1);
            if (EDLY) __builtin_amdgcn_s_sleep(EDLY);
#pragma unroll 1
            for (u32 spn = 0; spn < SPIN_CAP; ++spn) {
                FENCE();
#pragma unroll
                for (int k = 0; k < 8; ++k) ut[k] = __builtin_amdgcn_raw_buffer_load_b128(ws, vo[k], 0, PAUX);
                u32 bad = 0;
#pragma unroll
                for (int k = 0; k < 8; ++k) bad |= vo[k] != OOB ? ((ut[k].y ^ ta) | (ut[k].w ^ tb)) : 0u;
                if (__builtin_amdgcn_ballot_w64(bad != 0) == 0) break;
            }
            cf G = {u2f(ut[7].x), u2f(ut[7].z)}, G0, G1;
#pragma unroll
            for (int k = 6; k >= 0; --k) G = cfma(G, L256, cf{u2f(ut[k].x), u2f(ut[k].z)});
            swap_halves(G, G0, G1);
            lds_st<f32x2>(lds0 + L_CB2 + r32 * 8, cfma(G1, L128, G0));
            FENCE();
            STAMP(5);
        }
        __builtin_amdgcn_s_setprio(1);
        const int dsel = uw >> 1, half = uw & 1;
        const int dd = 64 * xcd + half * 32;
        const u32 ta = get_tag(0), tb = tag2(ta);
        const int sel = 2 * idx + dsel, prr = sel >> 4;
        const u32 tbase = (u32)(512 * prr + 64 * (sel & 3) + 4 * ((sel >> 2) & 3));
        u32 yv = (u32)r32 * 4096 + (u32)hh * 16, ovt[4];
#pragma unroll
        for (int r = 0; r < 4; ++r) { ovt[r] = (u32)(16 * hh + r) * 4096 + (u32)r32 * 4; asm volatile("" : "+v"(ovt[r])); }
        asm volatile("" : "+v"(yv));
        STAMP(6);
        if (ABL == 0) {
            STAMP(7);
            if (uw == 3) { wait4s(syncK, 5); if (YGATE) wait4s(syncA, YGATE); __builtin_amdgcn_s_sleep(YDLY); poll_flags<YSL>(ws, WS_YFLAG + (u32)((prr >> 1) * 512 + 64 * xcd + lane) * FS, ta, tb); zsig(wss, voob, lds0 + L_ZL + 76); }
            else SPINW(peek1(lds0 + L_ZL + 76) != 0, 2);
            __builtin_amdgcn_s_setprio(3);
            STAMP(8);
            const u32 so = WS_YT + (u32)dd * 4096 + (u32)sel * 64;
            u32x4 yy[2];
#pragma unroll
            for (int k = 0; k < 2; ++k) yy[k] = __builtin_amdgcn_raw_buffer_load_b128(ws, yv + k * 32, so, 16);
            const rsrc_t ro = mk_rsrc(p.out, 2048 * 4096);
#pragma unroll
            for (int k = 0; k < 2; ++k)
#pragma unroll
                for (int j = 0; j < 8; ++j) {
                    __builtin_amdgcn_raw_buffer_store_b32((j & 1) ? yy[k][j >> 1] & 0xFFFF0000u : yy[k][j >> 1] << 16, ro, ovt[j & 3], (tbase + 32 * k + 256 * (j >> 2)) * 4096 + (u32)(512 + dd) * 4, OAUXB);
                    if (PACE2) { FENCE(); __builtin_amdgcn_s_sleep(PACE2); FENCE(); } }
        }
        STAMP(9);
    } else if (__builtin_expect(wv2 < 8, 1)) {
        const int aw = wv - 4;
        __builtin_amdgcn_s_setprio(PRA);
        u32 fo[4], xo[4], xh[4];
#pragma unroll
        for (int k = 0; k < 4; ++k) { const u32 f = lds0 + (u32)r32 * 128 + (u32)(((2 * k + hh) ^ ((r32 >> 1) & 7)) * 16); fo[k] = f + 81920; xo[k] = f + aw * 4096; xh[k] = xo[k] + 65536; }
#pragma unroll
        for (int k = 0; k < 4; ++k) asm volatile("" : "+v"(fo[k]), "+v"(xo[k]), "+v"(xh[k]));
        u32 mysig = syncA + aw * 4, vsl = lds0 + L_ZL, c1r = lds0 + L_CB1 + r32 * 8, l16 = (u32)lane * 16;
        u32 c1w = c1r + aw * 256, evo = hh ? OOB : (u32)r32 * 16, fvo = lane ? OOB : 0u;
        asm volatile("" : "+v"(mysig), "+v"(vsl), "+v"(c1r), "+v"(c1w), "+v"(evo), "+v"(fvo), "+v"(l16));
        u32x4 idm[2];
#pragma unroll
        for (int s = 0; s < 2; ++s) {
#pragma unroll
            for (int j = 0; j < 4; ++j) { const int dlt = r32 - 16 * hh - 8 * s - 2 * j; idm[s][j] = dlt == 0 ? 0x3F80u : dlt == 1 ? 0x3F800000u : 0u; }
            asm volatile("" : "+v"(idm[s]));
        }
        STAMP(1);
        cf P[4], L8, L16, L32, coef;
        f32x2 cqr[2], cqi[2];
        u32 ta, tb;
        f32x16 accR = {}, accI = {};
        {
            bf16x8 fa[4], fr[4], fi[4];
            auto rd = [&](int n) INL { const int s = n >> 2, k = n & 3;
                fa[k] = lds_ld<bf16x8>(s % 5 < 4 ? xo[k] + (s % 5) * 16384 : xh[k]);
                fr[k] = lds_ld<bf16x8>(fo[k] + (s % 6) * 8192);
                fi[k] = lds_ld<bf16x8>(fo[k] + (s % 6) * 8192 + 4096); };
            {
                    SPINW(peek1(zpar) != 0, 1);
                    asm volatile("" :: "v"(cpf));
#pragma unroll
                    for (int i = 0; i < 3; ++i) pr[i] = lds_ld<u16>(lds0 + L_PAR + 3072 + i * 64 + g32 * 2);
                    sig(mysig, 1);
                    ta = get_tag(0); tb = tag2(ta);
                    const float a_lr = bf2f(pr[0]), a_li = bf2f(pr[1]);
                    const float dt = rbf(__builtin_expf(bf2f(pr[2])));
                    P[0] = cexpk(dt * a_lr, dt * a_li, 1.f);
                    P[1] = csq(P[0]); P[2] = cmul(P[0], P[1]); P[3] = csq(P[1]);
                    L8 = csq(P[3]); L16 = csq(L8); L32 = csq(L16);
                    const float nr = P[0].x - 1.f, ni = P[0].y, inv = 2.f / (a_lr * a_lr + a_li * a_li);
                    coef = cf{(nr * a_lr + ni * a_li) * inv, (ni * a_lr - nr * a_li) * inv};
                    cf Q = hh ? cmul(coef, L16) : coef;
                    { const cf Q1 = cmul(Q, L32); Q = (aw & 1) ? Q1 : Q; }
                    { const cf Q2 = cmul(Q, csq(L32)); Q = (aw & 2) ? Q2 : Q; }
                    const cf q1 = cmul(Q, P[3]), q2 = cmul(Q, L8), q3 = cmul(q1, L8);
                    cqr[0] = f32x2{Q.x, q1.x}; cqi[0] = f32x2{Q.y, q1.y}; cqr[1] = f32x2{q2.x, q3.x}; cqi[1] = f32x2{q2.y, q3.y};
                    asm volatile("" : "+v"(P[0]), "+v"(P[1]), "+v"(P[2]), "+v"(P[3]), "+v"(L8), "+v"(L16), "+v"(L32), "+v"(coef), "+v"(cqr[0]), "+v"(cqi[0]), "+v"(cqr[1]), "+v"(cqi[1]));
                    SBAR();
                }
            SPINW(peekz2(vsl + FIRSTW * 8) != 0, 0);
            STAMP(10);
            u32x2 fl = *(volatile const LAS u32x2*)(uintptr_t)(vsl + 8);
            rd(0); rd(1); rd(2);
            SBAR();
            sfor<0, 32>([&](auto nn) INL {
                constexpr int n = decltype(nn)::v, s = n >> 2, k = n & 3;
                if constexpr (DBG1 && (n == 3 || n == 4 || n == 8 || n == 12 || n == 13 || n == 16 || n == 20 || n == 24)) STAMP(n == 3 ? 4 : n == 4 ? 5 : n == 8 ? 6 : n == 12 ? 7 : n == 13 ? 8 : n == 16 ? 9 : n == 20 ? 11 : 12);
                if constexpr (k == 1 && s < 7) {
                    if (rfl(fl.x | fl.y) != 0) SPINW(peekz2(vsl + (s + 1) * 8) != 0, 0);
                    FENCE();
                }
                if constexpr (k == 2 && s < 6) fl = *(volatile const LAS u32x2*)(uintptr_t)(vsl + (s + 2) * 8);
                accR = MFMA32(fa[k], fr[k], accR);
                SBAR();
                if constexpr (n + 3 < 32) rd(n + 3);
                if constexpr (k == 3 && (s < 3 || s == 4 || s == 7)) zsig(wss, voob, lds0 + L_ZA + s * 16 + aw * 4);
                accI = MFMA32(fa[k], fi[k], accI);
                SBAR();
            });
        }
        __builtin_amdgcn_s_setprio(3);
        STAMP(3);
        if (ABL == 3) { asm volatile("" :: "v"(accR), "v"(accI)); STAMP_OUT(); return; }
        f32x2 hr[8], hi[8];
#pragma unroll
        for (int i = 0; i < 8; ++i) { hr[i] = f32x2{accR[2 * i], accR[2 * i + 1]}; hi[i] = f32x2{accI[2 * i], accI[2 * i + 1]}; }
#pragma unroll
        for (int i = 2; i < 8; ++i) {
            f32x2 nr = fma2(-P[0].y, hi[i - 2], fma2(P[0].x, hr[i - 2], hr[i]));
            f32x2 ni = fma2(P[0].y, hr[i - 2], fma2(P[0].x, hi[i - 2], hi[i]));
            hr[i] = nr; hi[i] = ni;
        }
        cf e0, e1, pre = {0.f, 0.f};
        {
            const f32x2 zr = fma2(-L8.y, hi[6], fma2(L8.x, hr[6], hr[7])), zi = fma2(L8.y, hr[6], fma2(L8.x, hi[6], hi[7]));
            const cf e16 = {__builtin_fmaf(-P[3].y, zi.x, __builtin_fmaf(P[3].x, zr.x, zr.y)), __builtin_fmaf(P[3].y, zr.x, __builtin_fmaf(P[3].x, zi.x, zi.y))};
            swap_halves(e16, e0, e1);
        }
        const cf e32 = cfma(e0, L16, e1);
        if (aw < 3) { lds_st<f32x2>(c1w, e32); FENCE(); }
        if (aw > 0) {
            cf w0, w1, w2;
#pragma unroll 1
            for (u32 spn = 0; spn < (1u << 22); ++spn) {
                asm volatile("ds_read_b64 %0, %3\n\tds_read_b64 %1, %3 offset:256\n\tds_read_b64 %2, %3 offset:512\n\ts_waitcnt lgkmcnt(0)" : "=&v"(w0), "=&v"(w1), "=&v"(w2) : "v"(c1r) : "memory");
                const u32 a = f2u(w0.x), b = f2u(w1.x), cc = f2u(w2.x);
                const u32 mx = a > b ? (a > cc ? a : cc) : (b > cc ? b : cc);
                if (__builtin_amdgcn_ballot_w64(mx == SENT) == 0) break;
            }
            const cf p2 = cfma(w0, L32, w1), p3 = cfma(p2, L32, w2);
            if (aw == 3 && c < 15) { const cf E = cfma(p3, L32, e32); st128<0, true>(wss, u32x4{f2u(E.x), ta, f2u(E.y), tb}, evo, WS_E + (u32)(c * 512 + pb * 32) * 16); }
            pre = aw == 1 ? w0 : (aw == 2 ? p2 : p3);
        }
        sig(mysig, 3);
        __builtin_amdgcn_s_setprio(PRSH);
        STAMP(4);
        const int th = aw & 1, kh = aw >> 1;
        const int t0 = tb64 * 64 + th * 32, dcol = db * 64 + kh * 32, t32 = tb64 * 2 + th;
        u32 xk[16], dsk = 0;
        {
            const u32x4 rx = mk_srd(p.in[0], NOSK ? 0 : 2048 * 1024), rd = mk_srd(p.in[8], 1024);
            const u32 vx = (u32)hh * 4096 + (u32)r32 * 2;
#pragma unroll
            for (int r = 0; r < 16; ++r) xk[r] = 0;
#pragma unroll
            for (int k = 0; k < 4; ++k)
                asm volatile("buffer_load_short_d16_hi %0, %4, %5, %6 offen\n\tbuffer_load_short_d16_hi %1, %4, %5, %6 offen offset:1024\n\t"
                             "buffer_load_short_d16_hi %2, %4, %5, %6 offen offset:2048\n\tbuffer_load_short_d16_hi %3, %4, %5, %6 offen offset:3072"
                             : "+v"(xk[4 * k]), "+v"(xk[4 * k + 1]), "+v"(xk[4 * k + 2]), "+v"(xk[4 * k + 3]) : "v"(vx), "s"(rx), "s"((u32)t0 * 1024 + (u32)dcol * 2 + k * 8192) : "memory");
            asm volatile("buffer_load_short_d16_hi %0, %1, %2, %3 offen" : "+v"(dsk) : "v"((u32)r32 * 2), "s"(rd), "s"((u32)dcol * 2) : "memory");
        }
        SBAR();
        f32x2 cpr[2], cpi[2];
        {
            { const cf pre1 = cfma(pre, L16, e0); pre = hh ? pre1 : pre; }
            cf s[4];
            s[0] = pre;
            s[1] = cfma(s[0], P[3], cf{hr[6].x, hi[6].x}); s[2] = cfma(s[1], P[3], cf{hr[6].y, hi[6].y}); s[3] = cfma(s[2], P[3], cf{hr[7].x, hi[7].x});
#pragma unroll
            for (int a = 0; a < 4; ++a) s[a] = cmul(s[a], coef);
#pragma unroll
            for (int u = 0; u < 2; ++u) { cpr[u] = f32x2{s[2 * u].x, s[2 * u + 1].x}; cpi[u] = f32x2{s[2 * u].y, s[2 * u + 1].y}; asm volatile("" : "+v"(cpr[u]), "+v"(cpi[u])); }
        }
#pragma unroll
        for (int i = 0; i < 8; ++i) {
            f32x2 nr = fma2(-coef.y, hi[i], coef.x * hr[i]);
            f32x2 ni = fma2(-coef.y, hr[i], -coef.x * hi[i]);
            hr[i] = nr; hi[i] = ni;
            asm volatile("" : "+v"(hr[i]), "+v"(hi[i]));
        }
        u32 ca[4], ov[4];
#pragma unroll
        for (int k = 0; k < 4; ++k) { ca[k] = lds0 + (u32)r32 * 128 + (u32)(((4 * (k >> 1) + 2 * hh + (k & 1)) ^ ((r32 >> 1) & 7)) * 16) + (u32)kh * 65536; ov[k] = (u32)hh * 16384 + (u32)r32 * 4 + k * 4096; asm volatile("" : "+v"(ca[k]), "+v"(ov[k])); }
        u32 xb = lds0 + L_S0 + (u32)aw * S0_SZ + l16, xp = lds0 + L_S0 + (u32)(aw ^ 2) * S0_SZ + l16;
        u32 pvo = lane < 8 ? WS_CS + (u32)(t32 * 16 + kh * 8 + lane) * 512 + 508 : OOB, l8 = (u32)lane * 8;
        asm volatile("" : "+v"(xb), "+v"(xp), "+v"(pvo), "+v"(l8));
        STAMP(2);
        {
            cf H;
#pragma unroll 1
            for (u32 spn = 0; spn < (1u << 24); ++spn) {
                asm volatile("ds_read_b64 %0, %1 offset:768\n\ts_waitcnt lgkmcnt(0)" : "=v"(H) : "v"(c1r) : "memory");
                if (__builtin_amdgcn_ballot_w64(f2u(H.x) == SENT) == 0) break;
            }
            __builtin_amdgcn_s_setprio(3);
            asm volatile("s_waitcnt vmcnt(0)" : "+v"(dsk), "+v"(xk[0]), "+v"(xk[1]), "+v"(xk[2]), "+v"(xk[3]), "+v"(xk[4]), "+v"(xk[5]), "+v"(xk[6]), "+v"(xk[7]), "+v"(xk[8]), "+v"(xk[9]), "+v"(xk[10]), "+v"(xk[11]), "+v"(xk[12]), "+v"(xk[13]), "+v"(xk[14]), "+v"(xk[15]));
            STAMP(5);
            f32x2 gr[2], gi[2];
#pragma unroll
            for (int u = 0; u < 2; ++u) { gr[u] = fma2(-H.y, cqi[u], fma2(H.x, cqr[u], cpr[u])); gi[u] = fma2(H.x, cqi[u], fma2(H.y, cqr[u], cpi[u])); }
#pragma unroll
            for (int i = 0; i < 8; ++i) hr[i] = fma2(-P[i >> 1].y, gi[i & 1], fma2(P[i >> 1].x, gr[i & 1], hr[i]));
            const u32 hs = WS_H + (u32)((c * 4 + aw) * 16 + pb) * 4096;
            f32x16 zr = MFMA32(__builtin_bit_cast(bf16x8, u32x4{pkbf(hr[0].x, hr[2].x), pkbf(hr[4].x, hr[6].x), pkbf(hr[0].y, hr[2].y), pkbf(hr[4].y, hr[6].y)}), __builtin_bit_cast(bf16x8, idm[0]), f32x16{});
            zr = MFMA32(__builtin_bit_cast(bf16x8, u32x4{pkbf(hr[1].x, hr[3].x), pkbf(hr[5].x, hr[7].x), pkbf(hr[1].y, hr[3].y), pkbf(hr[5].y, hr[7].y)}), __builtin_bit_cast(bf16x8, idm[1]), zr);
            SBAR();
#pragma unroll
            for (int i = 0; i < 8; ++i) hi[i] = fma2(-P[i >> 1].y, gr[i & 1], fma2(-P[i >> 1].x, gi[i & 1], hi[i]));
            f32x16 zi = MFMA32(__builtin_bit_cast(bf16x8, u32x4{pkbf(hi[0].x, hi[2].x), pkbf(hi[4].x, hi[6].x), pkbf(hi[0].y, hi[2].y), pkbf(hi[4].y, hi[6].y)}), __builtin_bit_cast(bf16x8, idm[0]), f32x16{});
            zi = MFMA32(__builtin_bit_cast(bf16x8, u32x4{pkbf(hi[1].x, hi[3].x), pkbf(hi[5].x, hi[7].x), pkbf(hi[1].y, hi[3].y), pkbf(hi[5].y, hi[7].y)}), __builtin_bit_cast(bf16x8, idm[1]), zi);
            SBAR();
#define PK2(z, a) __builtin_amdgcn_perm(f2u(z[4 + (a)]), f2u(z[(a)]), 0x07060302u), __builtin_amdgcn_perm(f2u(z[12 + (a)]), f2u(z[8 + (a)]), 0x07060302u)
            const u32x4 o0 = {PK2(zr, 0), PK2(zr, 1)}, o1 = {PK2(zr, 2), PK2(zr, 3)};
            st128<0, true>(wss, o0, l16, hs);
            st128<1024, true>(wss, o1, l16, hs);
            u32 cs = ADD4(ADD4(tb, o0), o1);
            const u32x4 o2 = {PK2(zi, 0), PK2(zi, 1)}, o3 = {PK2(zi, 2), PK2(zi, 3)};
            st128<2048, true>(wss, o2, l16, hs);
            st128<3072, true>(wss, o3, l16, hs);
            cs = ADD4(ADD4(cs, o2), o3);
            __builtin_amdgcn_raw_buffer_store_b64(u32x2{cs, ta}, ws, l8, WS_CS + (u32)((c * 4 + aw) * 16 + pb) * 512, 16);
            FENCE();
            sig(mysig, 4);
            STAMP(13);
        }
        f32x16 sk;
        {
            const float dk = u2f(dsk);
#pragma unroll
            for (int r = 0; r < 16; r += 2) { const u32 w = pkbf(u2f(xk[r]) * dk, u2f(xk[r + 1]) * dk); sk[r] = u2f(w << 16); sk[r + 1] = u2f(w & 0xFFFF0000u); }
        }
        asm volatile("" : "+v"(sk));
        u32 hso[4], rso[4], cof[4];
#pragma unroll
        for (int jj = 0; jj < 4; ++jj) { const u32 cg = (u32)(4 * kh + ((jj + db) & 3));
            hso[jj] = WS_H + (u32)t32 * 65536 + cg * 8192; rso[jj] = WS_CS + (u32)t32 * 8192 + cg * 1024; cof[jj] = (u32)((jj + db - idx) & 3) * 16384;
            asm volatile("" : "+s"(hso[jj]), "+s"(rso[jj]), "+s"(cof[jj])); }
        u32 l16b = l16 + 4096;
        asm volatile("" : "+v"(l16b));
        if (HDLY) __builtin_amdgcn_s_sleep(HDLY);
        SPINW(peekz2(vsl + 64) != 0, 0);
        STAMP(12);
        if (!SPEC) {
#pragma unroll 1
        for (u32 spn = 0; spn < SPIN_CAP; ++spn) {
            FENCE();
            const u32 v = __builtin_amdgcn_raw_buffer_load_b32(ws, pvo, 0, PAUX);
            pvo = v == ta ? OOB : pvo;
            if (__builtin_amdgcn_ballot_w64(pvo != OOB) == 0) break;
            if (HSL) __builtin_amdgcn_s_sleep(HSL);
        }
        }
        STAMP(7);
        u32x4 kfl;
        f32x16 y0, y1;
#pragma unroll 1
        for (u32 att = 0; att < SPIN_CAP; ++att) {
            asm volatile("" : "+v"(ca[0]), "+v"(ca[1]), "+v"(ca[2]), "+v"(ca[3]), "+v"(l16), "+v"(l16b), "+v"(l8) :: "memory");
            bf16x8 cb0[4], cb1[4];
            u32x4 hf[16];
            u32 mk = 0, sm = tb, rec[8];
            u32 cab[4];
            auto cad = [&](int f) INL { const int jj = f >> 3, a = (f >> 2) & 1, s = f & 3, k = 2 * a + (s & 1);
                if (s < 2) { cab[k] = ca[k] + cof[jj]; asm volatile("" : "+v"(cab[k])); }
                return cab[k] + (s >> 1) * 8192; };
            auto ldh = [&](int f) INL { return __builtin_amdgcn_raw_buffer_load_b128(ws, ((f & 4) ? l16b : l16) + (f & 3) * 1024, hso[f >> 3], 16); };
            auto ldr = [&](int g) INL { return __builtin_amdgcn_raw_buffer_load_b32(ws, l8 + (g & 1) * 512, rso[g >> 1], 16); };
            u32 pv0 = 0;
            if (SPEC) { pv0 = __builtin_amdgcn_raw_buffer_load_b32(ws, pvo, 0, PAUX); SBAR(); }
#pragma unroll
            for (int f = 0; f < HWIN; ++f) { if ((f & 3) == 3) rec[f >> 2] = ldr(f >> 2); hf[f] = ldh(f); SBAR(); }
#pragma unroll
            for (int f = 0; f < 3; ++f) { const u32 ad = cad(f); cb0[f] = lds_ld<bf16x8>(ad); cb1[f] = lds_ld<bf16x8>(ad + 4096); }
            SBAR();
            if (SPEC) { pvo = pv0 == ta ? OOB : pvo; if (__builtin_amdgcn_ballot_w64(pvo != OOB) != 0) {
#pragma unroll 1
        for (u32 spn = 0; spn < SPIN_CAP; ++spn) {
            FENCE();
            const u32 v = __builtin_amdgcn_raw_buffer_load_b32(ws, pvo, 0, PAUX);
            pvo = v == ta ? OOB : pvo;
            if (__builtin_amdgcn_ballot_w64(pvo != OOB) == 0) break;
            if (HSL) __builtin_amdgcn_s_sleep(HSL);
        }

                continue; } }
#pragma unroll
            for (int f = 0; f < 32; ++f) {
                if (f + HWIN < 32) { if (((f + HWIN) & 3) == 3) rec[(f + HWIN) >> 2] = ldr((f + HWIN) >> 2); hf[(f + HWIN) & 15] = ldh(f + HWIN); SBAR(); }
                const u32x4 hv = hf[f & 15];
                bf16x8 av = __builtin_bit_cast(bf16x8, hv);
                y0 = MFMA32(av, cb0[f & 3], f ? y0 : f32x16{});
                SBAR();
                if (f + 3 < 32) { const u32 ad = cad(f + 3); cb0[(f + 3) & 3] = lds_ld<bf16x8>(ad); cb1[(f + 3) & 3] = lds_ld<bf16x8>(ad + 4096); }
                SBAR();
                y1 = MFMA32(av, cb1[f & 3], f ? y1 : f32x16{});
                SBAR();
                sm = ADD4(sm, hv);
                if ((f & 3) == 3) { mk |= sm ^ rec[f >> 2]; sm = tb; asm volatile("" : "+v"(mk)); } else asm volatile("" : "+v"(sm));
                SBAR();
                if (DBG2 && f == 0) STAMP(6);
                if (DBG2 && f == 9) STAMP(11);
                if (DBG2 && f == 19) STAMP(15);
                if (f == SIG5) { sig(mysig, 5); SBAR(); }
                if (f == 26) { kfl = *(volatile const LAS u32x4*)(uintptr_t)syncK; SBAR(); }
            }
            if (__builtin_amdgcn_ballot_w64(mk != 0) == 0) break;
        }
        STAMP(8);
        {
            if (ABL == 0 && rfl(umin(umin(kfl.x, kfl.y), umin(kfl.z, kfl.w))) < 5) wait4(syncK, 5);
#pragma unroll
            for (int k = 0; k < 4; ++k) lds_st<f32x4>(xb + k * 1024, f32x4{y1[4 * k], y1[4 * k + 1], y1[4 * k + 2], y1[4 * k + 3]});
            sig(mysig, 6);
            y0 += sk;
            asm volatile("" : "+v"(y0));
            wait1(syncA + (aw ^ 2) * 4, 6);
            const rsrc_t ro = mk_rsrc(p.out, 2048 * 4096);
            f32x4 o[4];
#pragma unroll
            for (int k = 0; k < 4; ++k) o[k] = lds_ld<f32x4>(xp + k * 1024);
#pragma unroll
            for (int k = 0; k < 4; ++k) {
#pragma unroll
                for (int j = 0; j < 4; ++j)
                    __builtin_amdgcn_raw_buffer_store_b32(f2u(y0[4 * k + j] + o[k][j]), ro, ov[j], (u32)(t0 + 8 * k) * 4096 + (u32)dcol * 4, OAUX);
            }
            if (tb64 == 31 && db == 0 && aw == 0) __builtin_amdgcn_raw_buffer_store_b32(ta, ws, fvo, WS_W, 16);
        }
        STAMP(9);
    } else {
        const int bw = wv - 8, dsel = bw >> 1, half = bw & 1;
        if (ABL >= 1) return;
        __builtin_amdgcn_s_setprio(PRK);
        const int d = dK;
        const u32 s0 = lds0 + L_S0 + bw * S0_SZ, kc = lds0 + L_KC + dsel * KC_SZ, ub = lds0 + L_UB;
        const u32 mysig = syncK + bw * 4, psig = syncK + (bw ^ 1) * 4;
        lds_st<u32>(kc - 48 + umin(lane, 11) * 4, 0u);
        u32 ga[20];
        {
            const int lag0 = 256 * rr + 128 * half + 127 - 18 * sp;
            const float f0 = (float)lag0;
            const int l2 = (int)kc + 2 * lag0, cap = (int)kc + 256, cb = (int)kc - 2 * (0x4300 - 128);
#pragma unroll
            for (int e = 0; e < 20; e += 2) {
                const int w = (int)pkbf(f0 - (float)e, f0 - (float)(e + 1));
                const int x0 = ((w << 16) >> 15) + cb, x1 = ((w >> 16) << 1) + cb, y0 = l2 - 2 * e, y1 = l2 - 2 * e - 2;
                asm("v_med3_i32 %0, %1, %2, %3" : "=v"(ga[e]) : "v"(x0), "v"(y0), "s"(cap));
                asm("v_med3_i32 %0, %1, %2, %3" : "=v"(ga[e + 1]) : "v"(x1), "v"(y1), "s"(cap));
            }
#pragma unroll
            for (int e = 0; e < 19; ++e) asm volatile("" : "+v"(ga[e]));
        }
        u32 wa = s0 + lane * 4, ra = s0 + (u32)m16 * 288 + (u32)q4 * 16, xa = s0 + lane * 16, xp = lds0 + L_S0 + (bw ^ 1) * S0_SZ + lane * 16, cpa = s0 + 36 * lane;
        u32 k0a = kc + 2 * (128 * (q4 >> 1) + 4 * (q4 & 1) + 8 * m16), k1a = kc + 2 * (q4 < 2 ? 256 + 64 * q4 + 4 * m16 : 384 + 64 * (q4 - 2) + 2 * m16), k1b = k1a + (q4 < 2 ? 4 : 64);
        u32 k2a = kc + 2 * (512 + (q4 == 3 ? 144 : 64 * q4) + m16), para = lds0 + L_PAR + bw * 128 + lane * 2, vms = mysig, vps = psig;
        u32 bb = ub + 512 - 512 * half + 64 * m16 + 32 * q4 + 16 * dsel, ab = s0 + 4 * (592 * (1 - (m16 & 1)) + ((110 - m16 + (m16 & 1)) >> 1) + 4 * q4 - 48);
        asm volatile("" : "+v"(wa), "+v"(ra), "+v"(xa), "+v"(xp), "+v"(cpa), "+v"(k0a), "+v"(k1a), "+v"(k1b), "+v"(k2a), "+v"(para), "+v"(vms), "+v"(vps), "+v"(bb), "+v"(ab));
        STAMP(1);
        SPINW(peek1(zpar) != 0, 1);
#pragma unroll
        for (int i = 0; i < 6; ++i) pr[i] = lds_ld<u16>(para + i * 512);
        pr[6] = lds_ld<u16>(lds0 + L_PAR + 3264 + dsel * 2); wv0 = lds_ld<u16>(lds0 + L_PAR + 3268 + dsel * 2);
        sig(vms, 1);
        const float dt = rbf(__builtin_expf(bf2f(pr[6]))), dsk0 = lane == 0 ? bf2f(wv0) : 0.f;
        STAMP(2);
        f32x4 ka[3];
        {
            const float zr = dt * bf2f(pr[0]), zi = dt * bf2f(pr[1]);
            const cf db_ = {dt * bf2f(pr[2]), -dt * bf2f(pr[3])}, cc = {bf2f(pr[4]), -bf2f(pr[5])};
            const cf E1 = cexpk(zr, zi, 1.f), E8c = cexpk(zr, -zi, 8.f), E128 = cexpk(zr, zi, 128.f);
            cf V[16], R[8], W[17];
            { const cf E16 = csq(E8c), E32 = csq(E16), E64 = csq(E32);
              V[0] = cmul(cc, db_); V[1] = cmul(V[0], E8c);
#pragma unroll
              for (int j = 0; j < 2; ++j) V[2 + j] = cmul(V[j], E16);
#pragma unroll
              for (int j = 0; j < 4; ++j) V[4 + j] = cmul(V[j], E32);
#pragma unroll
              for (int j = 0; j < 8; ++j) V[8 + j] = cmul(V[j], E64); }
            R[0] = cf{1.f, 0.f}; R[1] = E1; R[2] = csq(E1); R[3] = cmul(R[1], R[2]); R[4] = csq(R[2]);
#pragma unroll
            for (int r = 1; r < 4; ++r) R[4 + r] = cmul(R[r], R[4]);
            W[1] = E128; W[2] = csq(W[1]); W[3] = cmul(W[1], W[2]); W[4] = csq(W[2]);
#pragma unroll
            for (int k = 1; k <= 4; ++k) W[4 + k] = cmul(W[k], W[4]);
#pragma unroll
            for (int k = 1; k <= 8; ++k) W[8 + k] = cmul(W[k], W[8]);
            u32 pv[16], p0[16], p1[16], p2[9];
#pragma unroll
            for (int j = 0; j < 16; ++j) pv[j] = pkc(V[j]);
#pragma unroll
            for (int r = 0; r < 8; ++r) { p0[r] = pkc(R[r]); p0[8 + r] = pkc(r ? cmul(R[r], W[1]) : W[1]); }
#pragma unroll
            for (int k = 0; k < 2; ++k)
#pragma unroll
                for (int r = 0; r < 4; ++r) p1[4 * k + r] = pkc(r ? cmul(R[2 * r], W[2 + k]) : W[2 + k]);
#pragma unroll
            for (int k = 0; k < 4; ++k) { p1[8 + 2 * k] = pkc(W[4 + k]); p1[9 + 2 * k] = pkc(cmul(W[4 + k], R[4])); }
#pragma unroll
            for (int k = 0; k < 9; ++k) p2[k] = pkc(W[8 + k]);
            if (KGATE >= 2) {
#pragma unroll
                for (int j = 0; j < 16; ++j) asm volatile("" : "+v"(pv[j]), "+v"(p0[j]), "+v"(p1[j]), "+v"(p2[j < 9 ? j : 0]));
                STAMP(8);
                if (SPF2) {
                    SPINW(peekz2(lds0 + L_ZL + SPFW * 8) != 0, 2);
                    u64 a0 = (u64)(half ? p.in[6] : p.in[5]) + (u32)db * 65536 + (u32)dsel * 32768 + (4u | ((6u + (u32)idx) & 3u)) * 128;
                    u32 dmy = 0;
#pragma unroll 1
                    for (int i = 0; i < SPF2; ++i) {
                        asm volatile("s_load_dword %0, %1, 0x0\n\ts_load_dword %0, %1, 0x40\n\ts_load_dword %0, %1, 0x400\n\ts_load_dword %0, %1, 0x440\n\ts_load_dword %0, %1, 0x800\n\ts_load_dword %0, %1, 0x840\n\ts_load_dword %0, %1, 0xc00\n\ts_load_dword %0, %1, 0xc40\n\t"
                                     "s_load_dword %0, %1, 0x1000\n\ts_load_dword %0, %1, 0x1040\n\ts_load_dword %0, %1, 0x1400\n\ts_load_dword %0, %1, 0x1440\n\ts_load_dword %0, %1, 0x1800\n\ts_load_dword %0, %1, 0x1840\n\ts_load_dword %0, %1, 0x1c00\n\ts_load_dword %0, %1, 0x1c40"
                                     : "+s"(dmy) : "s"(a0));
                        a0 += 8192;
                    }
                    asm volatile("s_waitcnt lgkmcnt(0)" : "+s"(dmy));
                    STAMP(9);
                }
                SPINW(peekz2(lds0 + L_ZL + 56) != 0, 2);
            }
#pragma unroll
            for (int j = 0; j < 16; ++j) lds_st<u32>(wa + j * 288, pv[j]);
            FENCE();
            bf16x8 vf[4], uf[4];
#pragma unroll
            for (int s = 0; s < 4; ++s) vf[s] = lds_ld<bf16x8>(ra + s * 64);
            FENCE();
#pragma unroll
            for (int j = 0; j < 16; ++j) lds_st<u32>(wa + j * 288, p0[j]);
            FENCE();
#pragma unroll
            for (int s = 0; s < 4; ++s) uf[s] = lds_ld<bf16x8>(ra + s * 64);
            FENCE();
#pragma unroll
            for (int j = 0; j < 16; ++j) lds_st<u32>(wa + j * 288, p1[j]);
            FENCE();
            ka[0] = f32x4{};
#pragma unroll
            for (int s = 0; s < 4; ++s) ka[0] = MFMA16(uf[s], vf[s], ka[0]);
#pragma unroll
            for (int s = 0; s < 4; ++s) uf[s] = lds_ld<bf16x8>(ra + s * 64);
            FENCE();
#pragma unroll
            for (int k = 0; k < 9; ++k) lds_st<u32>(wa + k * 288, p2[k]);
            FENCE();
            ka[1] = f32x4{};
#pragma unroll
            for (int s = 0; s < 4; ++s) ka[1] = MFMA16(uf[s], vf[s], ka[1]);
#pragma unroll
            for (int s = 0; s < 4; ++s) uf[s] = lds_ld<bf16x8>(ra + s * 64);
            FENCE();
            ka[2] = f32x4{};
#pragma unroll
            for (int s = 0; s < 4; ++s) ka[2] = MFMA16(uf[s], vf[s], ka[2]);
        }
        STAMP(3);
#pragma unroll
        for (int t = 0; t < 3; ++t) lds_st<f32x4>(xa + t * 1024, ka[t]);
        sig(vms, 2);
        wait1(vps, 2);
        {
#pragma unroll
            for (int t = 0; t < 3; ++t) ka[t] += lds_ld<f32x4>(xp + t * 1024);
            sig(vms, 3);
            ka[0][0] += dsk0;
            lds_st<u32x2>(k0a, u32x2{pkbf(ka[0][0], ka[0][1]), pkbf(ka[0][2], ka[0][3])});
            lds_st<u32>(k1a, pkbf(ka[1][0], ka[1][1]));
            lds_st<u32>(k1b, pkbf(ka[1][2], ka[1][3]));
            const u32 w01 = pkbf(ka[2][0], ka[2][1]), w23 = pkbf(ka[2][2], ka[2][3]);
            lds_st<u16>(k2a, (u16)w01); lds_st<u16>(k2a + 32, (u16)(w01 >> 16)); lds_st<u16>(k2a + 64, (u16)w23); lds_st<u16>(k2a + 96, (u16)(w23 >> 16));
            FENCE();
            u32 gv[19];
#pragma unroll
            for (int e = 0; e < 19; ++e) gv[e] = lds_ld<u16>(ga[e]);
            wait1(vps, 3);
#pragma unroll
            for (int k = 0; k < 9; ++k) {
                lds_st<u32>(cpa + 4 * k, gv[2 * k] | (gv[2 * k + 1] << 16));
                lds_st<u32>(cpa + 2368 + 4 * k, gv[2 * k + 1] | (gv[2 * k + 2] << 16));
            }
            FENCE();
        }
        const u32 ta = get_tag(0), tb = tag2(ta);
        STAMP(4);
        SPINW(peek1(lds0 + L_ZL + 72) != 0, 2);
        if (KCG) wait4(syncA, KCG);
        __builtin_amdgcn_s_setprio(PRCV);
        STAMP(5);
        f32x4 acc[8];
#pragma unroll
        for (int i = 0; i < 8; ++i) acc[i] = f32x4{};
        {
            bf16x8 bfr[2][8], afr[4];
            auto lda = [&](int n) { const int r = n >> 3, i = n & 7; const u32 o = ab + 4 * (72 * i + 16 * (3 - r));
                u32x4 av = {lds_ld<u32>(o), lds_ld<u32>(o + 4), lds_ld<u32>(o + 8), lds_ld<u32>(o + 12)}; return __builtin_bit_cast(bf16x8, av); };
            afr[0] = lda(0);
#pragma unroll
            for (int m = 0; m < 8; ++m) bfr[0][m] = lds_ld<bf16x8>(bb + 1024 * m + 384);
            afr[1] = lda(1); afr[2] = lda(2);
            SBAR();
#pragma unroll
            for (int r = 0; r < 4; ++r) {
                const int cu = r & 1, nx = cu ^ 1;
#pragma unroll
                for (int i = 0; i < 8; ++i) {
                    const int n = 8 * r + i;
                    if (n + 3 < 32) afr[(n + 3) & 3] = lda(n + 3);
                    if (r < 3 && i < 4) { bfr[nx][2 * i] = lds_ld<bf16x8>(bb + 2048 * i + 128 * (2 - r)); bfr[nx][2 * i + 1] = lds_ld<bf16x8>(bb + 2048 * i + 1024 + 128 * (2 - r)); }
#pragma unroll
                    for (int cb = i; cb < 8; ++cb) acc[cb] = MFMA16(afr[n & 3], bfr[cu][cb - i], acc[cb]);
                    SBAR();
                }
            }
        }
        FENCE();
        __builtin_amdgcn_s_setprio(3);
        STAMP(6);
        {
            f32x4 kp[4];
            if (half) {
#pragma unroll
                for (int k = 0; k < 4; ++k) { lds_st<f32x4>(xa + k * 1024, acc[k]); kp[k] = acc[4 + k]; }
            } else {
#pragma unroll
                for (int k = 0; k < 4; ++k) { lds_st<f32x4>(xa + k * 1024, acc[4 + k]); kp[k] = acc[k]; }
            }
            sig(vms, 4);
            wait1(vps, 4);
#pragma unroll
            for (int k = 0; k < 4; ++k) kp[k] += lds_ld<f32x4>(xp + k * 1024);
            sig(vms, 5);
            if (YSG) wait4s(syncA, YSG);
            if (PACE3) { for (int i = 0; i < bw; ++i) __builtin_amdgcn_s_sleep(2 * PACE3); }
            st128<0, true>(wss, u32x4{pkbf(kp[0][0], kp[0][1]), pkbf(kp[0][2], kp[0][3]), pkbf(kp[1][0], kp[1][1]), pkbf(kp[1][2], kp[1][3])}, (u32)lane * 16, WS_YT + (u32)d * 4096 + (u32)half * 2048);
            if (PACE3) __builtin_amdgcn_s_sleep(PACE3);
            st128<1024, true>(wss, u32x4{pkbf(kp[2][0], kp[2][1]), pkbf(kp[2][2], kp[2][3]), pkbf(kp[3][0], kp[3][1]), pkbf(kp[3][2], kp[3][3])}, (u32)lane * 16, WS_YT + (u32)d * 4096 + (u32)half * 2048);
            WAITV(0);
            __builtin_amdgcn_raw_buffer_store_b64(u32x2{ta, tb}, ws, lane ? OOB : 0u, WS_YFLAG + (u32)(half * 512 + d) * FS, 16);
        }
        STAMP(7);
    }
    STAMP_OUT();
    if (MW && bid == 0 && tid == 0) *(volatile u64*)(p.ws + (11u << 20)) = tstart;
    if (MS && bid == 0) { const u64 t0 = MW ? tstart : *(volatile u64*)(p.ws + (11u << 20)); while (__builtin_amdgcn_s_memrealtime() - t0 < ENDT) __builtin_amdgcn_s_sleep(4); }
}
__global__ void kfirst(char* ws) { const u64 t = __builtin_amdgcn_s_memrealtime(); if (__builtin_amdgcn_workgroup_id_x() == 0 && __builtin_amdgcn_workitem_id_x() == 0) *(volatile u64*)(ws + (11u << 20)) = t; }
__global__ void klast(char* ws) { if (__builtin_amdgcn_workgroup_id_x() == 0) { const u64 t0 = *(volatile u64*)(ws + (11u << 20)); while (__builtin_amdgcn_s_memrealtime() - t0 < ENDT) __builtin_amdgcn_s_sleep(4); } }

#if defined(__HIP_DEVICE_COMPILE__) && !PK
#pragma clang attribute pop
#endif

extern "C" void kernel_launch(void* const* d_in, const int* in_sizes, int n_in, void* d_out, int out_size, void* d_ws, size_t ws_size, hipStream_t stream) {
    Params p;
    for (int i = 0; i < 18; ++i) p.in[i] = d_in[i];
    p.out = (float*)d_out;
    p.ws = (char*)d_ws;
    (void)hipFuncSetAttribute((const void*)ssm_fused, hipFuncAttributeMaxDynamicSharedMemorySize, LDS_BYTES);
    if (!MW) kfirst<<<FG, 64, 0, stream>>>((char*)d_ws);
    ssm_fused<<<256, 768, LDS_BYTES, stream>>>(p);
    if (!MS) klast<<<LG, 64, 0, stream>>>((char*)d_ws);
}
```

```cpp
#define ENC 10
#define ENCW 4
#include <hip/hip_runtime.h>
#include <stdint.h>

typedef unsigned short u16;
typedef unsigned u32;
typedef unsigned long long u64;
typedef short bf16x8 __attribute__((ext_vector_type(8)));
typedef short s16x4 __attribute__((ext_vector_type(4)));
typedef float f32x2 __attribute__((ext_vector_type(2)));
typedef float f32x4 __attribute__((ext_vector_type(4)));
typedef float f32x16 __attribute__((ext_vector_type(16)));
typedef unsigned u32x2 __attribute__((ext_vector_type(2)));
typedef unsigned u32x4 __attribute__((ext_vector_type(4)));
typedef unsigned u32x8 __attribute__((ext_vector_type(8)));
typedef unsigned u32x16 __attribute__((ext_vector_type(16)));
typedef __bf16 bf16x2_t __attribute__((ext_vector_type(2)));
#define LAS __attribute__((address_space(3)))
#define GAS __attribute__((address_space(1)))
typedef __amdgpu_buffer_rsrc_t rsrc_t;

#define ABL 0
#define PRA 3
#define PRK 1
#define UM 7
#define DBG1 0
#define LSTAG 5
#define CTHR 0
#define PRL 3
#define WSL 0
#define LWIN 12
#define UFN 6
#define TOUCH 0
#define CPF 2
#define OAUX 16
#define KGATE 2
#define CGATE 0
#define CVN 2
#define UDLY 0
#define UPOLL 7
#define CWIN 8
#define MATHN 12
#define NOZ 0
#define NOU 0
#define L0P 1
#define CB1N 3
#define EDLY 14
#define LDLY 3
#define CSTART 1
#define YDLY 1
#define YSL 2
#define REMAP 1
#define XW 0
#define NOKP 0
#define UFIRST 0
#define CPAUSE 7
#define FIRSTW 0
#define NOT1 0
#define NOC 0
#define NOTCH 0
#define NOSK 0
#define HSL 0
#define PRSH 0
#define PRCV 2
#define SPF 0
#define SPFW 0
#define YGATE 5
#define SIG5 8
#define PDLY 0
#define YSG 0
#define PPOS 1
#define CW 5
#define PAUX 16
#define HDLY 0
#define SPEC 0
#define CWI 1
#define ROTG 8
#define PK 0
#define KCG 0
#define SPF2 0
#define PACE1 0
#define PACE2 0
#define PACE3 0
#define OAUXB 16
#define DBG2 0
#define ASELF 0
#define ADLY 0
#define NOCHK 0
#define CDLY 20
#define ECP 1
#define HPROBE 1
#define PDL 1
#define PSL 0
#define ATOM 1
#define ATOMF 1
#define PGATE 6
#define NT 0
#define NTU ""
#define NTG ""
#define NTC ""
#define STAMPS 0

constexpr u32 WS_W = 0;
constexpr u32 FS = 128;
constexpr u32 WS_UFLAG = 524288;
constexpr u32 WS_HFLAG = 131072;
constexpr u32 WS_YFLAG = 262144;
constexpr u32 WS_E = 10u << 20;
constexpr u32 WS_UT = 1u << 20;
constexpr u32 WS_YT = 3u << 20;
constexpr u32 WS_H = 5u << 20;
constexpr u32 WS_CS = 9u << 20;
constexpr u32 WS_BYTES = 11u << 20;
constexpr u32 WS_STAMP = 11u << 20;
constexpr u32 OOB = 0x7FFFFF00u;
constexpr u32 L_R2 = 131072;
constexpr u32 L_S0 = L_R2, S0_SZ = 4672;
constexpr u32 L_KC = L_R2 + 4 * S0_SZ + 48, KC_SZ = 1488;
constexpr u32 L_CB1 = L_KC + 2 * KC_SZ - 48;
constexpr u32 L_CB2 = L_CB1 + 768;
constexpr u32 L_ZL = L_CB2 + 256;
constexpr u32 L_ZA = L_ZL + 128;
constexpr u32 L_SYNC = L_ZA + 128;
constexpr u32 L_UB = L_SYNC + 64, UB_PAD = 960;
constexpr u32 L_W = L_UB + UB_PAD + 8192;
constexpr u32 L_PAR = 65536;
constexpr u32 LDS_BYTES = 163840;
static_assert(L_W + 16 <= LDS_BYTES, "lds");
constexpr int NWAVES = 12;
#define HWIN 9
#define PSLEEP 1
constexpr u32 SPIN_CAP = 1u << 15;
constexpr u32 CODE_BYTES = ABL >= 2 ? 6144 : ABL ? 17408 : 28416;
constexpr u32 CODE_SLICES = 3;
constexpr u32 SENT = 0xFFFFFFFFu;

struct Params { const void* in[18]; float* out; char* ws; };

#if defined(__HIP_DEVICE_COMPILE__) && !PK
#pragma clang attribute push(__attribute__((target("no-packed-fp32-ops"))), apply_to = function)
#endif
template <int I> struct ic { static constexpr int v = I; };
template <int B, int E, typename F> __device__ __forceinline__ void sfor(F&& f) { if constexpr (B < E) { f(ic<B>{}); sfor<B + 1, E>(f); } }
#define INL __attribute__((always_inline))
__device__ __forceinline__ u32 umin(u32 a, u32 b) { return a < b ? a : b; }
__device__ __forceinline__ u32 umax(u32 a, u32 b) { return a > b ? a : b; }
__device__ __forceinline__ u32 umax3(u32 a, u32 b, u32 c) { return umax(umax(a, b), c); }
__device__ __forceinline__ float u2f(u32 v) { return __builtin_bit_cast(float, v); }
__device__ __forceinline__ u32 f2u(float v) { return __builtin_bit_cast(u32, v); }

__device__ __forceinline__ float bf2f(u32 v) { return u2f(v << 16); }
__device__ __forceinline__ u32 pkbf(float lo, float hi) { f32x2 a = {lo, hi}; return __builtin_bit_cast(u32, __builtin_convertvector(a, bf16x2_t)); }
__device__ __forceinline__ float rbf(float x) { return u2f(pkbf(x, x) << 16); }
__device__ __forceinline__ u32x4 mk_srd(const void* p, u32 bytes) { u64 a = (u64)p; u32x4 r = {(u32)a, (u32)(a >> 32) & 0xFFFFu, bytes, 0x00020000u}; return r; }
__device__ __forceinline__ rsrc_t mk_rsrc(const void* p, u32 bytes) { return __builtin_amdgcn_make_buffer_rsrc((void*)p, 0, bytes, 0x00020000); }
__device__ __forceinline__ void dma4(u32x4 srd, u32 v0, u32 v1, u32 soff, u32 ldst) {
    asm volatile("s_mov_b32 m0, %0\n\ts_nop 0\n\t"
                 "buffer_load_dwordx4 %1, %3, %4 offen" NTG " lds\n\t"
                 "buffer_load_dwordx4 %2, %3, %4 offen offset:1024" NTG " lds\n\t"
                 "buffer_load_dwordx4 %1, %3, %4 offen offset:2048" NTG " lds\n\t"
                 "buffer_load_dwordx4 %2, %3, %4 offen offset:3072" NTG " lds"
                 :: "s"(ldst), "v"(v0), "v"(v1), "s"(srd), "s"(soff) : "m0", "memory");
}
__device__ __forceinline__ void dma4n(u32x4 srd, const u32 (&v)[4], u32 soff, u32 ldst) {
    asm volatile("s_mov_b32 m0, %0\n\ts_nop 0\n\t"
                 "buffer_load_dwordx4 %1, %5, %6 offen" NTC " lds\n\t"
                 "buffer_load_dwordx4 %2, %5, %6 offen offset:1024" NTC " lds\n\t"
                 "buffer_load_dwordx4 %3, %5, %6 offen offset:2048" NTC " lds\n\t"
                 "buffer_load_dwordx4 %4, %5, %6 offen offset:3072" NTC " lds"
                 :: "s"(ldst), "v"(v[0]), "v"(v[1]), "v"(v[2]), "v"(v[3]), "s"(srd), "s"(soff) : "m0", "memory");
}
__device__ __forceinline__ void dma1(u32x4 srd, u32 v0, u32 soff, u32 ldst) {
    asm volatile("s_mov_b32 m0, %0\n\ts_nop 0\n\tbuffer_load_dwordx4 %1, %2, %3 offen lds" :: "s"(ldst), "v"(v0), "s"(srd), "s"(soff) : "m0", "memory");
}
__device__ __forceinline__ void zsig(u32x4 srd, u32 voob, u32 ldst) {
    asm volatile("s_mov_b32 m0, %0\n\ts_mov_b64 exec, 1\n\tbuffer_load_dword %1, %2, 0 offen lds\n\ts_mov_b64 exec, -1" :: "s"(ldst), "v"(voob), "s"(srd) : "m0", "memory");
}
template <int IMM, bool SC1> __device__ __forceinline__ void st128(u32x4 srd, u32x4 v, u32 voff, u32 soff) {
    if (SC1) asm volatile("buffer_store_dwordx4 %0, %1, %2, %3 offen offset:%4 sc1\n\ts_nop 1" :: "v"(v), "v"(voff), "s"(srd), "s"(soff), "i"(IMM) : "memory");
    else if (OAUX == 2) asm volatile("buffer_store_dwordx4 %0, %1, %2, %3 offen offset:%4 nt\n\ts_nop 1" :: "v"(v), "v"(voff), "s"(srd), "s"(soff), "i"(IMM) : "memory");
    else if (OAUX == 16) asm volatile("buffer_store_dwordx4 %0, %1, %2, %3 offen offset:%4 sc1\n\ts_nop 1" :: "v"(v), "v"(voff), "s"(srd), "s"(soff), "i"(IMM) : "memory");
    else asm volatile("buffer_store_dwordx4 %0, %1, %2, %3 offen offset:%4\n\ts_nop 1" :: "v"(v), "v"(voff), "s"(srd), "s"(soff), "i"(IMM) : "memory");
}
#define SBAR() __builtin_amdgcn_sched_barrier(0)
#define WAITV(N) asm volatile("s_waitcnt vmcnt(%0)" :: "i"(N) : "memory")
#define WAITL() asm volatile("s_waitcnt lgkmcnt(0)" ::: "memory")
#define FENCE() asm volatile("" ::: "memory")
template <typename T> __device__ __forceinline__ T lds_ld(u32 a) { return *(const LAS T*)(uintptr_t)a; }
template <typename T> __device__ __forceinline__ void lds_st(u32 a, T v) { *(LAS T*)(uintptr_t)a = v; }
__device__ __forceinline__ u32x2 lds_tr(u32 a) { return __builtin_bit_cast(u32x2, __builtin_amdgcn_ds_read_tr16_b64_v4i16((LAS s16x4*)(uintptr_t)a)); }
__device__ __forceinline__ void sig(u32 addr, u32 val) { asm volatile("ds_write_b32 %0, %1" :: "v"(addr), "v"(val) : "memory"); }
__device__ __forceinline__ u32 rfl(u32 v) { return (u32)__builtin_amdgcn_readfirstlane(v); }
__device__ __forceinline__ u32 peek4(u32 addr) { u32x4 v; asm volatile("ds_read_b128 %0, %1\n\ts_waitcnt lgkmcnt(0)" : "=v"(v) : "v"(addr) : "memory"); return rfl(umin(umin(v.x, v.y), umin(v.z, v.w))); }
__device__ __forceinline__ u32 peek3(u32 addr) { u32x4 v; asm volatile("ds_read_b128 %0, %1\n\ts_waitcnt lgkmcnt(0)" : "=v"(v) : "v"(addr) : "memory"); return rfl(umin(umin(v.y, v.z), v.w)); }
__device__ __forceinline__ u32 peek1(u32 addr) { u32 v; asm volatile("ds_read_b32 %0, %1\n\ts_waitcnt lgkmcnt(0)" : "=v"(v) : "v"(addr) : "memory"); return rfl(v); }
__device__ __forceinline__ u32 peekz2(u32 addr) { u32x2 v; asm volatile("ds_read_b64 %0, %1\n\ts_waitcnt lgkmcnt(0)" : "=v"(v) : "v"(addr) : "memory"); return rfl(v.x | v.y); }
__device__ __forceinline__ u32 peekz3(u32 addr) { u32x4 v; asm volatile("ds_read_b128 %0, %1\n\ts_waitcnt lgkmcnt(0)" : "=v"(v) : "v"(addr) : "memory"); return rfl(v.x | v.y | v.z); }
__device__ __forceinline__ u32 peekz4(u32 addr) { u32x4 v; asm volatile("ds_read_b128 %0, %1\n\ts_waitcnt lgkmcnt(0)" : "=v"(v) : "v"(addr) : "memory"); return rfl(v.x | v.y | v.z | v.w); }
#define SPINW(cond, slp) do { _Pragma("unroll 1") for (u32 sp_ = 0; (cond) && sp_ < (1u << 22); ++sp_) { if (slp) __builtin_amdgcn_s_sleep(slp); } } while (0)
__device__ __forceinline__ void waitz3(u32 addr) { SPINW(peekz3(addr) != 0, WSL); }
__device__ __forceinline__ void waitz3s(u32 addr) { SPINW(peekz3(addr) != 0, 2); }
__device__ __forceinline__ void waitz4(u32 addr) { SPINW(peekz4(addr) != 0, WSL); }
#define BAR() asm volatile("s_waitcnt lgkmcnt(0)\n\ts_barrier" ::: "memory")
__device__ __forceinline__ void wait4(u32 addr, u32 val) { SPINW(peek4(addr) < val, WSL); }
__device__ __forceinline__ void wait4s(u32 addr, u32 val) { SPINW(peek4(addr) < val, 2); }
__device__ __forceinline__ void wait3(u32 addr, u32 val) { SPINW(peek3(addr) < val, WSL); }
__device__ __forceinline__ void wait1(u32 addr, u32 val) { SPINW(peek1(addr) < val, WSL); }
__device__ __forceinline__ void wait1s(u32 addr, u32 val) { SPINW(peek1(addr) < val, 2); }
__device__ __forceinline__ u32 get_tag_old(u32 addr) {
    u32x2 v = {0u, 0u};
#pragma unroll 1
    for (u32 s = 0; s < (1u << 22); ++s) {
        asm volatile("ds_read_b64 %0, %1\n\ts_waitcnt lgkmcnt(0)" : "=v"(v) : "v"(addr) : "memory");
        if (__builtin_amdgcn_readfirstlane(v.y) != 0) break;
    }
    return __builtin_amdgcn_readfirstlane(v.x);
}
__device__ __forceinline__ u32 tag2(u32 t) { return t * 2654435761u + 0x2545F491u; }
typedef f32x2 cf;
__device__ __forceinline__ cf cmul(cf a, cf b) { return cf{__builtin_fmaf(-a.y, b.y, a.x * b.x), __builtin_fmaf(a.y, b.x, a.x * b.y)}; }
__device__ __forceinline__ cf csq(cf a) { return cf{__builtin_fmaf(-a.y, a.y, a.x * a.x), (a.x + a.x) * a.y}; }
__device__ __forceinline__ cf cfma(cf a, cf b, cf c) { return cf{__builtin_fmaf(-a.y, b.y, __builtin_fmaf(a.x, b.x, c.x)), __builtin_fmaf(a.y, b.x, __builtin_fmaf(a.x, b.y, c.y))}; }
__device__ __forceinline__ f32x2 fma2(float a, f32x2 b, f32x2 c) { return f32x2{__builtin_fmaf(a, b.x, c.x), __builtin_fmaf(a, b.y, c.y)}; }
__device__ __forceinline__ u32 pkc(cf a) { return __builtin_bit_cast(u32, __builtin_convertvector(a, bf16x2_t)); }
__device__ __forceinline__ cf cexpk(float zr, float zi, float k) {
    float m = __builtin_amdgcn_exp2f(zr * (k * 1.4426950408889634f));
    float f = __builtin_amdgcn_fractf(zi * (k * 0.15915494309189535f));
    return cf{m * __builtin_amdgcn_cosf(f), m * __builtin_amdgcn_sinf(f)};
}
__device__ __forceinline__ void swap_halves(cf v, cf& lo, cf& hi) {
    auto a = __builtin_amdgcn_permlane32_swap(f2u(v.x), f2u(v.x), false, false);
    auto b = __builtin_amdgcn_permlane32_swap(f2u(v.y), f2u(v.y), false, false);
    lo = cf{u2f(a[0]), u2f(b[0])}; hi = cf{u2f(a[1]), u2f(b[1])};
}
template <int SL = PSLEEP> __device__ __forceinline__ void poll_flags(rsrc_t ws, u32x4 wsd, u32 voff, u32 ta, u32 tb) {
#pragma unroll 1
    for (u32 s = 0; s < SPIN_CAP; ++s) {
        FENCE();
        u32x2 v = {0u, 0u};
        if (ATOMF) asm volatile("buffer_atomic_or_x2 %0, %1, %2, 0 offen sc0 sc1\n\ts_waitcnt vmcnt(0)" : "+v"(v) : "v"(voff), "s"(wsd) : "memory");
        else v = __builtin_amdgcn_raw_buffer_load_b64(ws, voff, 0, PAUX);
        voff = ((v.x ^ ta) | (v.y ^ tb)) == 0 ? OOB : voff;
        if (__builtin_amdgcn_ballot_w64(voff != OOB) == 0) break;
        if (SL) __builtin_amdgcn_s_sleep(SL);
    }
    FENCE();
}
__device__ __forceinline__ f32x4 sel4(bool c, f32x4 a, f32x4 b) { asm volatile("" : "+v"(a), "+v"(b)); return c ? a : b; }
#define ADD4(s, v) (((s) + (v).x + (v).y) + (v).z + (v).w)
#define MFMA32(a, b, c) __builtin_amdgcn_mfma_f32_32x32x16_bf16(a, b, c, 0, 0, 0)
#define MFMA16(a, b, c) __builtin_amdgcn_mfma_f32_16x16x32_bf16(a, b, c, 0, 0, 0)

#define STAMP(i) do { if ((i) == ENC) { SBAR(); tenc = __builtin_amdgcn_s_memrealtime(); SBAR(); } } while (0)
#define STAMP_OUT() do { if (wv >= ENCW && wv < ENCW + 4) { while (__builtin_amdgcn_s_memrealtime() - tenc < 2000) __builtin_amdgcn_s_sleep(4); } } while (0)

__global__ __launch_bounds__(768, 3) void ssm_fused(Params) {
    u64 tenc = 0;
    STAMP(0);
    const auto kseg = __builtin_amdgcn_kernarg_segment_ptr();
    extern __shared__ __attribute__((aligned(16))) char smem[];
    const u32 lds0 = (u32)(uintptr_t)(LAS char*)smem;
    const u32 tid = __builtin_amdgcn_workitem_id_x();
    const int lane = tid & 63;
    const int hwv = __builtin_amdgcn_readfirstlane(tid >> 6);
    const u32 bid = __builtin_amdgcn_workgroup_id_x();
    u32x16 k0, k1; u32x8 k2; u32x4 cpf;
    if (hwv >= 8) {
        asm volatile("s_load_dwordx16 %0, %3, 0x0\n\ts_load_dwordx16 %1, %3, 0x40\n\ts_load_dwordx8 %2, %3, 0x80\n\ts_setprio 3\n\ts_barrier" : "=&s"(k0), "=&s"(k1), "=&s"(k2) : "s"(kseg) : "memory");
    } else {
        u64 pc0; asm volatile("s_getpc_b64 %0" : "=s"(pc0));
        cpf = __builtin_amdgcn_raw_buffer_load_b128(mk_rsrc((const void*)(pc0 & ~255ull), hwv < CPF ? CODE_BYTES : 0u), (u32)lane * 16, ((bid >> 3) ^ ((u32)hwv * 16)) * 1024, 0);
        const u32 iv = tid < 160 ? SENT : 0u;
        lds_st<u32x2>(lds0 + L_CB1 + umin(tid, 287u) * 8, u32x2{iv, iv});
        asm volatile("s_waitcnt lgkmcnt(0)\n\ts_barrier" ::: "memory");
    }
    const int wv = (int)((0x0213BA987654ull >> (4 * hwv)) & 15);
    const int xcd = bid & 7, idx = bid >> 3;
    const u32 syncU = lds0 + L_SYNC, syncA = syncU + 16, syncK = syncU + 32, tagw = syncU + 48;
    const int rr = lane >> 3, sp = lane & 7;
    const int m16 = lane & 15, q4 = lane >> 4;
    const int r32 = lane & 31, hh = lane >> 5;
    const int g32 = 4 * (r32 & 7) + (r32 >> 3);
    const int ppos = (xcd < 4 ? xcd : (xcd ^ 2)) >> 1;
    const int c = 4 * ppos + (idx & 3), pb = 8 * (xcd & 1) + (idx >> 2);
    const int tb64 = 8 * ppos + 4 * (xcd & 1) + (idx & 3), db = idx >> 2;
    const int dK = 64 * xcd + 2 * idx + ((wv >> 1) & 1);
    u32 voob = OOB;
#define LATE_P() auto kl_ = kseg; asm volatile("" : "+s"(kl_)); const __attribute__((address_space(4))) Params& p = *(const __attribute__((address_space(4))) Params*)kl_
#define KPTR(k, i) ((const GAS char*)(((u64)k[2 * (i) + 1] << 32) | k[2 * (i)]))
    u32 pr[7], wv0;
    const u32 zpar = lds0 + L_ZL + 84, zu = lds0 + L_ZL + 80;
#define get_tag(x) (peek1(lds0 + L_W) + 1u)
    asm volatile("" : "+v"(voob));
    if (hwv == 9) {
        u32 vq0 = (u32)(64 * xcd + 2 * idx) * 256 + (u32)lane * 16, vq1 = (u32)pb * 64 + (u32)lane * 16, vq2 = (u32)(64 * xcd + 2 * idx) * 2;
        asm volatile("s_waitcnt lgkmcnt(0)\n\t"
                     "s_mov_b32 m0, %0\n\ts_mov_b32 exec_hi, 0\n\tglobal_load_lds_dwordx4 %1, %[b0]\n\t"
                     "s_add_u32 m0, m0, 512\n\ts_nop 0\n\tglobal_load_lds_dwordx4 %1, %[b1]\n\t"
                     "s_add_u32 m0, m0, 512\n\ts_nop 0\n\tglobal_load_lds_dwordx4 %1, %[b2]\n\t"
                     "s_add_u32 m0, m0, 512\n\ts_nop 0\n\tglobal_load_lds_dwordx4 %1, %[b3]\n\t"
                     "s_add_u32 m0, m0, 512\n\ts_nop 0\n\tglobal_load_lds_dwordx4 %1, %[b4]\n\t"
                     "s_add_u32 m0, m0, 512\n\ts_nop 0\n\tglobal_load_lds_dwordx4 %1, %[b5]\n\t"
                     "s_add_u32 m0, m0, 512\n\ts_mov_b32 exec_lo, 15\n\tglobal_load_lds_dwordx4 %2, %[a0]\n\t"
                     "s_add_u32 m0, m0, 64\n\ts_nop 0\n\tglobal_load_lds_dwordx4 %2, %[a1]\n\t"
                     "s_add_u32 m0, m0, 64\n\ts_nop 0\n\tglobal_load_lds_dwordx4 %2, %[a2]\n\t"
                     "s_add_u32 m0, m0, 64\n\ts_mov_b32 exec_lo, 1\n\tglobal_load_lds_dword %3, %[c0]\n\t"
                     "s_add_u32 m0, m0, 4\n\ts_nop 0\n\tglobal_load_lds_dword %3, %[c1]\n\t"
                     "s_mov_b32 m0, %4\n\ts_nop 0\n\tglobal_load_lds_dword %5, %[w] sc1\n\ts_mov_b64 exec, -1"
                     :: "s"(lds0 + L_PAR), "v"(vq0), "v"(vq1), "v"(vq2), "s"(lds0 + L_W), "v"(0u),
                        [b0] "s"(k1.s45), [b1] "s"(k1.s67), [b2] "s"(k1.s89), [b3] "s"(k1.sab), [b4] "s"(k1.scd), [b5] "s"(k1.sef),
                        [a0] "s"(k0.s23), [a1] "s"(k0.s45), [a2] "s"(k0.sef), [c0] "s"(k2.s01), [c1] "s"(k2.s23), [w] "s"(k2.s67), "s"(k0), "s"(k1), "s"(k2) : "m0", "scc", "memory");
        zsig(u32x4{0u, 0u, 0u, 0x00020000u}, voob, zpar);
        if (DBG1) { STAMP(1); WAITV(7); STAMP(2); WAITV(4); STAMP(3); WAITV(2); STAMP(4); WAITV(1); STAMP(10); WAITV(0); STAMP(11); }
    } else if (hwv == 8) {
        u32 vu = (u32)(idx * 64 + rr) * 1024 + (u32)xcd * 128 + (u32)((sp ^ ((rr >> 1) & 1)) * 16);
        if (UDLY) __builtin_amdgcn_s_sleep(UDLY);
        asm volatile("s_waitcnt lgkmcnt(0)\n\t"
                     "s_mov_b32 m0, %0\n\ts_nop 0\n\tglobal_load_lds_dwordx4 %1, %9" NTU "\n\t"
                     "s_add_u32 m0, m0, 1024\n\ts_nop 0\n\tglobal_load_lds_dwordx4 %2, %9" NTU "\n\t"
                     "s_add_u32 m0, m0, 1024\n\ts_nop 0\n\tglobal_load_lds_dwordx4 %3, %9" NTU "\n\t"
                     "s_add_u32 m0, m0, 1024\n\ts_nop 0\n\tglobal_load_lds_dwordx4 %4, %9" NTU "\n\t"
                     "s_add_u32 m0, m0, 1024\n\ts_nop 0\n\tglobal_load_lds_dwordx4 %5, %9" NTU "\n\t"
                     "s_add_u32 m0, m0, 1024\n\ts_nop 0\n\tglobal_load_lds_dwordx4 %6, %9" NTU "\n\t"
                     "s_add_u32 m0, m0, 1024\n\ts_nop 0\n\tglobal_load_lds_dwordx4 %7, %9" NTU "\n\t"
                     "s_add_u32 m0, m0, 1024\n\ts_nop 0\n\tglobal_load_lds_dwordx4 %8, %9" NTU
                     :: "s"(lds0 + L_UB + UB_PAD), "v"(vu), "v"((vu ^ 32) + 8192), "v"((vu ^ 64) + 16384), "v"((vu ^ 96) + 24576), "v"(vu + 32768), "v"((vu ^ 32) + 40960), "v"((vu ^ 64) + 49152), "v"((vu ^ 96) + 57344),
                        "s"(k1.s23), "s"(k0), "s"(k1), "s"(k2) : "m0", "scc", "memory");
    } else if (hwv >= 10) {
        const u32 lw = (u32)hwv & 1u ^ 1u;
        u32 vd0 = (u32)rr * 4096 + (u32)((sp ^ (rr >> 1)) * 16), vd1 = vd0 ^ 64, za = lds0 + L_ZA;
        u32 xs = (u32)c * 131072, bs = (u32)pb * 32768, r0 = ((u32)(ROTG == 8 ? (idx >> 2) + 2 * (idx & 3) : ROTG == 4 ? 2 * ((idx >> 2) + idx) + (xcd & 1) : 4 * ((idx >> 2) + idx) + (xcd & 3)) & 7u) * 128;
        asm volatile("" : "+s"(xs), "+s"(bs));
        u32 o0 = lw ? xs + 32768 : bs, o1 = lw ? xs + 65536 : bs, o2 = xs + lw * 98304, nrec = lw ? 2048 * 1024 : 512 * 1024;
        u32 l0 = lds0 + (lw ? 4096 : 81920 + 8192), lstep = lw ? 16384 : 8192, lwrap = lw ? 81920 : 49152, adv = lw ? 0 : 128;
        u32 lx = lds0 + lw * 12288, zl = lds0 + L_ZL + lw * 4;
        asm volatile("" : "+v"(vd0), "+v"(vd1), "+v"(za));
        asm volatile("" : "+s"(o0), "+s"(o1), "+s"(o2), "+s"(r0), "+s"(nrec), "+s"(l0), "+s"(lstep), "+s"(lwrap), "+s"(adv), "+s"(lx), "+s"(zl));
        u32x2 px, pa, pb_, pc_;
        asm volatile("s_waitcnt lgkmcnt(0)\n\ts_cmp_lg_u32 %4, 0\n\ts_mov_b64 %0, %5\n\ts_cselect_b64 %1, %5, %6\n\ts_cselect_b64 %2, %5, %7\n\ts_cselect_b64 %3, %9, %8"
                     : "=&s"(px), "=&s"(pa), "=&s"(pb_), "=&s"(pc_) : "s"(lw), "s"(k0.s01), "s"(k0.s67), "s"(k0.s89), "s"(k0.sab), "s"(k0.scd), "s"(k0), "s"(k1), "s"(k2) : "scc", "memory");
        STAMP(11);
        if (LDLY) __builtin_amdgcn_s_sleep(LDLY);
        const u32x4 sx = {px.x, px.y & 0xFFFFu, 2048 * 1024, 0x00020000u}, s0 = {pa.x, pa.y & 0xFFFFu, nrec, 0x00020000u}, s1 = {pb_.x, pb_.y & 0xFFFFu, nrec, 0x00020000u};
        if (lw == 0) { dma4(s0, vd0, vd1, bs + r0, lds0 + 81920); dma4(s1, vd0, vd1, bs + r0, lds0 + 81920 + 4096); }
            u32 vc[4];
#pragma unroll
            for (int b = 0; b < 4; ++b) vc[b] = (((u32)rr * 1024 + (u32)((sp ^ (rr >> 1)) * 16)) ^ ((b & 1) * 64)) + b * 7168;
            asm volatile("" : "+v"(vc[0]), "+v"(vc[1]), "+v"(vc[2]), "+v"(vc[3]));
        u32x4 fl = {0u, 0u, 0u, 0u};
#pragma unroll 1
        for (int s = 0; s < 8; ++s) {
            const u32 ra = (r0 + adv) & 0x380u;
            if (s == 4 && lw == 0 && ABL < 2) { wait4(syncA, 1); if (ABL == 0) wait4(syncK, 1); if (!ASELF) wait1(syncU + 4, 1); }
            if (s >= 5 && ABL != 2) { if (rfl(fl.x | fl.y | fl.z | fl.w) != 0) waitz4(za); za += 16; if (s == 5) { lx -= 81920; l0 -= lwrap; } }
            if (s < 7 || lw) { dma4(s0, vd0, vd1, o0 + ra, l0); dma4(s1, vd0, vd1, o1 + ra, l0 + 4096); }
            fl = *(volatile const LAS u32x4*)(uintptr_t)za;
            dma4(sx, vd0, vd1, o2 + r0, lx);
            zsig(sx, voob, zl);
            r0 = (r0 + 128) & 0x380u; lx += 16384; l0 += lstep; zl += 8;
        }
        STAMP(3);
        if (ABL >= 2) { WAITV(0); STAMP(4); LATE_P(); STAMP_OUT(); return; }
        {
            const u32x4 sc = {pc_.x, pc_.y & 0xFFFFu, 512 * 1024, 0x00020000u};
            if (rfl(fl.x | fl.y | fl.z | fl.w) != 0) waitz4(lds0 + L_ZA + 3 * 16);
#pragma unroll 1
            for (u32 i = 0; i < 8; ++i) {
                const u32 j = (0x52107463u >> (4 * i)) & 15u;
                if (i == CWI) { STAMP(4); wait4(syncA, 3); if (CW == 5) __builtin_amdgcn_s_sleep(CDLY); if (CW == 1) WAITV(0); if (CW == 2) SPINW(peek1(lds0 + L_CB2) == SENT, 1); if (CW == 3) wait4s(syncA, 4); if (CW == 4) wait4s(syncA, 3); STAMP(1); }
                if (PGATE && i == PGATE) zsig(sc, voob, lds0 + L_ZL + 96 + lw * 4);
                const u32 so = (u32)db * 65536 + ((j & 4) | ((j + (u32)idx) & 3)) * 128, ld = lds0 + j * 16384 + lw * 8192 + (j >> 2) * 4096;
                dma4n(sc, vc, so, ld);
                dma4n(sc, vc, so + 32768, ld ^ 4096);
            }
            zsig(sc, voob, lds0 + L_ZL + 64 + lw * 4);
        }
        STAMP(5);
    }
    LATE_P();
    const rsrc_t ws = mk_rsrc(p.ws, WS_BYTES);
    const u32x4 wss = mk_srd(p.ws, WS_BYTES);
    if (ABL == 2 || (ABL == 3 && (wv < 4 || wv >= 8))) { WAITV(0); STAMP(1); STAMP_OUT(); return; }
    int wv2 = wv;
    asm volatile("" : "+s"(wv2));
    if (wv2 < 4) {
        const int uw = wv;
        if (uw == 3) {
            const u32 im0 = lds0 + L_UB + UB_PAD;
            __builtin_amdgcn_s_setprio(3);
            const u32 ra = (u32)(8 * (lane & 3) + (m16 >> 2)) * 128 + (u32)(((q4 >> 1) ^ (2 * (lane & 3) + ((m16 >> 3) & 1))) * 16) + (u32)(q4 & 1) * 8;
            WAITV(0);
            SPINW(peek1(zpar) != 0, 1);
            const u32 ta = get_tag(0), tb = tag2(ta);
            STAMP(1);
            u32x4 ut[8];
#pragma unroll
            for (int k = 0; k < 8; ++k) { const u32 a = (ra ^ ((k & 3) * 32)) + im0 + (k >> 2) * 4096; const u32x2 x = lds_tr(a), y = lds_tr(a + 512); ut[k] = u32x4{x.x, x.y, y.x, y.y}; }
#pragma unroll
            for (int k = 0; k < 8; ++k) { st128<0, true>(wss, ut[k], (u32)q4 * 16384 + (u32)m16 * 16, WS_UT + (u32)(16 * xcd + 4 * (k & 3)) * 16384 + (u32)(2 * idx + (k >> 2)) * 256); if (PACE1) __builtin_amdgcn_s_sleep(PACE1); }
            WAITV(0);
            __builtin_amdgcn_raw_buffer_store_b64(u32x2{ta, tb}, ws, lane ? OOB : 0u, WS_UFLAG + (u32)(xcd * 32 + idx) * FS, 16);
            FENCE();
            STAMP(2);
            SPINW(peekz2(lds0 + L_ZL + UPOLL * 8) != 0, 2);
            poll_flags(ws, wss, lane < 32 ? WS_UFLAG + (u32)(xcd * 32 + lane) * FS : OOB, ta, tb);
            STAMP(3);
            {
                const u32 vt = (u32)(lane >> 1) * 64 + (u32)(lane & 1) * 16, so = WS_UT + (u32)(16 * xcd + (idx >> 1)) * 16384 + (u32)(idx & 1) * 32;
#pragma unroll
                for (int k = 0; k < 8; ++k)
                    asm volatile("s_mov_b32 m0, %0\n\ts_nop 0\n\tbuffer_load_dwordx4 %1, %2, %3 offen sc1 lds" :: "s"(im0 + k * 1024), "v"(vt), "s"(wss), "s"(so + k * 2048) : "m0", "memory");
                zsig(wss, voob, lds0 + L_ZL + 72);
            }
            STAMP(4);
        } else if (uw == 1 && !ASELF) {
            u32 tw = 0, to = 0, tc = 0;
            if (TOUCH) {
                tw = __builtin_amdgcn_raw_buffer_load_b32(ws, lane < 9 * (1048576 / TOUCH) ? (u32)lane * TOUCH + 4096 : OOB, 0, 16);
                to = __builtin_amdgcn_raw_buffer_load_b32(mk_rsrc(p.out, 2048 * 4096), (u32)lane * TOUCH, 0, 16);
                tc = __builtin_amdgcn_raw_buffer_load_b32(mk_rsrc(p.in[5], 4), 0, 0, 0) + __builtin_amdgcn_raw_buffer_load_b32(mk_rsrc(p.in[6], 4), 0, 0, 0) + __builtin_amdgcn_raw_buffer_load_b32(mk_rsrc(p.in[8], 4), 0, 0, 0);
            }
            SPINW(peek1(zpar) != 0, 1);
#pragma unroll
            for (int i = 0; i < 3; ++i) pr[i] = lds_ld<u16>(lds0 + L_PAR + 3072 + i * 64 + g32 * 2);
            sig(syncU + 4, 1);
            cf L128, L256;
            {
                const float dt = rbf(__builtin_expf(bf2f(pr[2])));
                L128 = cexpk(dt * bf2f(pr[0]), dt * bf2f(pr[1]), 1.f);
#pragma unroll
                for (int i = 0; i < 7; ++i) L128 = csq(L128);
                L256 = csq(L128);
            }
            u32 vo[8];
            {
                const int cb0 = c - 1 - hh;
                const u32 vo0 = WS_E + (u32)(cb0 * 512 + pb * 32 + r32) * 16;
#pragma unroll
                for (int k = 0; k < 8; ++k) { vo[k] = 2 * k <= cb0 ? vo0 - (u32)k * 16384 : OOB; asm volatile("" : "+v"(vo[k])); }
            }
            asm volatile("" : "+v"(L128), "+v"(L256) : "v"(tw), "v"(to), "v"(tc));
            const u32 ta = get_tag(tagw), tb = tag2(ta);
            if (SPF) {
                SPINW(peekz2(lds0 + L_ZL + SPFW * 8) != 0, 2);
                const u32 off = (u32)db * 65536 + (u32)(idx & 3) * 16384 + 8192;
                u64 a0 = (u64)p.in[5] + off, a1 = (u64)p.in[6] + off;
                u32 dmy = 0;
#pragma unroll 1
                for (int i = 0; i < 16; ++i) {
                    asm volatile("s_load_dword %0, %1, 0x0\n\ts_load_dword %0, %1, 0x40\n\ts_load_dword %0, %1, 0x80\n\ts_load_dword %0, %1, 0xc0\n\ts_load_dword %0, %1, 0x100\n\ts_load_dword %0, %1, 0x140\n\ts_load_dword %0, %1, 0x180\n\ts_load_dword %0, %1, 0x1c0\n\t"
                                 "s_load_dword %0, %2, 0x0\n\ts_load_dword %0, %2, 0x40\n\ts_load_dword %0, %2, 0x80\n\ts_load_dword %0, %2, 0xc0\n\ts_load_dword %0, %2, 0x100\n\ts_load_dword %0, %2, 0x140\n\ts_load_dword %0, %2, 0x180\n\ts_load_dword %0, %2, 0x1c0"
                                 : "+s"(dmy) : "s"(a0), "s"(a1));
                    a0 += 512; a1 += 512;
                }
                asm volatile("s_waitcnt lgkmcnt(0)" : "+s"(dmy));
                STAMP(4);
            }
            u32x4 ut[8];
            SPINW(peek1(lds0 + L_CB1 + 512) == SENT, 1);
            if (EDLY && c) __builtin_amdgcn_s_sleep(EDLY);
#pragma unroll 1
            for (u32 spn = 0; spn < SPIN_CAP; ++spn) {
                FENCE();
#pragma unroll
                for (int k = 0; k < 8; ++k) ut[k] = __builtin_amdgcn_raw_buffer_load_b128(ws, vo[k], umin(spn, ECP - 1) * 131072, PAUX);
                u32 bad = 0;
#pragma unroll
                for (int k = 0; k < 8; ++k) bad |= vo[k] != OOB ? ((ut[k].y ^ ta) | (ut[k].w ^ tb)) : 0u;
                if (__builtin_amdgcn_ballot_w64(bad != 0) == 0) break;
            }
            cf G = {u2f(ut[7].x), u2f(ut[7].z)}, G0, G1;
#pragma unroll
            for (int k = 6; k >= 0; --k) G = cfma(G, L256, cf{u2f(ut[k].x), u2f(ut[k].z)});
            swap_halves(G, G0, G1);
            lds_st<f32x2>(lds0 + L_CB2 + r32 * 8, cfma(G1, L128, G0));
            FENCE();
            STAMP(5);
            if (HPROBE) {
                u32 pv = lane < 32 ? WS_CS + (u32)(tb64 * 32 + lane) * 512 + 508 : OOB;
                if (PDL) __builtin_amdgcn_s_sleep(PDL);
                if (PGATE) SPINW(peekz2(lds0 + L_ZL + 96) != 0, 1);
#pragma unroll 1
                for (u32 spn = 0; spn < SPIN_CAP; ++spn) {
                    FENCE();
                    u32 v = 0;
                    if (ATOM) asm volatile("buffer_atomic_or %0, %1, %2, 0 offen sc0 sc1\n\ts_waitcnt vmcnt(0)" : "+v"(v) : "v"(pv), "s"(wss) : "memory");
                    else v = __builtin_amdgcn_raw_buffer_load_b32(ws, pv, 0, 16);
                    pv = v == ta ? OOB : pv;
                    const u32 m = (u32)__builtin_amdgcn_ballot_w64(pv != OOB);
                    sig(lds0 + L_ZL + 88, m);
                    if (m == 0) break;
                    if (PSL) __builtin_amdgcn_s_sleep(PSL);
                }
                sig(lds0 + L_ZL + 88, 0);
                STAMP(4);
            }
        }
        __builtin_amdgcn_s_setprio(1);
        const int dsel = uw >> 1, half = uw & 1;
        const int dd = 64 * xcd + half * 32;
        const u32 ta = get_tag(0), tb = tag2(ta);
        const int sel = 2 * idx + dsel, prr = sel >> 4;
        const u32 tbase = (u32)(512 * prr + 64 * (sel & 3) + 4 * ((sel >> 2) & 3));
        u32 yv = (u32)r32 * 4096 + (u32)hh * 16, ovt[4];
#pragma unroll
        for (int r = 0; r < 4; ++r) { ovt[r] = (u32)(16 * hh + r) * 4096 + (u32)r32 * 4; asm volatile("" : "+v"(ovt[r])); }
        asm volatile("" : "+v"(yv));
        STAMP(6);
        if (ABL == 0) {
            STAMP(7);
            if (uw == 3) { wait4s(syncK, 5); if (YGATE) wait4s(syncA, YGATE); __builtin_amdgcn_s_sleep(YDLY); poll_flags<YSL>(ws, wss, WS_YFLAG + (u32)((prr >> 1) * 512 + 64 * xcd + lane) * FS, ta, tb); zsig(wss, voob, lds0 + L_ZL + 76); }
            else SPINW(peek1(lds0 + L_ZL + 76) != 0, 2);
            __builtin_amdgcn_s_setprio(3);
            STAMP(8);
            const u32 so = WS_YT + (u32)dd * 4096 + (u32)sel * 64;
            u32x4 yy[2];
#pragma unroll
            for (int k = 0; k < 2; ++k) yy[k] = __builtin_amdgcn_raw_buffer_load_b128(ws, yv + k * 32, so, 16);
            const rsrc_t ro = mk_rsrc(p.out, 2048 * 4096);
#pragma unroll
            for (int k = 0; k < 2; ++k)
#pragma unroll
                for (int j = 0; j < 8; ++j) {
                    __builtin_amdgcn_raw_buffer_store_b32((j & 1) ? yy[k][j >> 1] & 0xFFFF0000u : yy[k][j >> 1] << 16, ro, ovt[j & 3], (tbase + 32 * k + 256 * (j >> 2)) * 4096 + (u32)(512 + dd) * 4, OAUXB);
                    if (PACE2) { FENCE(); __builtin_amdgcn_s_sleep(PACE2); FENCE(); } }
        }
        STAMP(9);
    } else if (__builtin_expect(wv2 < 8, 1)) {
        const int aw = wv - 4;
        __builtin_amdgcn_s_setprio(PRA);
        u32 fo[4], xo[4], xh[4];
#pragma unroll
        for (int k = 0; k < 4; ++k) { const u32 f = lds0 + (u32)r32 * 128 + (u32)(((2 * k + hh) ^ ((r32 >> 1) & 7)) * 16); fo[k] = f + 81920; xo[k] = f + aw * 4096; xh[k] = xo[k] + 65536; }
#pragma unroll
        for (int k = 0; k < 4; ++k) asm volatile("" : "+v"(fo[k]), "+v"(xo[k]), "+v"(xh[k]));
        u32 mysig = syncA + aw * 4, vsl = lds0 + L_ZL, c1r = lds0 + L_CB1 + r32 * 8, l16 = (u32)lane * 16;
        u32 c1w = c1r + aw * 256, evo = hh ? OOB : (u32)r32 * 16, fvo = lane ? OOB : 0u;
        asm volatile("" : "+v"(mysig), "+v"(vsl), "+v"(c1r), "+v"(c1w), "+v"(evo), "+v"(fvo), "+v"(l16));
        u32x4 idm[2];
#pragma unroll
        for (int s = 0; s < 2; ++s) {
#pragma unroll
            for (int j = 0; j < 4; ++j) { const int dlt = r32 - 16 * hh - 8 * s - 2 * j; idm[s][j] = dlt == 0 ? 0x3F80u : dlt == 1 ? 0x3F800000u : 0u; }
            asm volatile("" : "+v"(idm[s]));
        }
        STAMP(1);
        cf P[4], L8, L16, L32, coef, L128, L256;
        f32x2 cqr[2], cqi[2];
        u32 ta, tb;
        f32x16 accR = {}, accI = {};
        {
            bf16x8 fa[4], fr[4], fi[4];
            auto rd = [&](int n) INL { const int s = n >> 2, k = n & 3;
                fa[k] = lds_ld<bf16x8>(s % 5 < 4 ? xo[k] + (s % 5) * 16384 : xh[k]);
                fr[k] = lds_ld<bf16x8>(fo[k] + (s % 6) * 8192);
                fi[k] = lds_ld<bf16x8>(fo[k] + (s % 6) * 8192 + 4096); };
            {
                    SPINW(peek1(zpar) != 0, 1);
                    asm volatile("" :: "v"(cpf));
#pragma unroll
                    for (int i = 0; i < 3; ++i) pr[i] = lds_ld<u16>(lds0 + L_PAR + 3072 + i * 64 + g32 * 2);
                    sig(mysig, 1);
                    ta = get_tag(0); tb = tag2(ta);
                    const float a_lr = bf2f(pr[0]), a_li = bf2f(pr[1]);
                    const float dt = rbf(__builtin_expf(bf2f(pr[2])));
                    P[0] = cexpk(dt * a_lr, dt * a_li, 1.f);
                    P[1] = csq(P[0]); P[2] = cmul(P[0], P[1]); P[3] = csq(P[1]);
                    L8 = csq(P[3]); L16 = csq(L8); L32 = csq(L16);
                    const float nr = P[0].x - 1.f, ni = P[0].y, inv = 2.f / (a_lr * a_lr + a_li * a_li);
                    coef = cf{(nr * a_lr + ni * a_li) * inv, (ni * a_lr - nr * a_li) * inv};
                    cf Q = hh ? cmul(coef, L16) : coef;
                    { const cf Q1 = cmul(Q, L32); Q = (aw & 1) ? Q1 : Q; }
                    { const cf L64 = csq(L32), Q2 = cmul(Q, L64); Q = (aw & 2) ? Q2 : Q; L128 = csq(L64); L256 = csq(L128); asm volatile("" : "+v"(L128), "+v"(L256)); }
                    const cf q1 = cmul(Q, P[3]), q2 = cmul(Q, L8), q3 = cmul(q1, L8);
                    cqr[0] = f32x2{Q.x, q1.x}; cqi[0] = f32x2{Q.y, q1.y}; cqr[1] = f32x2{q2.x, q3.x}; cqi[1] = f32x2{q2.y, q3.y};
                    asm volatile("" : "+v"(P[0]), "+v"(P[1]), "+v"(P[2]), "+v"(P[3]), "+v"(L8), "+v"(L16), "+v"(L32), "+v"(coef), "+v"(cqr[0]), "+v"(cqi[0]), "+v"(cqr[1]), "+v"(cqi[1]));
                    SBAR();
                }
            SPINW(peekz2(vsl + FIRSTW * 8) != 0, 0);
            STAMP(10);
            u32x2 fl = *(volatile const LAS u32x2*)(uintptr_t)(vsl + 8);
            rd(0); rd(1); rd(2);
            SBAR();
            sfor<0, 32>([&](auto nn) INL {
                constexpr int n = decltype(nn)::v, s = n >> 2, k = n & 3;
                if constexpr (DBG1 && (n == 3 || n == 4 || n == 8 || n == 12 || n == 13 || n == 16 || n == 20 || n == 24)) STAMP(n == 3 ? 4 : n == 4 ? 5 : n == 8 ? 6 : n == 12 ? 7 : n == 13 ? 8 : n == 16 ? 9 : n == 20 ? 11 : 12);
                if constexpr (k == 1 && s < 7) {
                    if (rfl(fl.x | fl.y) != 0) SPINW(peekz2(vsl + (s + 1) * 8) != 0, 0);
                    FENCE();
                }
                if constexpr (k == 2 && s < 6) fl = *(volatile const LAS u32x2*)(uintptr_t)(vsl + (s + 2) * 8);
                accR = MFMA32(fa[k], fr[k], accR);
                SBAR();
                if constexpr (n + 3 < 32) rd(n + 3);
                if constexpr (k == 3 && (s < 3 || s == 4)) zsig(wss, voob, lds0 + L_ZA + (s == 4 ? 3 : s) * 16 + aw * 4);
                accI = MFMA32(fa[k], fi[k], accI);
                SBAR();
            });
        }
        __builtin_amdgcn_s_setprio(3);
        STAMP(3);
        if (ABL == 3) { asm volatile("" :: "v"(accR), "v"(accI)); STAMP_OUT(); return; }
        f32x2 hr[8], hi[8];
#pragma unroll
        for (int i = 0; i < 8; ++i) { hr[i] = f32x2{accR[2 * i], accR[2 * i + 1]}; hi[i] = f32x2{accI[2 * i], accI[2 * i + 1]}; }
#pragma unroll
        for (int i = 2; i < 8; ++i) {
            f32x2 nr = fma2(-P[0].y, hi[i - 2], fma2(P[0].x, hr[i - 2], hr[i]));
            f32x2 ni = fma2(P[0].y, hr[i - 2], fma2(P[0].x, hi[i - 2], hi[i]));
            hr[i] = nr; hi[i] = ni;
        }
        cf e0, e1, pre = {0.f, 0.f};
        {
            const f32x2 zr = fma2(-L8.y, hi[6], fma2(L8.x, hr[6], hr[7])), zi = fma2(L8.y, hr[6], fma2(L8.x, hi[6], hi[7]));
            const cf e16 = {__builtin_fmaf(-P[3].y, zi.x, __builtin_fmaf(P[3].x, zr.x, zr.y)), __builtin_fmaf(P[3].y, zr.x, __builtin_fmaf(P[3].x, zi.x, zi.y))};
            swap_halves(e16, e0, e1);
        }
        const cf e32 = cfma(e0, L16, e1);
        if (aw < 3) { lds_st<f32x2>(c1w, e32); FENCE(); }
        if (aw > 0) {
            cf w0, w1, w2;
#pragma unroll 1
            for (u32 spn = 0; spn < (1u << 22); ++spn) {
                asm volatile("ds_read_b64 %0, %3\n\tds_read_b64 %1, %3 offset:256\n\tds_read_b64 %2, %3 offset:512\n\ts_waitcnt lgkmcnt(0)" : "=&v"(w0), "=&v"(w1), "=&v"(w2) : "v"(c1r) : "memory");
                const u32 a = f2u(w0.x), b = f2u(w1.x), cc = f2u(w2.x);
                const u32 mx = a > b ? (a > cc ? a : cc) : (b > cc ? b : cc);
                if (__builtin_amdgcn_ballot_w64(mx == SENT) == 0) break;
            }
            const cf p2 = cfma(w0, L32, w1), p3 = cfma(p2, L32, w2);
            if (aw == 3 && c < 15) { const cf E = cfma(p3, L32, e32);
#pragma unroll
                for (int r = 0; r < ECP; r += 2) st128<0, true>(wss, u32x4{f2u(E.x), ta, f2u(E.y), tb}, ECP > 1 ? (u32)r32 * 16 + (u32)hh * 131072 : evo, WS_E + (u32)(c * 512 + pb * 32) * 16 + r * 131072); }
            pre = aw == 1 ? w0 : (aw == 2 ? p2 : p3);
        }
        sig(mysig, 3);
        __builtin_amdgcn_s_setprio(PRSH);
        STAMP(4);
        const int th = aw & 1, kh = aw >> 1;
        const int t0 = tb64 * 64 + th * 32, dcol = db * 64 + kh * 32, t32 = tb64 * 2 + th;
        u32 xk[16], dsk = 0;
        {
            const u32x4 rx = mk_srd(p.in[0], NOSK ? 0 : 2048 * 1024), rd = mk_srd(p.in[8], 1024);
            const u32 vx = (u32)hh * 4096 + (u32)r32 * 2;
#pragma unroll
            for (int r = 0; r < 16; ++r) xk[r] = 0;
#pragma unroll
            for (int k = 0; k < 4; ++k)
                asm volatile("buffer_load_short_d16_hi %0, %4, %5, %6 offen\n\tbuffer_load_short_d16_hi %1, %4, %5, %6 offen offset:1024\n\t"
                             "buffer_load_short_d16_hi %2, %4, %5, %6 offen offset:2048\n\tbuffer_load_short_d16_hi %3, %4, %5, %6 offen offset:3072"
                             : "+v"(xk[4 * k]), "+v"(xk[4 * k + 1]), "+v"(xk[4 * k + 2]), "+v"(xk[4 * k + 3]) : "v"(vx), "s"(rx), "s"((u32)t0 * 1024 + (u32)dcol * 2 + k * 8192) : "memory");
            asm volatile("buffer_load_short_d16_hi %0, %1, %2, %3 offen" : "+v"(dsk) : "v"((u32)r32 * 2), "s"(rd), "s"((u32)dcol * 2) : "memory");
        }
        SBAR();
        f32x2 cpr[2], cpi[2];
        {
            { const cf pre1 = cfma(pre, L16, e0); pre = hh ? pre1 : pre; }
            cf s[4];
            s[0] = pre;
            s[1] = cfma(s[0], P[3], cf{hr[6].x, hi[6].x}); s[2] = cfma(s[1], P[3], cf{hr[6].y, hi[6].y}); s[3] = cfma(s[2], P[3], cf{hr[7].x, hi[7].x});
#pragma unroll
            for (int a = 0; a < 4; ++a) s[a] = cmul(s[a], coef);
#pragma unroll
            for (int u = 0; u < 2; ++u) { cpr[u] = f32x2{s[2 * u].x, s[2 * u + 1].x}; cpi[u] = f32x2{s[2 * u].y, s[2 * u + 1].y}; asm volatile("" : "+v"(cpr[u]), "+v"(cpi[u])); }
        }
#pragma unroll
        for (int i = 0; i < 8; ++i) {
            f32x2 nr = fma2(-coef.y, hi[i], coef.x * hr[i]);
            f32x2 ni = fma2(-coef.y, hr[i], -coef.x * hi[i]);
            hr[i] = nr; hi[i] = ni;
            asm volatile("" : "+v"(hr[i]), "+v"(hi[i]));
        }
        u32 ca[4], ov[4];
#pragma unroll
        for (int k = 0; k < 4; ++k) { ca[k] = lds0 + (u32)r32 * 128 + (u32)(((4 * (k >> 1) + 2 * hh + (k & 1)) ^ ((r32 >> 1) & 7)) * 16) + (u32)kh * 65536; ov[k] = (u32)hh * 16384 + (u32)r32 * 4 + k * 4096; asm volatile("" : "+v"(ca[k]), "+v"(ov[k])); }
        u32 xb = lds0 + L_S0 + (u32)aw * S0_SZ + l16, xp = lds0 + L_S0 + (u32)(aw ^ 2) * S0_SZ + l16;
        u32 pvo = lane < 8 ? WS_CS + (u32)(t32 * 16 + kh * 8 + lane) * 512 + 508 : OOB, l8 = (u32)lane * 8;
        asm volatile("" : "+v"(xb), "+v"(xp), "+v"(pvo), "+v"(l8));
        STAMP(2);
        {
            cf H;
#pragma unroll 1
            for (u32 spn = 0; spn < (1u << 24); ++spn) {
                asm volatile("ds_read_b64 %0, %1 offset:768\n\ts_waitcnt lgkmcnt(0)" : "=v"(H) : "v"(c1r) : "memory");
                if (__builtin_amdgcn_ballot_w64(f2u(H.x) == SENT) == 0) break;
            }
            __builtin_amdgcn_s_setprio(3);
            asm volatile("s_waitcnt vmcnt(0)" : "+v"(dsk), "+v"(xk[0]), "+v"(xk[1]), "+v"(xk[2]), "+v"(xk[3]), "+v"(xk[4]), "+v"(xk[5]), "+v"(xk[6]), "+v"(xk[7]), "+v"(xk[8]), "+v"(xk[9]), "+v"(xk[10]), "+v"(xk[11]), "+v"(xk[12]), "+v"(xk[13]), "+v"(xk[14]), "+v"(xk[15]));
            STAMP(5);
            f32x2 gr[2], gi[2];
#pragma unroll
            for (int u = 0; u < 2; ++u) { gr[u] = fma2(-H.y, cqi[u], fma2(H.x, cqr[u], cpr[u])); gi[u] = fma2(H.x, cqi[u], fma2(H.y, cqr[u], cpi[u])); }
#pragma unroll
            for (int i = 0; i < 8; ++i) hr[i] = fma2(-P[i >> 1].y, gi[i & 1], fma2(P[i >> 1].x, gr[i & 1], hr[i]));
            const u32 hs = WS_H + (u32)((c * 4 + aw) * 16 + pb) * 4096;
            f32x16 zr = MFMA32(__builtin_bit_cast(bf16x8, u32x4{pkbf(hr[0].x, hr[2].x), pkbf(hr[4].x, hr[6].x), pkbf(hr[0].y, hr[2].y), pkbf(hr[4].y, hr[6].y)}), __builtin_bit_cast(bf16x8, idm[0]), f32x16{});
            zr = MFMA32(__builtin_bit_cast(bf16x8, u32x4{pkbf(hr[1].x, hr[3].x), pkbf(hr[5].x, hr[7].x), pkbf(hr[1].y, hr[3].y), pkbf(hr[5].y, hr[7].y)}), __builtin_bit_cast(bf16x8, idm[1]), zr);
            SBAR();
#pragma unroll
            for (int i = 0; i < 8; ++i) hi[i] = fma2(-P[i >> 1].y, gr[i & 1], fma2(-P[i >> 1].x, gi[i & 1], hi[i]));
            f32x16 zi = MFMA32(__builtin_bit_cast(bf16x8, u32x4{pkbf(hi[0].x, hi[2].x), pkbf(hi[4].x, hi[6].x), pkbf(hi[0].y, hi[2].y), pkbf(hi[4].y, hi[6].y)}), __builtin_bit_cast(bf16x8, idm[0]), f32x16{});
            zi = MFMA32(__builtin_bit_cast(bf16x8, u32x4{pkbf(hi[1].x, hi[3].x), pkbf(hi[5].x, hi[7].x), pkbf(hi[1].y, hi[3].y), pkbf(hi[5].y, hi[7].y)}), __builtin_bit_cast(bf16x8, idm[1]), zi);
            SBAR();
#define PK2(z, a) __builtin_amdgcn_perm(f2u(z[4 + (a)]), f2u(z[(a)]), 0x07060302u), __builtin_amdgcn_perm(f2u(z[12 + (a)]), f2u(z[8 + (a)]), 0x07060302u)
            const u32x4 o0 = {PK2(zr, 0), PK2(zr, 1)}, o1 = {PK2(zr, 2), PK2(zr, 3)};
            st128<0, true>(wss, o0, l16, hs);
            st128<1024, true>(wss, o1, l16, hs);
            u32 cs = ADD4(ADD4(tb, o0), o1);
            const u32x4 o2 = {PK2(zi, 0), PK2(zi, 1)}, o3 = {PK2(zi, 2), PK2(zi, 3)};
            st128<2048, true>(wss, o2, l16, hs);
            st128<3072, true>(wss, o3, l16, hs);
            cs = ADD4(ADD4(cs, o2), o3);
            __builtin_amdgcn_raw_buffer_store_b64(u32x2{cs, ta}, ws, l8, WS_CS + (u32)((c * 4 + aw) * 16 + pb) * 512, 16);
            FENCE();
            sig(mysig, 4);
            STAMP(13);
        }
        f32x16 sk;
        {
            const float dk = u2f(dsk);
#pragma unroll
            for (int r = 0; r < 16; r += 2) { const u32 w = pkbf(u2f(xk[r]) * dk, u2f(xk[r + 1]) * dk); sk[r] = u2f(w << 16); sk[r + 1] = u2f(w & 0xFFFF0000u); }
        }
        asm volatile("" : "+v"(sk));
        u32 hso[4], rso[4], cof[4];
#pragma unroll
        for (int jj = 0; jj < 4; ++jj) { const u32 cg = (u32)(4 * kh + ((jj + db) & 3));
            hso[jj] = WS_H + (u32)t32 * 65536 + cg * 8192; rso[jj] = WS_CS + (u32)t32 * 8192 + cg * 1024; cof[jj] = (u32)((jj + db - idx) & 3) * 16384;
            asm volatile("" : "+s"(hso[jj]), "+s"(rso[jj]), "+s"(cof[jj])); }
        u32 l16b = l16 + 4096;
        asm volatile("" : "+v"(l16b));
        if (HDLY) __builtin_amdgcn_s_sleep(HDLY);
        SPINW(peekz2(vsl + 64) != 0, 0);
        STAMP(12);
        if (HPROBE) SPINW(((peek1(vsl + 88) >> (16 * th + 8 * kh)) & 0xFFu) != 0, 0);
        if (!SPEC && !HPROBE) {
#pragma unroll 1
        for (u32 spn = 0; spn < SPIN_CAP; ++spn) {
            FENCE();
            const u32 v = __builtin_amdgcn_raw_buffer_load_b32(ws, pvo, 0, PAUX);
            pvo = v == ta ? OOB : pvo;
            if (__builtin_amdgcn_ballot_w64(pvo != OOB) == 0) break;
            if (HSL) __builtin_amdgcn_s_sleep(HSL);
        }
        }
        STAMP(7);
        u32x4 kfl;
        f32x16 y0, y1;
#pragma unroll 1
        for (u32 att = 0; att < SPIN_CAP; ++att) {
            asm volatile("" : "+v"(ca[0]), "+v"(ca[1]), "+v"(ca[2]), "+v"(ca[3]), "+v"(l16), "+v"(l16b), "+v"(l8) :: "memory");
            bf16x8 cb0[4], cb1[4];
            u32x4 hf[16];
            u32 mk = 0, sm = tb, rec[8];
            u32 cab[4];
            auto cad = [&](int f) INL { const int jj = f >> 3, a = (f >> 2) & 1, s = f & 3, k = 2 * a + (s & 1);
                if (s < 2) { cab[k] = ca[k] + cof[jj]; asm volatile("" : "+v"(cab[k])); }
                return cab[k] + (s >> 1) * 8192; };
            auto ldh = [&](int f) INL { return __builtin_amdgcn_raw_buffer_load_b128(ws, ((f & 4) ? l16b : l16) + (f & 3) * 1024, hso[f >> 3], 16); };
            auto ldr = [&](int g) INL { if (NOCHK & 1) return 0u; return __builtin_amdgcn_raw_buffer_load_b32(ws, l8 + (g & 1) * 512, rso[g >> 1], 16); };
            u32 pv0 = 0;
            if (SPEC) { pv0 = __builtin_amdgcn_raw_buffer_load_b32(ws, pvo, 0, PAUX); SBAR(); }
#pragma unroll
            for (int f = 0; f < HWIN; ++f) { if ((f & 3) == 3) rec[f >> 2] = ldr(f >> 2); hf[f] = ldh(f); SBAR(); }
#pragma unroll
            for (int f = 0; f < 3; ++f) { const u32 ad = cad(f); cb0[f] = lds_ld<bf16x8>(ad); cb1[f] = lds_ld<bf16x8>(ad + 4096); }
            SBAR();
            if (SPEC) { pvo = pv0 == ta ? OOB : pvo; if (!(NOCHK & 4) && __builtin_amdgcn_ballot_w64(pvo != OOB) != 0) {
#pragma unroll 1
        for (u32 spn = 0; spn < SPIN_CAP; ++spn) {
            FENCE();
            const u32 v = __builtin_amdgcn_raw_buffer_load_b32(ws, pvo, 0, PAUX);
            pvo = v == ta ? OOB : pvo;
            if (__builtin_amdgcn_ballot_w64(pvo != OOB) == 0) break;
            if (HSL) __builtin_amdgcn_s_sleep(HSL);
        }

                continue; } }
#pragma unroll
            for (int f = 0; f < 32; ++f) {
                if (f + HWIN < 32) { if (((f + HWIN) & 3) == 3) rec[(f + HWIN) >> 2] = ldr((f + HWIN) >> 2); hf[(f + HWIN) & 15] = ldh(f + HWIN); SBAR(); }
                const u32x4 hv = hf[f & 15];
                bf16x8 av = __builtin_bit_cast(bf16x8, hv);
                y0 = MFMA32(av, cb0[f & 3], f ? y0 : f32x16{});
                SBAR();
                if (f + 3 < 32) { const u32 ad = cad(f + 3); cb0[(f + 3) & 3] = lds_ld<bf16x8>(ad); cb1[(f + 3) & 3] = lds_ld<bf16x8>(ad + 4096); }
                SBAR();
                y1 = MFMA32(av, cb1[f & 3], f ? y1 : f32x16{});
                SBAR();
                if (!(NOCHK & 2)) sm = ADD4(sm, hv);
                if ((f & 3) == 3) { mk |= sm ^ rec[f >> 2]; sm = tb; asm volatile("" : "+v"(mk)); } else asm volatile("" : "+v"(sm));
                SBAR();
                if (DBG2 && f == 0) STAMP(6);
                if (DBG2 && f == 9) STAMP(11);
                if (DBG2 && f == 19) STAMP(15);
                if (f == SIG5) { sig(mysig, 5); SBAR(); }
                if (f == 26) { kfl = *(volatile const LAS u32x4*)(uintptr_t)syncK; SBAR(); }
            }
            if (NOCHK || __builtin_amdgcn_ballot_w64(mk != 0) == 0) break;
        }
        STAMP(8);
        {
            if (ABL == 0 && rfl(umin(umin(kfl.x, kfl.y), umin(kfl.z, kfl.w))) < 5) wait4(syncK, 5);
#pragma unroll
            for (int k = 0; k < 4; ++k) lds_st<f32x4>(xb + k * 1024, f32x4{y1[4 * k], y1[4 * k + 1], y1[4 * k + 2], y1[4 * k + 3]});
            sig(mysig, 6);
            y0 += sk;
            asm volatile("" : "+v"(y0));
            wait1(syncA + (aw ^ 2) * 4, 6);
            const rsrc_t ro = mk_rsrc(p.out, 2048 * 4096);
            f32x4 o[4];
#pragma unroll
            for (int k = 0; k < 4; ++k) o[k] = lds_ld<f32x4>(xp + k * 1024);
#pragma unroll
            for (int k = 0; k < 4; ++k) {
#pragma unroll
                for (int j = 0; j < 4; ++j)
                    __builtin_amdgcn_raw_buffer_store_b32(f2u(y0[4 * k + j] + o[k][j]), ro, ov[j], (u32)(t0 + 8 * k) * 4096 + (u32)dcol * 4, OAUX);
            }
            if (tb64 == 31 && db == 0 && aw == 0) __builtin_amdgcn_raw_buffer_store_b32(ta, ws, fvo, WS_W, 16);
        }
        STAMP(9);
    } else {
        const int bw = wv - 8, dsel = bw >> 1, half = bw & 1;
        if (ABL >= 1) return;
        __builtin_amdgcn_s_setprio(PRK);
        const int d = dK;
        const u32 s0 = lds0 + L_S0 + bw * S0_SZ, kc = lds0 + L_KC + dsel * KC_SZ, ub = lds0 + L_UB;
        const u32 mysig = syncK + bw * 4, psig = syncK + (bw ^ 1) * 4;
        lds_st<u32>(kc - 48 + umin(lane, 11) * 4, 0u);
        u32 ga[20];
        {
            const int lag0 = 256 * rr + 128 * half + 127 - 18 * sp;
            const float f0 = (float)lag0;
            const int l2 = (int)kc + 2 * lag0, cap = (int)kc + 256, cb = (int)kc - 2 * (0x4300 - 128);
#pragma unroll
            for (int e = 0; e < 20; e += 2) {
                const int w = (int)pkbf(f0 - (float)e, f0 - (float)(e + 1));
                const int x0 = ((w << 16) >> 15) + cb, x1 = ((w >> 16) << 1) + cb, y0 = l2 - 2 * e, y1 = l2 - 2 * e - 2;
                asm("v_med3_i32 %0, %1, %2, %3" : "=v"(ga[e]) : "v"(x0), "v"(y0), "s"(cap));
                asm("v_med3_i32 %0, %1, %2, %3" : "=v"(ga[e + 1]) : "v"(x1), "v"(y1), "s"(cap));
            }
#pragma unroll
            for (int e = 0; e < 19; ++e) asm volatile("" : "+v"(ga[e]));
        }
        u32 wa = s0 + lane * 4, ra = s0 + (u32)m16 * 288 + (u32)q4 * 16, xa = s0 + lane * 16, xp = lds0 + L_S0 + (bw ^ 1) * S0_SZ + lane * 16, cpa = s0 + 36 * lane;
        u32 k0a = kc + 2 * (128 * (q4 >> 1) + 4 * (q4 & 1) + 8 * m16), k1a = kc + 2 * (q4 < 2 ? 256 + 64 * q4 + 4 * m16 : 384 + 64 * (q4 - 2) + 2 * m16), k1b = k1a + (q4 < 2 ? 4 : 64);
        u32 k2a = kc + 2 * (512 + (q4 == 3 ? 144 : 64 * q4) + m16), para = lds0 + L_PAR + bw * 128 + lane * 2, vms = mysig, vps = psig;
        u32 bb = ub + 512 - 512 * half + 64 * m16 + 32 * q4 + 16 * dsel, ab = s0 + 4 * (592 * (1 - (m16 & 1)) + ((110 - m16 + (m16 & 1)) >> 1) + 4 * q4 - 48);
        asm volatile("" : "+v"(wa), "+v"(ra), "+v"(xa), "+v"(xp), "+v"(cpa), "+v"(k0a), "+v"(k1a), "+v"(k1b), "+v"(k2a), "+v"(para), "+v"(vms), "+v"(vps), "+v"(bb), "+v"(ab));
        STAMP(1);
        SPINW(peek1(zpar) != 0, 1);
#pragma unroll
        for (int i = 0; i < 6; ++i) pr[i] = lds_ld<u16>(para + i * 512);
        pr[6] = lds_ld<u16>(lds0 + L_PAR + 3264 + dsel * 2); wv0 = lds_ld<u16>(lds0 + L_PAR + 3268 + dsel * 2);
        sig(vms, 1);
        const float dt = rbf(__builtin_expf(bf2f(pr[6]))), dsk0 = lane == 0 ? bf2f(wv0) : 0.f;
        STAMP(2);
        f32x4 ka[3];
        {
            const float zr = dt * bf2f(pr[0]), zi = dt * bf2f(pr[1]);
            const cf db_ = {dt * bf2f(pr[2]), -dt * bf2f(pr[3])}, cc = {bf2f(pr[4]), -bf2f(pr[5])};
            const cf E1 = cexpk(zr, zi, 1.f), E8c = cexpk(zr, -zi, 8.f), E128 = cexpk(zr, zi, 128.f);
            cf V[16], R[8], W[17];
            { const cf E16 = csq(E8c), E32 = csq(E16), E64 = csq(E32);
              V[0] = cmul(cc, db_); V[1] = cmul(V[0], E8c);
#pragma unroll
              for (int j = 0; j < 2; ++j) V[2 + j] = cmul(V[j], E16);
#pragma unroll
              for (int j = 0; j < 4; ++j) V[4 + j] = cmul(V[j], E32);
#pragma unroll
              for (int j = 0; j < 8; ++j) V[8 + j] = cmul(V[j], E64); }
            R[0] = cf{1.f, 0.f}; R[1] = E1; R[2] = csq(E1); R[3] = cmul(R[1], R[2]); R[4] = csq(R[2]);
#pragma unroll
            for (int r = 1; r < 4; ++r) R[4 + r] = cmul(R[r], R[4]);
            W[1] = E128; W[2] = csq(W[1]); W[3] = cmul(W[1], W[2]); W[4] = csq(W[2]);
#pragma unroll
            for (int k = 1; k <= 4; ++k) W[4 + k] = cmul(W[k], W[4]);
#pragma unroll
            for (int k = 1; k <= 8; ++k) W[8 + k] = cmul(W[k], W[8]);
            u32 pv[16], p0[16], p1[16], p2[9];
#pragma unroll
            for (int j = 0; j < 16; ++j) pv[j] = pkc(V[j]);
#pragma unroll
            for (int r = 0; r < 8; ++r) { p0[r] = pkc(R[r]); p0[8 + r] = pkc(r ? cmul(R[r], W[1]) : W[1]); }
#pragma unroll
            for (int k = 0; k < 2; ++k)
#pragma unroll
                for (int r = 0; r < 4; ++r) p1[4 * k + r] = pkc(r ? cmul(R[2 * r], W[2 + k]) : W[2 + k]);
#pragma unroll
            for (int k = 0; k < 4; ++k) { p1[8 + 2 * k] = pkc(W[4 + k]); p1[9 + 2 * k] = pkc(cmul(W[4 + k], R[4])); }
#pragma unroll
            for (int k = 0; k < 9; ++k) p2[k] = pkc(W[8 + k]);
            if (KGATE >= 2) {
#pragma unroll
                for (int j = 0; j < 16; ++j) asm volatile("" : "+v"(pv[j]), "+v"(p0[j]), "+v"(p1[j]), "+v"(p2[j < 9 ? j : 0]));
                STAMP(8);
                if (SPF2) {
                    SPINW(peekz2(lds0 + L_ZL + SPFW * 8) != 0, 2);
                    u64 a0 = (u64)(half ? p.in[6] : p.in[5]) + (u32)db * 65536 + (u32)dsel * 32768 + (4u | ((6u + (u32)idx) & 3u)) * 128;
                    u32 dmy = 0;
#pragma unroll 1
                    for (int i = 0; i < SPF2; ++i) {
                        asm volatile("s_load_dword %0, %1, 0x0\n\ts_load_dword %0, %1, 0x40\n\ts_load_dword %0, %1, 0x400\n\ts_load_dword %0, %1, 0x440\n\ts_load_dword %0, %1, 0x800\n\ts_load_dword %0, %1, 0x840\n\ts_load_dword %0, %1, 0xc00\n\ts_load_dword %0, %1, 0xc40\n\t"
                                     "s_load_dword %0, %1, 0x1000\n\ts_load_dword %0, %1, 0x1040\n\ts_load_dword %0, %1, 0x1400\n\ts_load_dword %0, %1, 0x1440\n\ts_load_dword %0, %1, 0x1800\n\ts_load_dword %0, %1, 0x1840\n\ts_load_dword %0, %1, 0x1c00\n\ts_load_dword %0, %1, 0x1c40"
                                     : "+s"(dmy) : "s"(a0));
                        a0 += 8192;
                    }
                    asm volatile("s_waitcnt lgkmcnt(0)" : "+s"(dmy));
                    STAMP(9);
                }
                SPINW(peekz2(lds0 + L_ZL + 56) != 0, 2);
            }
#pragma unroll
            for (int j = 0; j < 16; ++j) lds_st<u32>(wa + j * 288, pv[j]);
            FENCE();
            bf16x8 vf[4], uf[4];
#pragma unroll
            for (int s = 0; s < 4; ++s) vf[s] = lds_ld<bf16x8>(ra + s * 64);
            FENCE();
#pragma unroll
            for (int j = 0; j < 16; ++j) lds_st<u32>(wa + j * 288, p0[j]);
            FENCE();
#pragma unroll
            for (int s = 0; s < 4; ++s) uf[s] = lds_ld<bf16x8>(ra + s * 64);
            FENCE();
#pragma unroll
            for (int j = 0; j < 16; ++j) lds_st<u32>(wa + j * 288, p1[j]);
            FENCE();
            ka[0] = f32x4{};
#pragma unroll
            for (int s = 0; s < 4; ++s) ka[0] = MFMA16(uf[s], vf[s], ka[0]);
#pragma unroll
            for (int s = 0; s < 4; ++s) uf[s] = lds_ld<bf16x8>(ra + s * 64);
            FENCE();
#pragma unroll
            for (int k = 0; k < 9; ++k) lds_st<u32>(wa + k * 288, p2[k]);
            FENCE();
            ka[1] = f32x4{};
#pragma unroll
            for (int s = 0; s < 4; ++s) ka[1] = MFMA16(uf[s], vf[s], ka[1]);
#pragma unroll
            for (int s = 0; s < 4; ++s) uf[s] = lds_ld<bf16x8>(ra + s * 64);
            FENCE();
            ka[2] = f32x4{};
#pragma unroll
            for (int s = 0; s < 4; ++s) ka[2] = MFMA16(uf[s], vf[s], ka[2]);
        }
        STAMP(3);
#pragma unroll
        for (int t = 0; t < 3; ++t) lds_st<f32x4>(xa + t * 1024, ka[t]);
        sig(vms, 2);
        wait1(vps, 2);
        {
#pragma unroll
            for (int t = 0; t < 3; ++t) ka[t] += lds_ld<f32x4>(xp + t * 1024);
            sig(vms, 3);
            ka[0][0] += dsk0;
            lds_st<u32x2>(k0a, u32x2{pkbf(ka[0][0], ka[0][1]), pkbf(ka[0][2], ka[0][3])});
            lds_st<u32>(k1a, pkbf(ka[1][0], ka[1][1]));
            lds_st<u32>(k1b, pkbf(ka[1][2], ka[1][3]));
            const u32 w01 = pkbf(ka[2][0], ka[2][1]), w23 = pkbf(ka[2][2], ka[2][3]);
            lds_st<u16>(k2a, (u16)w01); lds_st<u16>(k2a + 32, (u16)(w01 >> 16)); lds_st<u16>(k2a + 64, (u16)w23); lds_st<u16>(k2a + 96, (u16)(w23 >> 16));
            FENCE();
            u32 gv[19];
#pragma unroll
            for (int e = 0; e < 19; ++e) gv[e] = lds_ld<u16>(ga[e]);
            wait1(vps, 3);
#pragma unroll
            for (int k = 0; k < 9; ++k) {
                lds_st<u32>(cpa + 4 * k, gv[2 * k] | (gv[2 * k + 1] << 16));
                lds_st<u32>(cpa + 2368 + 4 * k, gv[2 * k + 1] | (gv[2 * k + 2] << 16));
            }
            FENCE();
        }
        const u32 ta = get_tag(0), tb = tag2(ta);
        STAMP(4);
        SPINW(peek1(lds0 + L_ZL + 72) != 0, 2);
        if (KCG) wait4(syncA, KCG);
        __builtin_amdgcn_s_setprio(PRCV);
        STAMP(5);
        f32x4 acc[8];
#pragma unroll
        for (int i = 0; i < 8; ++i) acc[i] = f32x4{};
        {
            bf16x8 bfr[2][8], afr[4];
            auto lda = [&](int n) { const int r = n >> 3, i = n & 7; const u32 o = ab + 4 * (72 * i + 16 * (3 - r));
                u32x4 av = {lds_ld<u32>(o), lds_ld<u32>(o + 4), lds_ld<u32>(o + 8), lds_ld<u32>(o + 12)}; return __builtin_bit_cast(bf16x8, av); };
            afr[0] = lda(0);
#pragma unroll
            for (int m = 0; m < 8; ++m) bfr[0][m] = lds_ld<bf16x8>(bb + 1024 * m + 384);
            afr[1] = lda(1); afr[2] = lda(2);
            SBAR();
#pragma unroll
            for (int r = 0; r < 4; ++r) {
                const int cu = r & 1, nx = cu ^ 1;
#pragma unroll
                for (int i = 0; i < 8; ++i) {
                    const int n = 8 * r + i;
                    if (n + 3 < 32) afr[(n + 3) & 3] = lda(n + 3);
                    if (r < 3 && i < 4) { bfr[nx][2 * i] = lds_ld<bf16x8>(bb + 2048 * i + 128 * (2 - r)); bfr[nx][2 * i + 1] = lds_ld<bf16x8>(bb + 2048 * i + 1024 + 128 * (2 - r)); }
#pragma unroll
                    for (int cb = i; cb < 8; ++cb) acc[cb] = MFMA16(afr[n & 3], bfr[cu][cb - i], acc[cb]);
                    SBAR();
                }
            }
        }
        FENCE();
        __builtin_amdgcn_s_setprio(3);
        STAMP(6);
        {
            f32x4 kp[4];
            if (half) {
#pragma unroll
                for (int k = 0; k < 4; ++k) { lds_st<f32x4>(xa + k * 1024, acc[k]); kp[k] = acc[4 + k]; }
            } else {
#pragma unroll
                for (int k = 0; k < 4; ++k) { lds_st<f32x4>(xa + k * 1024, acc[4 + k]); kp[k] = acc[k]; }
            }
            sig(vms, 4);
            wait1(vps, 4);
#pragma unroll
            for (int k = 0; k < 4; ++k) kp[k] += lds_ld<f32x4>(xp + k * 1024);
            sig(vms, 5);
            if (YSG) wait4s(syncA, YSG);
            if (PACE3) { for (int i = 0; i < bw; ++i) __builtin_amdgcn_s_sleep(2 * PACE3); }
            st128<0, true>(wss, u32x4{pkbf(kp[0][0], kp[0][1]), pkbf(kp[0][2], kp[0][3]), pkbf(kp[1][0], kp[1][1]), pkbf(kp[1][2], kp[1][3])}, (u32)lane * 16, WS_YT + (u32)d * 4096 + (u32)half * 2048);
            if (PACE3) __builtin_amdgcn_s_sleep(PACE3);
            st128<1024, true>(wss, u32x4{pkbf(kp[2][0], kp[2][1]), pkbf(kp[2][2], kp[2][3]), pkbf(kp[3][0], kp[3][1]), pkbf(kp[3][2], kp[3][3])}, (u32)lane * 16, WS_YT + (u32)d * 4096 + (u32)half * 2048);
            WAITV(0);
            __builtin_amdgcn_raw_buffer_store_b64(u32x2{ta, tb}, ws, lane ? OOB : 0u, WS_YFLAG + (u32)(half * 512 + d) * FS, 16);
        }
        STAMP(7);
    }
    STAMP_OUT();
}

#if defined(__HIP_DEVICE_COMPILE__) && !PK
#pragma clang attribute pop
#endif

extern "C" void kernel_launch(void* const* d_in, const int* in_sizes, int n_in, void* d_out, int out_size, void* d_ws, size_t ws_size, hipStream_t stream) {
    Params p;
    for (int i = 0; i < 18; ++i) p.in[i] = d_in[i];
    p.out = (float*)d_out;
    p.ws = (char*)d_ws;
    (void)hipFuncSetAttribute((const void*)ssm_fused, hipFuncAttributeMaxDynamicSharedMemorySize, LDS_BYTES);
    ssm_fused<<<256, 768, LDS_BYTES, stream>>>(p);
}
```

```cpp
#include <hip/hip_runtime.h>
#include <stdint.h>

typedef unsigned short u16;
typedef unsigned u32;
typedef unsigned long long u64;
typedef short bf16x8 __attribute__((ext_vector_type(8)));
typedef short s16x4 __attribute__((ext_vector_type(4)));
typedef float f32x2 __attribute__((ext_vector_type(2)));
typedef float f32x4 __attribute__((ext_vector_type(4)));
typedef float f32x16 __attribute__((ext_vector_type(16)));
typedef unsigned u32x2 __attribute__((ext_vector_type(2)));
typedef unsigned u32x4 __attribute__((ext_vector_type(4)));
typedef unsigned u32x8 __attribute__((ext_vector_type(8)));
typedef unsigned u32x16 __attribute__((ext_vector_type(16)));
typedef __bf16 bf16x2_t __attribute__((ext_vector_type(2)));
#define LAS __attribute__((address_space(3)))
typedef __amdgpu_buffer_rsrc_t rsrc_t;

constexpr int SLM = 3;
constexpr int HWIN = 9;
constexpr int EDLY = 10;
constexpr int CDLY = 8;
constexpr int PGATE = 6;
constexpr int SIG5 = 4;
constexpr int LDLY = 3;

constexpr u32 WS_W = 0;
constexpr u32 FS = 128;
constexpr u32 WS_UFLAG = 131072;
constexpr u32 WS_YFLAG = 262144;
constexpr u32 WS_E = 524288;
constexpr u32 WS_UT = 1u << 20;
constexpr u32 WS_YT = 3u << 20;
constexpr u32 WS_H = 5u << 20;
constexpr u32 WS_CS = 9u << 20;
constexpr u32 WS_BYTES = 10u << 20;
constexpr u32 OOB = 0x7FFFFF00u;
constexpr u32 L_S0 = 131072, S0_SZ = 4672;
constexpr u32 L_KC = L_S0 + 4 * S0_SZ + 48, KC_SZ = 1488;
constexpr u32 L_CB1 = L_KC + 2 * KC_SZ - 48;
constexpr u32 L_CB2 = L_CB1 + 768;
constexpr u32 L_ZL = L_CB2 + 256;
constexpr u32 L_ZA = L_ZL + 128;
constexpr u32 L_SYNC = L_ZA + 128;
constexpr u32 L_UB = L_SYNC + 64, UB_PAD = 960;
constexpr u32 L_W = L_UB + UB_PAD + 8192;
constexpr u32 L_PAR = L_S0;
constexpr u32 LDS_BYTES = 163840;
static_assert(L_W + 16 <= LDS_BYTES && L_CB1 % 16 == 0, "lds");
constexpr u32 Z_C = 64, Z_UT = 72, Z_Y = 76, Z_PAR = 84, Z_HM = 88, Z_EOK = 92, Z_PG = 96;
constexpr u32 SPIN_CAP = 1u << 15;
constexpr u32 CODE_BYTES = 25600;
constexpr u32 SENT = 0xFFFFFFFFu;

struct Params { const void* in[18]; float* out; char* ws; };

#if defined(__HIP_DEVICE_COMPILE__)
#pragma clang attribute push(__attribute__((target("no-packed-fp32-ops"))), apply_to = function)
#endif
template <int I> struct ic { static constexpr int v = I; };
template <int B, int E, typename F> __device__ __forceinline__ void sfor(F&& f) { if constexpr (B < E) { f(ic<B>{}); sfor<B + 1, E>(f); } }
#define INL __attribute__((always_inline))
__device__ __forceinline__ u32 umin(u32 a, u32 b) { return a < b ? a : b; }
__device__ __forceinline__ u32 umax(u32 a, u32 b) { return a > b ? a : b; }
__device__ __forceinline__ u32 umax3(u32 a, u32 b, u32 c) { return umax(umax(a, b), c); }
__device__ __forceinline__ float u2f(u32 v) { return __builtin_bit_cast(float, v); }
__device__ __forceinline__ u32 f2u(float v) { return __builtin_bit_cast(u32, v); }

__device__ __forceinline__ float bf2f(u32 v) { return u2f(v << 16); }
__device__ __forceinline__ u32 pkbf(float lo, float hi) { f32x2 a = {lo, hi}; return __builtin_bit_cast(u32, __builtin_convertvector(a, bf16x2_t)); }
__device__ __forceinline__ float rbf(float x) { return u2f(pkbf(x, x) << 16); }
__device__ __forceinline__ u32x4 mk_srd(const void* p, u32 bytes) { u64 a = (u64)p; u32x4 r = {(u32)a, (u32)(a >> 32) & 0xFFFFu, bytes, 0x00020000u}; return r; }
__device__ __forceinline__ rsrc_t mk_rsrc(const void* p, u32 bytes) { return __builtin_amdgcn_make_buffer_rsrc((void*)p, 0, bytes, 0x00020000); }
__device__ __forceinline__ void dma4(u32x4 srd, u32 v0, u32 v1, u32 soff, u32 ldst) {
    asm volatile("s_mov_b32 m0, %0\n\ts_nop 0\n\t"
                 "buffer_load_dwordx4 %1, %3, %4 offen lds\n\t"
                 "buffer_load_dwordx4 %2, %3, %4 offen offset:1024 lds\n\t"
                 "buffer_load_dwordx4 %1, %3, %4 offen offset:2048 lds\n\t"
                 "buffer_load_dwordx4 %2, %3, %4 offen offset:3072 lds"
                 :: "s"(ldst), "v"(v0), "v"(v1), "s"(srd), "s"(soff) : "m0", "memory");
}
__device__ __forceinline__ void dma4n(u32x4 srd, const u32 (&v)[4], u32 soff, u32 ldst) {
    asm volatile("s_mov_b32 m0, %0\n\ts_nop 0\n\t"
                 "buffer_load_dwordx4 %1, %5, %6 offen lds\n\t"
                 "buffer_load_dwordx4 %2, %5, %6 offen offset:1024 lds\n\t"
                 "buffer_load_dwordx4 %3, %5, %6 offen offset:2048 lds\n\t"
                 "buffer_load_dwordx4 %4, %5, %6 offen offset:3072 lds"
                 :: "s"(ldst), "v"(v[0]), "v"(v[1]), "v"(v[2]), "v"(v[3]), "s"(srd), "s"(soff) : "m0", "memory");
}
__device__ __forceinline__ void zsig(u32x4 srd, u32 voob, u32 ldst) {
    asm volatile("s_mov_b32 m0, %0\n\ts_mov_b64 exec, 1\n\tbuffer_load_dword %1, %2, 0 offen lds\n\ts_mov_b64 exec, -1" :: "s"(ldst), "v"(voob), "s"(srd) : "m0", "memory");
}
template <int IMM> __device__ __forceinline__ void st128(u32x4 srd, u32x4 v, u32 voff, u32 soff) {
    asm volatile("buffer_store_dwordx4 %0, %1, %2, %3 offen offset:%4 sc1\n\ts_nop 1" :: "v"(v), "v"(voff), "s"(srd), "s"(soff), "i"(IMM) : "memory");
}
#define SBAR() __builtin_amdgcn_sched_barrier(0)
#define WAITV(N) asm volatile("s_waitcnt vmcnt(%0)" :: "i"(N) : "memory")
#define FENCE() asm volatile("" ::: "memory")
template <typename T> __device__ __forceinline__ T lds_ld(u32 a) { return *(const LAS T*)(uintptr_t)a; }
template <typename T> __device__ __forceinline__ void lds_st(u32 a, T v) { *(LAS T*)(uintptr_t)a = v; }
__device__ __forceinline__ u32x2 lds_tr(u32 a) { return __builtin_bit_cast(u32x2, __builtin_amdgcn_ds_read_tr16_b64_v4i16((LAS s16x4*)(uintptr_t)a)); }
__device__ __forceinline__ void sig(u32 addr, u32 val) { asm volatile("ds_write_b32 %0, %1" :: "v"(addr), "v"(val) : "memory"); }
__device__ __forceinline__ u32 rfl(u32 v) { return (u32)__builtin_amdgcn_readfirstlane(v); }
__device__ __forceinline__ u32 peek4(u32 addr) { u32x4 v; asm volatile("ds_read_b128 %0, %1\n\ts_waitcnt lgkmcnt(0)" : "=v"(v) : "v"(addr) : "memory"); return rfl(umin(umin(v.x, v.y), umin(v.z, v.w))); }
__device__ __forceinline__ u32 peek1(u32 addr) { u32 v; asm volatile("ds_read_b32 %0, %1\n\ts_waitcnt lgkmcnt(0)" : "=v"(v) : "v"(addr) : "memory"); return rfl(v); }
__device__ __forceinline__ u32 peekz2(u32 addr) { u32x2 v; asm volatile("ds_read_b64 %0, %1\n\ts_waitcnt lgkmcnt(0)" : "=v"(v) : "v"(addr) : "memory"); return rfl(v.x | v.y); }
__device__ __forceinline__ u32 peekz4(u32 addr) { u32x4 v; asm volatile("ds_read_b128 %0, %1\n\ts_waitcnt lgkmcnt(0)" : "=v"(v) : "v"(addr) : "memory"); return rfl(v.x | v.y | v.z | v.w); }
#define SPINW(cond, slp) do { _Pragma("unroll 1") for (u32 sp_ = 0; (cond) && sp_ < (1u << 22); ++sp_) { if (slp) __builtin_amdgcn_s_sleep((slp) * SLM); } } while (0)
__device__ __forceinline__ void wait1(u32 addr, u32 val) { SPINW(peek1(addr) < val, 0); }
__device__ __forceinline__ u32 tag2(u32 t) { return t * 2654435761u + 0x2545F491u; }
typedef f32x2 cf;
__device__ __forceinline__ cf cmul(cf a, cf b) { return cf{__builtin_fmaf(-a.y, b.y, a.x * b.x), __builtin_fmaf(a.y, b.x, a.x * b.y)}; }
__device__ __forceinline__ cf csq(cf a) { return cf{__builtin_fmaf(-a.y, a.y, a.x * a.x), (a.x + a.x) * a.y}; }
__device__ __forceinline__ cf cfma(cf a, cf b, cf c) { return cf{__builtin_fmaf(-a.y, b.y, __builtin_fmaf(a.x, b.x, c.x)), __builtin_fmaf(a.y, b.x, __builtin_fmaf(a.x, b.y, c.y))}; }
__device__ __forceinline__ f32x2 fma2(float a, f32x2 b, f32x2 c) { return f32x2{__builtin_fmaf(a, b.x, c.x), __builtin_fmaf(a, b.y, c.y)}; }
__device__ __forceinline__ u32 pkc(cf a) { return __builtin_bit_cast(u32, __builtin_convertvector(a, bf16x2_t)); }
__device__ __forceinline__ cf cexpk(float zr, float zi, float k) {
    float m = __builtin_amdgcn_exp2f(zr * (k * 1.4426950408889634f));
    float f = __builtin_amdgcn_fractf(zi * (k * 0.15915494309189535f));
    return cf{m * __builtin_amdgcn_cosf(f), m * __builtin_amdgcn_sinf(f)};
}
__device__ __forceinline__ void swap_halves(cf v, cf& lo, cf& hi) {
    auto a = __builtin_amdgcn_permlane32_swap(f2u(v.x), f2u(v.x), false, false);
    auto b = __builtin_amdgcn_permlane32_swap(f2u(v.y), f2u(v.y), false, false);
    lo = cf{u2f(a[0]), u2f(b[0])}; hi = cf{u2f(a[1]), u2f(b[1])};
}
template <int SL> __device__ __forceinline__ void poll_flags(u32x4 wsd, u32 voff, u32 ta, u32 tb) {
#pragma unroll 1
    for (u32 s = 0; s < SPIN_CAP; ++s) {
        u32x2 v = {0u, 0u};
        asm volatile("buffer_atomic_or_x2 %0, %1, %2, 0 offen sc0 sc1\n\ts_waitcnt vmcnt(0)" : "+v"(v) : "v"(voff), "s"(wsd) : "memory");
        voff = ((v.x ^ ta) | (v.y ^ tb)) == 0 ? OOB : voff;
        if (__builtin_amdgcn_ballot_w64(voff != OOB) == 0) break;
        __builtin_amdgcn_s_sleep(SL);
    }
}
#define ADD4(s, v) (((s) + (v).x + (v).y) + (v).z + (v).w)
#define MFMA32(a, b, c) __builtin_amdgcn_mfma_f32_32x32x16_bf16(a, b, c, 0, 0, 0)
#define MFMA16(a, b, c) __builtin_amdgcn_mfma_f32_16x16x32_bf16(a, b, c, 0, 0, 0)

__global__ __launch_bounds__(768, 3) void ssm_fused(Params) {
    const auto kseg = __builtin_amdgcn_kernarg_segment_ptr();
    extern __shared__ __attribute__((aligned(16))) char smem[];
    const u32 lds0 = (u32)(uintptr_t)(LAS char*)smem;
    const u32 tid = __builtin_amdgcn_workitem_id_x();
    const int lane = tid & 63;
    const int hw = __builtin_amdgcn_readfirstlane(tid >> 6);
    const u32 bid = __builtin_amdgcn_workgroup_id_x();
    u32x16 k0, k1; u32x8 k2; u32x4 cpf;
    if (hw >= 8) {
        asm volatile("s_load_dwordx16 %0, %3, 0x0\n\ts_load_dwordx16 %1, %3, 0x40\n\ts_load_dwordx8 %2, %3, 0x80\n\ts_setprio 3\n\ts_barrier" : "=&s"(k0), "=&s"(k1), "=&s"(k2) : "s"(kseg) : "memory");
    } else {
        u64 pc0; asm volatile("s_getpc_b64 %0" : "=s"(pc0));
        cpf = __builtin_amdgcn_raw_buffer_load_b128(mk_rsrc((const void*)(pc0 & ~255ull), hw < 2 ? CODE_BYTES : 0u), (u32)lane * 16, ((bid >> 3) ^ ((u32)hw * 16)) * 1024, 0);
        const u32 iv = tid < 160 ? SENT : 0u;
        lds_st<u32x2>(lds0 + L_CB1 + umin(tid, 287u) * 8, u32x2{iv, iv});
        asm volatile("s_waitcnt lgkmcnt(0)\n\ts_barrier" ::: "memory");
    }
    const int xcd = bid & 7, idx = bid >> 3;
    const u32 syncE = lds0 + L_SYNC, syncA = syncE + 16, syncK = syncE + 32, zl0 = lds0 + L_ZL;
    const int rr = lane >> 3, sp = lane & 7;
    const int m16 = lane & 15, q4 = lane >> 4;
    const int r32 = lane & 31, hh = lane >> 5;
    const int g32 = 4 * (r32 & 7) + (r32 >> 3);
    const int ppos = (xcd < 4 ? xcd : (xcd ^ 2)) >> 1;
    const int c = 4 * ppos + (idx & 3), pb = 8 * (xcd & 1) + (idx >> 2);
    const int tb64 = 8 * ppos + 4 * (xcd & 1) + (idx & 3), db = idx >> 2;
    u32 voob = OOB;
#define LATE_P() auto kl_ = kseg; asm volatile("" : "+s"(kl_)); const __attribute__((address_space(4))) Params& p = *(const __attribute__((address_space(4))) Params*)kl_
#define GET_TAG() (peek1(lds0 + L_W) + 1u)
    asm volatile("" : "+v"(voob));
    if (hw == 9) {
        u32 vq0 = (u32)(64 * xcd + 2 * idx) * 256 + (u32)lane * 16, vq1 = (u32)pb * 64 + (u32)lane * 16, vq2 = (u32)(64 * xcd + 2 * idx) * 2;
        asm volatile("s_waitcnt lgkmcnt(0)\n\t"
                     "s_mov_b32 m0, %0\n\ts_mov_b32 exec_hi, 0\n\tglobal_load_lds_dwordx4 %1, %[b0]\n\t"
                     "s_add_u32 m0, m0, 512\n\ts_nop 0\n\tglobal_load_lds_dwordx4 %1, %[b1]\n\t"
                     "s_add_u32 m0, m0, 512\n\ts_nop 0\n\tglobal_load_lds_dwordx4 %1, %[b2]\n\t"
                     "s_add_u32 m0, m0, 512\n\ts_nop 0\n\tglobal_load_lds_dwordx4 %1, %[b3]\n\t"
                     "s_add_u32 m0, m0, 512\n\ts_nop 0\n\tglobal_load_lds_dwordx4 %1, %[b4]\n\t"
                     "s_add_u32 m0, m0, 512\n\ts_nop 0\n\tglobal_load_lds_dwordx4 %1, %[b5]\n\t"
                     "s_add_u32 m0, m0, 512\n\ts_mov_b32 exec_lo, 15\n\tglobal_load_lds_dwordx4 %2, %[a0]\n\t"
                     "s_add_u32 m0, m0, 64\n\ts_nop 0\n\tglobal_load_lds_dwordx4 %2, %[a1]\n\t"
                     "s_add_u32 m0, m0, 64\n\ts_nop 0\n\tglobal_load_lds_dwordx4 %2, %[a2]\n\t"
                     "s_add_u32 m0, m0, 64\n\ts_mov_b32 exec_lo, 1\n\tglobal_load_lds_dword %3, %[c0]\n\t"
                     "s_add_u32 m0, m0, 4\n\ts_nop 0\n\tglobal_load_lds_dword %3, %[c1]\n\t"
                     "s_mov_b32 m0, %4\n\ts_nop 0\n\tglobal_load_lds_dword %5, %[w] sc1\n\ts_mov_b64 exec, -1"
                     :: "s"(lds0 + L_PAR), "v"(vq0), "v"(vq1), "v"(vq2), "s"(lds0 + L_W), "v"(0u),
                        [b0] "s"(k1.s45), [b1] "s"(k1.s67), [b2] "s"(k1.s89), [b3] "s"(k1.sab), [b4] "s"(k1.scd), [b5] "s"(k1.sef),
                        [a0] "s"(k0.s23), [a1] "s"(k0.s45), [a2] "s"(k0.sef), [c0] "s"(k2.s01), [c1] "s"(k2.s23), [w] "s"(k2.s67), "s"(k0), "s"(k1), "s"(k2) : "m0", "scc", "memory");
        WAITV(0);
        sig(zl0 + Z_PAR, 0);
    } else if (hw == 8) {
        u32 vu = (u32)(idx * 64 + rr) * 1024 + (u32)xcd * 128 + (u32)((sp ^ ((rr >> 1) & 1)) * 16);
        asm volatile("s_waitcnt lgkmcnt(0)\n\t"
                     "s_mov_b32 m0, %0\n\ts_nop 0\n\tglobal_load_lds_dwordx4 %1, %9\n\t"
                     "s_add_u32 m0, m0, 1024\n\ts_nop 0\n\tglobal_load_lds_dwordx4 %2, %9\n\t"
                     "s_add_u32 m0, m0, 1024\n\ts_nop 0\n\tglobal_load_lds_dwordx4 %3, %9\n\t"
                     "s_add_u32 m0, m0, 1024\n\ts_nop 0\n\tglobal_load_lds_dwordx4 %4, %9\n\t"
                     "s_add_u32 m0, m0, 1024\n\ts_nop 0\n\tglobal_load_lds_dwordx4 %5, %9\n\t"
                     "s_add_u32 m0, m0, 1024\n\ts_nop 0\n\tglobal_load_lds_dwordx4 %6, %9\n\t"
                     "s_add_u32 m0, m0, 1024\n\ts_nop 0\n\tglobal_load_lds_dwordx4 %7, %9\n\t"
                     "s_add_u32 m0, m0, 1024\n\ts_nop 0\n\tglobal_load_lds_dwordx4 %8, %9"
                     :: "s"(lds0 + L_UB + UB_PAD), "v"(vu), "v"((vu ^ 32) + 8192), "v"((vu ^ 64) + 16384), "v"((vu ^ 96) + 24576), "v"(vu + 32768), "v"((vu ^ 32) + 40960), "v"((vu ^ 64) + 49152), "v"((vu ^ 96) + 57344),
                        "s"(k1.s23), "s"(k0), "s"(k1), "s"(k2) : "m0", "scc", "memory");
    } else if (hw >= 10) {
        const u32 lw = (u32)hw & 1u ^ 1u;
        u32 vd0 = (u32)rr * 4096 + (u32)((sp ^ (rr >> 1)) * 16), vd1 = vd0 ^ 64, za = lds0 + L_ZA;
        u32 xs = (u32)c * 131072, bs = (u32)pb * 32768, r0 = ((u32)((idx >> 2) + 2 * (idx & 3)) & 7u) * 128;
        asm volatile("" : "+s"(xs), "+s"(bs));
        u32 o0 = lw ? xs + 32768 : bs, o1 = lw ? xs + 65536 : bs, o2 = xs + lw * 98304, nrec = lw ? 2048 * 1024 : 512 * 1024;
        u32 l0 = lds0 + (lw ? 4096 : 81920 + 8192), lstep = lw ? 16384 : 8192, lwrap = lw ? 81920 : 49152, adv = lw ? 0 : 128;
        u32 lx = lds0 + lw * 12288, zl = zl0 + lw * 4;
        asm volatile("" : "+v"(vd0), "+v"(vd1), "+v"(za));
        asm volatile("" : "+s"(o0), "+s"(o1), "+s"(o2), "+s"(r0), "+s"(nrec), "+s"(l0), "+s"(lstep), "+s"(lwrap), "+s"(adv), "+s"(lx), "+s"(zl));
        u32x2 px, pa, pb_, pc_;
        asm volatile("s_waitcnt lgkmcnt(0)\n\ts_cmp_lg_u32 %4, 0\n\ts_mov_b64 %0, %5\n\ts_cselect_b64 %1, %5, %6\n\ts_cselect_b64 %2, %5, %7\n\ts_cselect_b64 %3, %9, %8"
                     : "=&s"(px), "=&s"(pa), "=&s"(pb_), "=&s"(pc_) : "s"(lw), "s"(k0.s01), "s"(k0.s67), "s"(k0.s89), "s"(k0.sab), "s"(k0.scd), "s"(k0), "s"(k1), "s"(k2) : "scc", "memory");
        __builtin_amdgcn_s_sleep(LDLY);
        const u32x4 sx = {px.x, px.y & 0xFFFFu, 2048 * 1024, 0x00020000u}, s0 = {pa.x, pa.y & 0xFFFFu, nrec, 0x00020000u}, s1 = {pb_.x, pb_.y & 0xFFFFu, nrec, 0x00020000u};
        if (lw == 0) { dma4(s0, vd0, vd1, bs + r0, lds0 + 81920); dma4(s1, vd0, vd1, bs + r0, lds0 + 81920 + 4096); }
        const u32x4 sc = {pc_.x, pc_.y & 0xFFFFu, 512 * 1024, 0x00020000u};
        u32x4 fl = {0u, 0u, 0u, 0u};
#pragma unroll 1
        for (int s = 0; s < 8; ++s) {
            const u32 ra = (r0 + adv) & 0x380u;
            if (s >= 5) { if (rfl(fl.x | fl.y | fl.z | fl.w) != 0) SPINW(peekz4(za) != 0, 0); za += 16; if (s == 5) { lx -= 81920; l0 -= lwrap; } }
            if (s < 7 || lw) { dma4(s0, vd0, vd1, o0 + ra, l0); dma4(s1, vd0, vd1, o1 + ra, l0 + 4096); }
            fl = *(volatile const LAS u32x4*)(uintptr_t)za;
            dma4(sx, vd0, vd1, o2 + r0, lx);
            zsig(sx, voob, zl);
            r0 = (r0 + 128) & 0x380u; lx += 16384; l0 += lstep; zl += 8;
        }
        u32 vc[4];
#pragma unroll
        for (int b = 0; b < 4; ++b) vc[b] = (((u32)rr * 1024 + (u32)((sp ^ (rr >> 1)) * 16)) ^ ((b & 1) * 64)) + b * 7168;
        if (rfl(fl.x | fl.y | fl.z | fl.w) != 0) SPINW(peekz4(za) != 0, 0);
#pragma unroll 1
        for (u32 i = 0; i < 8; ++i) {
            const u32 j = (0x57462103u >> (4 * i)) & 15u;
            if (i == 4) SPINW(peek1(zl0 + Z_EOK) != 0, 0);
            if (i == 1) { SPINW(peek4(syncA) < 1, 0); __builtin_amdgcn_s_sleep(CDLY); }
            if (i == PGATE) zsig(sc, voob, zl0 + Z_PG + lw * 4);
            const u32 so = (u32)db * 65536 + ((j & 4) | ((j + (u32)idx) & 3)) * 128, ld = lds0 + j * 16384 + lw * 8192 + (j >> 2) * 4096;
            dma4n(sc, vc, so, ld);
            dma4n(sc, vc, so + 32768, ld ^ 4096);
        }
        WAITV(0);
        sig(zl0 + Z_C + lw * 4, 0);
    }
    LATE_P();
    const rsrc_t ws = mk_rsrc(p.ws, WS_BYTES);
    const u32x4 wss = mk_srd(p.ws, WS_BYTES);
    int hw2 = hw;
    asm volatile("" : "+s"(hw2));
    if (hw2 >= 8) {
        const int uw = hw - 8;
        SPINW(peek1(zl0 + Z_PAR) != 0, 1);
        const u32 ta = GET_TAG(), tb = tag2(ta);
        u32x4 ut[8];
        if (uw == 0) {
            const u32 im0 = lds0 + L_UB + UB_PAD;
            const u32 ra = (u32)(8 * (lane & 3) + (m16 >> 2)) * 128 + (u32)(((q4 >> 1) ^ (2 * (lane & 3) + ((m16 >> 3) & 1))) * 16) + (u32)(q4 & 1) * 8;
            WAITV(0);
#pragma unroll
            for (int k = 0; k < 8; ++k) { const u32 a = (ra ^ ((k & 3) * 32)) + im0 + (k >> 2) * 4096; const u32x2 x = lds_tr(a), y = lds_tr(a + 512); ut[k] = u32x4{x.x, x.y, y.x, y.y}; }
#pragma unroll
            for (int k = 0; k < 8; ++k) st128<0>(wss, ut[k], (u32)q4 * 16384 + (u32)m16 * 16, WS_UT + (u32)(16 * xcd + 4 * (k & 3)) * 16384 + (u32)(2 * idx + (k >> 2)) * 256);
            WAITV(0);
            __builtin_amdgcn_raw_buffer_store_b64(u32x2{ta, tb}, ws, lane ? OOB : 0u, WS_UFLAG + (u32)(xcd * 32 + idx) * FS, 16);
            FENCE();
            SPINW(peekz2(zl0 + 56) != 0, 2);
            poll_flags<1>(wss, lane < 32 ? WS_UFLAG + (u32)(xcd * 32 + lane) * FS : OOB, ta, tb);
            const u32 vt = (u32)(lane >> 1) * 64 + (u32)(lane & 1) * 16, so = WS_UT + (u32)(16 * xcd + (idx >> 1)) * 16384 + (u32)(idx & 1) * 32;
#pragma unroll
            for (int k = 0; k < 8; ++k) ut[k] = __builtin_amdgcn_raw_buffer_load_b128(ws, vt + (k & 1) * 2048, so + (k >> 1) * 4096, 16);
#pragma unroll
            for (int k = 0; k < 8; ++k) lds_st<u32x4>(im0 + k * 1024 + lane * 16, ut[k]);
            sig(zl0 + Z_UT, 0);
        } else if (uw == 1) {
            {
                u32 pr[3];
#pragma unroll
                for (int i = 0; i < 3; ++i) pr[i] = lds_ld<u16>(lds0 + L_PAR + 3072 + i * 64 + g32 * 2);
                sig(syncE, 1);
                cf L128, L256;
                {
                    const float dt = rbf(__builtin_expf(bf2f(pr[2])));
                    L128 = cexpk(dt * bf2f(pr[0]), dt * bf2f(pr[1]), 1.f);
#pragma unroll
                    for (int i = 0; i < 7; ++i) L128 = csq(L128);
                    L256 = csq(L128);
                }
                u32 vo[8];
                {
                    const int cb0 = c - 1 - hh;
                    const u32 vo0 = WS_E + (u32)(cb0 * 512 + pb * 32 + r32) * 16;
#pragma unroll
                    for (int k = 0; k < 8; ++k) { vo[k] = 2 * k <= cb0 ? vo0 - (u32)k * 16384 : OOB; asm volatile("" : "+v"(vo[k])); }
                }
                asm volatile("" : "+v"(L128), "+v"(L256));
                SPINW(peek1(lds0 + L_CB1 + 512) == SENT, 1);
                if (c) __builtin_amdgcn_s_sleep(EDLY);
#pragma unroll 1
                for (u32 spn = 0; spn < SPIN_CAP; ++spn) {
                    FENCE();
#pragma unroll
                    for (int k = 0; k < 8; ++k) ut[k] = __builtin_amdgcn_raw_buffer_load_b128(ws, vo[k], 0, 16);
                    u32 bad = 0;
#pragma unroll
                    for (int k = 0; k < 8; ++k) bad |= vo[k] != OOB ? ((ut[k].y ^ ta) | (ut[k].w ^ tb)) : 0u;
                    if (__builtin_amdgcn_ballot_w64(bad != 0) == 0) break;
                }
                cf G = {u2f(ut[7].x), u2f(ut[7].z)}, G0, G1;
#pragma unroll
                for (int k = 6; k >= 0; --k) G = cfma(G, L256, cf{u2f(ut[k].x), u2f(ut[k].z)});
                swap_halves(G, G0, G1);
                lds_st<f32x2>(lds0 + L_CB2 + r32 * 8, cfma(G1, L128, G0));
                sig(zl0 + Z_EOK, 0);
                FENCE();
            }
            u32 pv = lane < 32 ? WS_CS + (u32)(tb64 * 32 + lane) * 512 + 508 : OOB;
            SPINW(peekz2(zl0 + Z_PG) != 0, 1);
#pragma unroll 1
            for (u32 spn = 0; spn < SPIN_CAP; ++spn) {
                u32 v = 0;
                asm volatile("buffer_atomic_or %0, %1, %2, 0 offen sc0 sc1\n\ts_waitcnt vmcnt(0)" : "+v"(v) : "v"(pv), "s"(wss) : "memory");
                pv = v == ta ? OOB : pv;
                const u32 m = (u32)__builtin_amdgcn_ballot_w64(pv != OOB);
                sig(zl0 + Z_HM, m);
                if (m == 0) break;
            }
            sig(zl0 + Z_HM, 0);
        }
        __builtin_amdgcn_s_setprio(1);
        const int dd = 64 * xcd + (uw & 1) * 32;
        const int sel = 2 * idx + (uw >> 1), prr = sel >> 4;
        const u32 tbase = (u32)(512 * prr + 64 * (sel & 3) + 4 * ((sel >> 2) & 3));
        u32 yv = (u32)r32 * 4096 + (u32)hh * 16, ovt[4];
#pragma unroll
        for (int r = 0; r < 4; ++r) { ovt[r] = (u32)(16 * hh + r) * 4096 + (u32)r32 * 4; asm volatile("" : "+v"(ovt[r])); }
        asm volatile("" : "+v"(yv));
        if (uw == 0) { SPINW(peek4(syncK) < 5, 2); SPINW(peek4(syncA) < 3, 2); poll_flags<2>(wss, WS_YFLAG + (u32)((prr >> 1) * 512 + 64 * xcd + lane) * FS, ta, tb); sig(zl0 + Z_Y, 0); }
        else SPINW(peek1(zl0 + Z_Y) != 0, 1);
        __builtin_amdgcn_s_setprio(3);
        {
            const u32 so = WS_YT + (u32)dd * 4096 + (u32)sel * 64;
            u32x4 yy[2];
#pragma unroll
            for (int k = 0; k < 2; ++k) yy[k] = __builtin_amdgcn_raw_buffer_load_b128(ws, yv + k * 32, so, 16);
            const rsrc_t ro = mk_rsrc(p.out, 2048 * 4096);
#pragma unroll
            for (int k = 0; k < 2; ++k)
#pragma unroll
                for (int j = 0; j < 8; ++j)
                    __builtin_amdgcn_raw_buffer_store_b32((j & 1) ? yy[k][j >> 1] & 0xFFFF0000u : yy[k][j >> 1] << 16, ro, ovt[j & 3], (tbase + 32 * k + 256 * (j >> 2)) * 4096 + (u32)(512 + dd) * 4, 16);
        }
    } else if (hw2 < 4) {
        const int aw = hw, th = aw & 1, kh = aw >> 1;
        const int t0 = tb64 * 64 + th * 32, dcol = db * 64 + kh * 32, t32 = tb64 * 2 + th;
        __builtin_amdgcn_s_setprio(3);
        u32 fo[4], xo[4], xh[4];
#pragma unroll
        for (int k = 0; k < 4; ++k) { const u32 f = lds0 + (u32)r32 * 128 + (u32)(((2 * k + hh) ^ ((r32 >> 1) & 7)) * 16); fo[k] = f + 81920; xo[k] = f + aw * 4096; xh[k] = xo[k] + 65536; }
#pragma unroll
        for (int k = 0; k < 4; ++k) asm volatile("" : "+v"(fo[k]), "+v"(xo[k]), "+v"(xh[k]));
        u32 mysig = syncA + aw * 4, vsl = zl0, c1r = lds0 + L_CB1 + r32 * 8, l16 = (u32)lane * 16, l8 = (u32)lane * 8;
        u32 c1w = c1r + aw * 256, evo = hh ? OOB : (u32)r32 * 16;
        asm volatile("" : "+v"(mysig), "+v"(vsl), "+v"(c1r), "+v"(c1w), "+v"(evo), "+v"(l16), "+v"(l8));
        u32x4 idm[2];
#pragma unroll
        for (int s = 0; s < 2; ++s) {
#pragma unroll
            for (int j = 0; j < 4; ++j) { const int dlt = r32 - 16 * hh - 8 * s - 2 * j; idm[s][j] = dlt == 0 ? 0x3F80u : dlt == 1 ? 0x3F800000u : 0u; }
            asm volatile("" : "+v"(idm[s]));
        }
        cf P[4], L8, L16, L32, coef;
        f32x2 cqr[2], cqi[2];
        u32 ta, tb;
        {
            SPINW(peek1(zl0 + Z_PAR) != 0, 1);
            asm volatile("" :: "v"(cpf));
            u32 pr[3];
#pragma unroll
            for (int i = 0; i < 3; ++i) pr[i] = lds_ld<u16>(lds0 + L_PAR + 3072 + i * 64 + g32 * 2);
            ta = GET_TAG(); tb = tag2(ta);
                    const float a_lr = bf2f(pr[0]), a_li = bf2f(pr[1]);
                    const float dt = rbf(__builtin_expf(bf2f(pr[2])));
                    P[0] = cexpk(dt * a_lr, dt * a_li, 1.f);
                    P[1] = csq(P[0]); P[2] = cmul(P[0], P[1]); P[3] = csq(P[1]);
                    L8 = csq(P[3]); L16 = csq(L8); L32 = csq(L16);
                    const float nr = P[0].x - 1.f, ni = P[0].y, inv = 2.f / (a_lr * a_lr + a_li * a_li);
                    coef = cf{(nr * a_lr + ni * a_li) * inv, (ni * a_lr - nr * a_li) * inv};
                    cf Q = hh ? cmul(coef, L16) : coef;
                    { const cf Q1 = cmul(Q, L32); Q = (aw & 1) ? Q1 : Q; }
                    { const cf L64 = csq(L32), Q2 = cmul(Q, L64); Q = (aw & 2) ? Q2 : Q; }
                    const cf q1 = cmul(Q, P[3]), q2 = cmul(Q, L8), q3 = cmul(q1, L8);
                    cqr[0] = f32x2{Q.x, q1.x}; cqi[0] = f32x2{Q.y, q1.y}; cqr[1] = f32x2{q2.x, q3.x}; cqi[1] = f32x2{q2.y, q3.y};
                    asm volatile("" : "+v"(P[0]), "+v"(P[1]), "+v"(P[2]), "+v"(P[3]), "+v"(L8), "+v"(L16), "+v"(L32), "+v"(coef), "+v"(cqr[0]), "+v"(cqi[0]), "+v"(cqr[1]), "+v"(cqi[1]));
        }
        f32x16 accR = {}, accI = {};
        {
            bf16x8 fa[4], fr[4], fi[4];
            auto rd = [&](int n) INL { const int s = n >> 2, k = n & 3;
                fa[k] = lds_ld<bf16x8>(s % 5 < 4 ? xo[k] + (s % 5) * 16384 : xh[k]);
                fr[k] = lds_ld<bf16x8>(fo[k] + (s % 6) * 8192);
                fi[k] = lds_ld<bf16x8>(fo[k] + (s % 6) * 8192 + 4096); };
            SPINW(peekz2(vsl) != 0, 0);
            u32x2 fl = *(volatile const LAS u32x2*)(uintptr_t)(vsl + 8);
            rd(0); rd(1); rd(2);
            SBAR();
            sfor<0, 32>([&](auto nn) INL {
                constexpr int n = decltype(nn)::v, s = n >> 2, k = n & 3;
                if constexpr (n == 28) { if (rfl(fl.x | fl.y) != 0) SPINW(peekz2(vsl + 56) != 0, 0); FENCE(); rd(28); rd(29); rd(30); SBAR(); }
                if constexpr (k == 1 && s < 6) {
                    if (rfl(fl.x | fl.y) != 0) SPINW(peekz2(vsl + (s + 1) * 8) != 0, 0);
                    FENCE();
                }
                if constexpr (k == 2 && s < 6) fl = *(volatile const LAS u32x2*)(uintptr_t)(vsl + (s + 2) * 8);
                accR = MFMA32(fa[k], fr[k], accR);
                SBAR();
                if constexpr (n + 3 < 32 && !(n >= 25 && n <= 27)) rd(n + 3);
                if constexpr (n == 26) fl = *(volatile const LAS u32x2*)(uintptr_t)(vsl + 56);
                if constexpr (k == 1 && s < 4) {
                    asm volatile("s_waitcnt lgkmcnt(3)" ::: "memory");
                    zsig(wss, voob, lds0 + L_ZA + s * 16 + aw * 4); }
                accI = MFMA32(fa[k], fi[k], accI);
                SBAR();
            });
        }
        f32x2 hr[8], hi[8];
#pragma unroll
        for (int i = 0; i < 8; ++i) { hr[i] = f32x2{accR[2 * i], accR[2 * i + 1]}; hi[i] = f32x2{accI[2 * i], accI[2 * i + 1]}; }
#pragma unroll
        for (int i = 2; i < 8; ++i) {
            f32x2 nr = fma2(-P[0].y, hi[i - 2], fma2(P[0].x, hr[i - 2], hr[i]));
            f32x2 ni = fma2(P[0].y, hr[i - 2], fma2(P[0].x, hi[i - 2], hi[i]));
            hr[i] = nr; hi[i] = ni;
        }
        cf e0, e1, pre = {0.f, 0.f};
        {
            const f32x2 zr = fma2(-L8.y, hi[6], fma2(L8.x, hr[6], hr[7])), zi = fma2(L8.y, hr[6], fma2(L8.x, hi[6], hi[7]));
            const cf e16 = {__builtin_fmaf(-P[3].y, zi.x, __builtin_fmaf(P[3].x, zr.x, zr.y)), __builtin_fmaf(P[3].y, zr.x, __builtin_fmaf(P[3].x, zi.x, zi.y))};
            swap_halves(e16, e0, e1);
        }
        const cf e32 = cfma(e0, L16, e1);
        if (aw < 3) { lds_st<f32x2>(c1w, e32); FENCE(); }
        if (aw > 0) {
            cf w0, w1, w2;
#pragma unroll 1
            for (u32 spn = 0; spn < (1u << 22); ++spn) {
                asm volatile("ds_read_b64 %0, %3\n\tds_read_b64 %1, %3 offset:256\n\tds_read_b64 %2, %3 offset:512\n\ts_waitcnt lgkmcnt(0)" : "=&v"(w0), "=&v"(w1), "=&v"(w2) : "v"(c1r) : "memory");
                if (__builtin_amdgcn_ballot_w64(umax3(f2u(w0.x), f2u(w1.x), f2u(w2.x)) == SENT) == 0) break;
            }
            const cf p2 = cfma(w0, L32, w1), p3 = cfma(p2, L32, w2);
            if (aw == 3 && c < 15) { const cf E = cfma(p3, L32, e32); st128<0>(wss, u32x4{f2u(E.x), ta, f2u(E.y), tb}, evo, WS_E + (u32)(c * 512 + pb * 32) * 16); }
            pre = aw == 1 ? w0 : (aw == 2 ? p2 : p3);
        }
        sig(mysig, 1);
        __builtin_amdgcn_s_setprio(0);
        u32 xk[16], dsk = 0;
        {
            const u32x4 rx = mk_srd(p.in[0], 2048 * 1024), rd = mk_srd(p.in[8], 1024);
            const u32 vx = (u32)hh * 4096 + (u32)r32 * 2;
#pragma unroll
            for (int r = 0; r < 16; ++r) xk[r] = 0;
#pragma unroll
            for (int k = 0; k < 4; ++k)
                asm volatile("buffer_load_short_d16_hi %0, %4, %5, %6 offen\n\tbuffer_load_short_d16_hi %1, %4, %5, %6 offen offset:1024\n\t"
                             "buffer_load_short_d16_hi %2, %4, %5, %6 offen offset:2048\n\tbuffer_load_short_d16_hi %3, %4, %5, %6 offen offset:3072"
                             : "+v"(xk[4 * k]), "+v"(xk[4 * k + 1]), "+v"(xk[4 * k + 2]), "+v"(xk[4 * k + 3]) : "v"(vx), "s"(rx), "s"((u32)t0 * 1024 + (u32)dcol * 2 + k * 8192) : "memory");
            asm volatile("buffer_load_short_d16_hi %0, %1, %2, %3 offen" : "+v"(dsk) : "v"((u32)r32 * 2), "s"(rd), "s"((u32)dcol * 2) : "memory");
        }
        SBAR();
        f32x2 cpr[2], cpi[2];
        {
            { const cf pre1 = cfma(pre, L16, e0); pre = hh ? pre1 : pre; }
            cf s[4];
            s[0] = pre;
            s[1] = cfma(s[0], P[3], cf{hr[6].x, hi[6].x}); s[2] = cfma(s[1], P[3], cf{hr[6].y, hi[6].y}); s[3] = cfma(s[2], P[3], cf{hr[7].x, hi[7].x});
#pragma unroll
            for (int a = 0; a < 4; ++a) s[a] = cmul(s[a], coef);
#pragma unroll
            for (int u = 0; u < 2; ++u) { cpr[u] = f32x2{s[2 * u].x, s[2 * u + 1].x}; cpi[u] = f32x2{s[2 * u].y, s[2 * u + 1].y}; asm volatile("" : "+v"(cpr[u]), "+v"(cpi[u])); }
        }
#pragma unroll
        for (int i = 0; i < 8; ++i) {
            f32x2 nr = fma2(-coef.y, hi[i], coef.x * hr[i]);
            f32x2 ni = fma2(-coef.y, hr[i], -coef.x * hi[i]);
            hr[i] = nr; hi[i] = ni;
            asm volatile("" : "+v"(hr[i]), "+v"(hi[i]));
        }
        {
            cf H;
#pragma unroll 1
            for (u32 spn = 0; spn < (1u << 24); ++spn) {
                asm volatile("ds_read_b64 %0, %1 offset:768\n\ts_waitcnt lgkmcnt(0)" : "=v"(H) : "v"(c1r) : "memory");
                if (__builtin_amdgcn_ballot_w64(f2u(H.x) == SENT) == 0) break;
            }
            __builtin_amdgcn_s_setprio(3);
            asm volatile("s_waitcnt vmcnt(0)" : "+v"(dsk), "+v"(xk[0]), "+v"(xk[1]), "+v"(xk[2]), "+v"(xk[3]), "+v"(xk[4]), "+v"(xk[5]), "+v"(xk[6]), "+v"(xk[7]),
                         "+v"(xk[8]), "+v"(xk[9]), "+v"(xk[10]), "+v"(xk[11]), "+v"(xk[12]), "+v"(xk[13]), "+v"(xk[14]), "+v"(xk[15]) :: "memory");
            f32x2 gr[2], gi[2];
#pragma unroll
            for (int u = 0; u < 2; ++u) { gr[u] = fma2(-H.y, cqi[u], fma2(H.x, cqr[u], cpr[u])); gi[u] = fma2(H.x, cqi[u], fma2(H.y, cqr[u], cpi[u])); }
#pragma unroll
            for (int i = 0; i < 8; ++i) hr[i] = fma2(-P[i >> 1].y, gi[i & 1], fma2(P[i >> 1].x, gr[i & 1], hr[i]));
            const u32 hs = WS_H + (u32)((c * 4 + aw) * 16 + pb) * 4096;
            f32x16 zr = MFMA32(__builtin_bit_cast(bf16x8, u32x4{pkbf(hr[0].x, hr[2].x), pkbf(hr[4].x, hr[6].x), pkbf(hr[0].y, hr[2].y), pkbf(hr[4].y, hr[6].y)}), __builtin_bit_cast(bf16x8, idm[0]), f32x16{});
            zr = MFMA32(__builtin_bit_cast(bf16x8, u32x4{pkbf(hr[1].x, hr[3].x), pkbf(hr[5].x, hr[7].x), pkbf(hr[1].y, hr[3].y), pkbf(hr[5].y, hr[7].y)}), __builtin_bit_cast(bf16x8, idm[1]), zr);
            SBAR();
#pragma unroll
            for (int i = 0; i < 8; ++i) hi[i] = fma2(-P[i >> 1].y, gr[i & 1], fma2(-P[i >> 1].x, gi[i & 1], hi[i]));
            f32x16 zi = MFMA32(__builtin_bit_cast(bf16x8, u32x4{pkbf(hi[0].x, hi[2].x), pkbf(hi[4].x, hi[6].x), pkbf(hi[0].y, hi[2].y), pkbf(hi[4].y, hi[6].y)}), __builtin_bit_cast(bf16x8, idm[0]), f32x16{});
            zi = MFMA32(__builtin_bit_cast(bf16x8, u32x4{pkbf(hi[1].x, hi[3].x), pkbf(hi[5].x, hi[7].x), pkbf(hi[1].y, hi[3].y), pkbf(hi[5].y, hi[7].y)}), __builtin_bit_cast(bf16x8, idm[1]), zi);
            SBAR();
#define PK2(z, a) __builtin_amdgcn_perm(f2u(z[4 + (a)]), f2u(z[(a)]), 0x07060302u), __builtin_amdgcn_perm(f2u(z[12 + (a)]), f2u(z[8 + (a)]), 0x07060302u)
            const u32x4 o0 = {PK2(zr, 0), PK2(zr, 1)}, o1 = {PK2(zr, 2), PK2(zr, 3)};
            st128<0>(wss, o0, l16, hs);
            st128<1024>(wss, o1, l16, hs);
            u32 cs = ADD4(ADD4(tb, o0), o1);
            const u32x4 o2 = {PK2(zi, 0), PK2(zi, 1)}, o3 = {PK2(zi, 2), PK2(zi, 3)};
            st128<2048>(wss, o2, l16, hs);
            st128<3072>(wss, o3, l16, hs);
            cs = ADD4(ADD4(cs, o2), o3);
            __builtin_amdgcn_raw_buffer_store_b64(u32x2{cs, ta}, ws, l8, WS_CS + (u32)((c * 4 + aw) * 16 + pb) * 512, 16);
            FENCE();
        }
        f32x16 sk;
        {
            const float dk = u2f(dsk);
#pragma unroll
            for (int r = 0; r < 16; r += 2) { const u32 w = pkbf(u2f(xk[r]) * dk, u2f(xk[r + 1]) * dk); sk[r] = u2f(w << 16); sk[r + 1] = u2f(w & 0xFFFF0000u); }
        }
        u32 ca[4], ov[4], hso[4], rso[4], cof[4];
#pragma unroll
        for (int k = 0; k < 4; ++k) { ca[k] = lds0 + (u32)r32 * 128 + (u32)(((4 * (k >> 1) + 2 * hh + (k & 1)) ^ ((r32 >> 1) & 7)) * 16) + (u32)kh * 65536; ov[k] = (u32)hh * 16384 + (u32)r32 * 4 + k * 4096; asm volatile("" : "+v"(ca[k]), "+v"(ov[k])); }
        u32 xb = lds0 + L_S0 + (u32)aw * S0_SZ + l16, xq = lds0 + L_S0 + (u32)(aw ^ 2) * S0_SZ + l16, l16b = l16 + 4096;
        asm volatile("" : "+v"(sk), "+v"(xb), "+v"(xq), "+v"(l16b));
#pragma unroll
        for (int jj = 0; jj < 4; ++jj) { const u32 cg = (u32)(4 * kh + ((jj + db) & 3));
            hso[jj] = WS_H + (u32)t32 * 65536 + cg * 8192; rso[jj] = WS_CS + (u32)t32 * 8192 + cg * 1024; cof[jj] = (u32)((jj + db - idx) & 3) * 16384;
            asm volatile("" : "+s"(hso[jj]), "+s"(rso[jj]), "+s"(cof[jj])); }
        SPINW(peekz2(vsl + Z_C) != 0, 0);
        SPINW(((peek1(vsl + Z_HM) >> (16 * th + 8 * kh)) & 0xFFu) != 0, 0);
        u32x4 kfl;
        f32x16 y0, y1;
#pragma unroll 1
        for (u32 att = 0; att < SPIN_CAP; ++att) {
            asm volatile("" : "+v"(ca[0]), "+v"(ca[1]), "+v"(ca[2]), "+v"(ca[3]), "+v"(l16), "+v"(l16b), "+v"(l8) :: "memory");
            bf16x8 cb0[4], cb1[4];
            u32x4 hf[16];
            u32 mk = 0, sm = tb, rec[8], cab[4];
            auto cad = [&](int f) INL { const int jj = f >> 3, a = (f >> 2) & 1, s = f & 3, k = 2 * a + (s & 1);
                if (s < 2) { cab[k] = ca[k] + cof[jj]; asm volatile("" : "+v"(cab[k])); }
                return cab[k] + (s >> 1) * 8192; };
            auto ldh = [&](int f) INL { return __builtin_amdgcn_raw_buffer_load_b128(ws, ((f & 4) ? l16b : l16) + (f & 3) * 1024, hso[f >> 3], 16); };
            auto ldr = [&](int g) INL { return __builtin_amdgcn_raw_buffer_load_b32(ws, l8 + (g & 1) * 512, rso[g >> 1], 16); };
#pragma unroll
            for (int f = 0; f < HWIN; ++f) { if ((f & 3) == 3) rec[f >> 2] = ldr(f >> 2); hf[f] = ldh(f); SBAR(); }
#pragma unroll
            for (int f = 0; f < 3; ++f) { const u32 ad = cad(f); cb0[f] = lds_ld<bf16x8>(ad); cb1[f] = lds_ld<bf16x8>(ad + 4096); }
            SBAR();
#pragma unroll
            for (int f = 0; f < 32; ++f) {
                if (f + HWIN < 32) { if (((f + HWIN) & 3) == 3) rec[(f + HWIN) >> 2] = ldr((f + HWIN) >> 2); hf[(f + HWIN) & 15] = ldh(f + HWIN); SBAR(); }
                const u32x4 hv = hf[f & 15];
                bf16x8 av = __builtin_bit_cast(bf16x8, hv);
                y0 = MFMA32(av, cb0[f & 3], f ? y0 : f32x16{});
                SBAR();
                if (f + 3 < 32) { const u32 ad = cad(f + 3); cb0[(f + 3) & 3] = lds_ld<bf16x8>(ad); cb1[(f + 3) & 3] = lds_ld<bf16x8>(ad + 4096); }
                SBAR();
                y1 = MFMA32(av, cb1[f & 3], f ? y1 : f32x16{});
                SBAR();
                sm = ADD4(sm, hv);
                if ((f & 3) == 3) { mk |= sm ^ rec[f >> 2]; sm = tb; asm volatile("" : "+v"(mk)); } else asm volatile("" : "+v"(sm));
                SBAR();
                if (f == SIG5) { sig(mysig, 3); SBAR(); }
                if (f == 26) { kfl = *(volatile const LAS u32x4*)(uintptr_t)syncK; SBAR(); }
            }
            if (__builtin_amdgcn_ballot_w64(mk != 0) == 0) break;
        }
        {
            if (rfl(umin(umin(kfl.x, kfl.y), umin(kfl.z, kfl.w))) < 5) SPINW(peek4(syncK) < 5, 0);
#pragma unroll
            for (int k = 0; k < 4; ++k) lds_st<f32x4>(xb + k * 1024, f32x4{y1[4 * k], y1[4 * k + 1], y1[4 * k + 2], y1[4 * k + 3]});
            sig(mysig, 4);
            y0 += sk;
            asm volatile("" : "+v"(y0));
            wait1(syncA + (aw ^ 2) * 4, 4);
            const rsrc_t ro = mk_rsrc(p.out, 2048 * 4096);
            f32x4 o[4];
#pragma unroll
            for (int k = 0; k < 4; ++k) o[k] = lds_ld<f32x4>(xq + k * 1024);
#pragma unroll
            for (int k = 0; k < 4; ++k) {
#pragma unroll
                for (int j = 0; j < 4; ++j)
                    __builtin_amdgcn_raw_buffer_store_b32(f2u(y0[4 * k + j] + o[k][j]), ro, ov[j], (u32)(t0 + 8 * k) * 4096 + (u32)dcol * 4, 16);
            }
            if (tb64 == 31 && db == 0 && aw == 0) __builtin_amdgcn_raw_buffer_store_b32(ta, ws, lane ? OOB : 0u, WS_W, 16);
        }
    } else {
        const int bw = hw - 4, dsel = bw >> 1, half = bw & 1, d = 64 * xcd + 2 * idx + dsel;
        __builtin_amdgcn_s_setprio(1);
        const u32 s0 = lds0 + L_S0 + bw * S0_SZ, kc = lds0 + L_KC + dsel * KC_SZ, ub = lds0 + L_UB;
        const u32 mysig = syncK + bw * 4, psig = syncK + (bw ^ 1) * 4;
        lds_st<u32>(kc - 48 + umin(lane, 11) * 4, 0u);
        u32 ga[20];
        {
            const int lag0 = 256 * rr + 128 * half + 127 - 18 * sp;
            const float f0 = (float)lag0;
            const int l2 = (int)kc + 2 * lag0, cap = (int)kc + 256, cb = (int)kc - 2 * (0x4300 - 128);
#pragma unroll
            for (int e = 0; e < 20; e += 2) {
                const int w = (int)pkbf(f0 - (float)e, f0 - (float)(e + 1));
                const int x0 = ((w << 16) >> 15) + cb, x1 = ((w >> 16) << 1) + cb, y0 = l2 - 2 * e, y1 = l2 - 2 * e - 2;
                asm("v_med3_i32 %0, %1, %2, %3" : "=v"(ga[e]) : "v"(x0), "v"(y0), "s"(cap));
                asm("v_med3_i32 %0, %1, %2, %3" : "=v"(ga[e + 1]) : "v"(x1), "v"(y1), "s"(cap));
            }
#pragma unroll
            for (int e = 0; e < 19; ++e) asm volatile("" : "+v"(ga[e]));
        }
        u32 wa = s0 + lane * 4, ra = s0 + (u32)m16 * 288 + (u32)q4 * 16, xa = s0 + lane * 16, xp = lds0 + L_S0 + (bw ^ 1) * S0_SZ + lane * 16, cpa = s0 + 36 * lane;
        u32 k0a = kc + 2 * (128 * (q4 >> 1) + 4 * (q4 & 1) + 8 * m16), k1a = kc + 2 * (q4 < 2 ? 256 + 64 * q4 + 4 * m16 : 384 + 64 * (q4 - 2) + 2 * m16), k1b = k1a + (q4 < 2 ? 4 : 64);
        u32 k2a = kc + 2 * (512 + (q4 == 3 ? 144 : 64 * q4) + m16), para = lds0 + L_PAR + bw * 128 + lane * 2, vms = mysig, vps = psig;
        u32 bb = ub + 512 - 512 * half + 64 * m16 + 32 * q4 + 16 * dsel, ab = s0 + 4 * (592 * (1 - (m16 & 1)) + ((110 - m16 + (m16 & 1)) >> 1) + 4 * q4 - 48);
        asm volatile("" : "+v"(wa), "+v"(ra), "+v"(xa), "+v"(xp), "+v"(cpa), "+v"(k0a), "+v"(k1a), "+v"(k1b), "+v"(k2a), "+v"(para), "+v"(vms), "+v"(vps), "+v"(bb), "+v"(ab));
        SPINW(peek1(zl0 + Z_PAR) != 0, 1);
        u32 pr[8];
#pragma unroll
        for (int i = 0; i < 6; ++i) pr[i] = lds_ld<u16>(para + i * 512);
        pr[6] = lds_ld<u16>(lds0 + L_PAR + 3264 + dsel * 2); pr[7] = lds_ld<u16>(lds0 + L_PAR + 3268 + dsel * 2);
        sig(vms, 1);
        const u32 ta = GET_TAG(), tb = tag2(ta);
        const float dt = rbf(__builtin_expf(bf2f(pr[6]))), dsk0 = lane == 0 ? bf2f(pr[7]) : 0.f;
        f32x4 ka[3];
        {
            const float zr = dt * bf2f(pr[0]), zi = dt * bf2f(pr[1]);
            const cf db_ = {dt * bf2f(pr[2]), -dt * bf2f(pr[3])}, cc = {bf2f(pr[4]), -bf2f(pr[5])};
            const cf E1 = cexpk(zr, zi, 1.f), E8c = cexpk(zr, -zi, 8.f), E128 = cexpk(zr, zi, 128.f);
            cf V[16], R[8], W[17];
            { const cf E16 = csq(E8c), E32 = csq(E16), E64 = csq(E32);
              V[0] = cmul(cc, db_); V[1] = cmul(V[0], E8c);
#pragma unroll
              for (int j = 0; j < 2; ++j) V[2 + j] = cmul(V[j], E16);
#pragma unroll
              for (int j = 0; j < 4; ++j) V[4 + j] = cmul(V[j], E32);
#pragma unroll
              for (int j = 0; j < 8; ++j) V[8 + j] = cmul(V[j], E64); }
            R[0] = cf{1.f, 0.f}; R[1] = E1; R[2] = csq(E1); R[3] = cmul(R[1], R[2]); R[4] = csq(R[2]);
#pragma unroll
            for (int r = 1; r < 4; ++r) R[4 + r] = cmul(R[r], R[4]);
            W[1] = E128; W[2] = csq(W[1]); W[3] = cmul(W[1], W[2]); W[4] = csq(W[2]);
#pragma unroll
            for (int k = 1; k <= 4; ++k) W[4 + k] = cmul(W[k], W[4]);
#pragma unroll
            for (int k = 1; k <= 8; ++k) W[8 + k] = cmul(W[k], W[8]);
            u32 pv[16], p0[16], p1[16], p2[9];
#pragma unroll
            for (int j = 0; j < 16; ++j) pv[j] = pkc(V[j]);
#pragma unroll
            for (int r = 0; r < 8; ++r) { p0[r] = pkc(R[r]); p0[8 + r] = pkc(r ? cmul(R[r], W[1]) : W[1]); }
#pragma unroll
            for (int k = 0; k < 2; ++k)
#pragma unroll
                for (int r = 0; r < 4; ++r) p1[4 * k + r] = pkc(r ? cmul(R[2 * r], W[2 + k]) : W[2 + k]);
#pragma unroll
            for (int k = 0; k < 4; ++k) { p1[8 + 2 * k] = pkc(W[4 + k]); p1[9 + 2 * k] = pkc(cmul(W[4 + k], R[4])); }
#pragma unroll
            for (int k = 0; k < 9; ++k) p2[k] = pkc(W[8 + k]);
#pragma unroll
            for (int j = 0; j < 16; ++j) asm volatile("" : "+v"(pv[j]), "+v"(p0[j]), "+v"(p1[j]), "+v"(p2[j < 9 ? j : 0]));
            SPINW(peek4(syncK) < 1 || peek1(syncE) < 1, 2);
            SPINW(peekz2(zl0 + 56) != 0, 2);
#pragma unroll
            for (int j = 0; j < 16; ++j) lds_st<u32>(wa + j * 288, pv[j]);
            FENCE();
            bf16x8 vf[4], uf[4];
#pragma unroll
            for (int s = 0; s < 4; ++s) vf[s] = lds_ld<bf16x8>(ra + s * 64);
            FENCE();
#pragma unroll
            for (int j = 0; j < 16; ++j) lds_st<u32>(wa + j * 288, p0[j]);
            FENCE();
#pragma unroll
            for (int s = 0; s < 4; ++s) uf[s] = lds_ld<bf16x8>(ra + s * 64);
            FENCE();
#pragma unroll
            for (int j = 0; j < 16; ++j) lds_st<u32>(wa + j * 288, p1[j]);
            FENCE();
            ka[0] = f32x4{};
#pragma unroll
            for (int s = 0; s < 4; ++s) ka[0] = MFMA16(uf[s], vf[s], ka[0]);
#pragma unroll
            for (int s = 0; s < 4; ++s) uf[s] = lds_ld<bf16x8>(ra + s * 64);
            FENCE();
#pragma unroll
            for (int k = 0; k < 9; ++k) lds_st<u32>(wa + k * 288, p2[k]);
            FENCE();
            ka[1] = f32x4{};
#pragma unroll
            for (int s = 0; s < 4; ++s) ka[1] = MFMA16(uf[s], vf[s], ka[1]);
#pragma unroll
            for (int s = 0; s < 4; ++s) uf[s] = lds_ld<bf16x8>(ra + s * 64);
            FENCE();
            ka[2] = f32x4{};
#pragma unroll
            for (int s = 0; s < 4; ++s) ka[2] = MFMA16(uf[s], vf[s], ka[2]);
        }
#pragma unroll
        for (int t = 0; t < 3; ++t) lds_st<f32x4>(xa + t * 1024, ka[t]);
        sig(vms, 2);
        wait1(vps, 2);
        {
#pragma unroll
            for (int t = 0; t < 3; ++t) ka[t] += lds_ld<f32x4>(xp + t * 1024);
            sig(vms, 3);
            ka[0][0] += dsk0;
            lds_st<u32x2>(k0a, u32x2{pkbf(ka[0][0], ka[0][1]), pkbf(ka[0][2], ka[0][3])});
            lds_st<u32>(k1a, pkbf(ka[1][0], ka[1][1]));
            lds_st<u32>(k1b, pkbf(ka[1][2], ka[1][3]));
            const u32 w01 = pkbf(ka[2][0], ka[2][1]), w23 = pkbf(ka[2][2], ka[2][3]);
            lds_st<u16>(k2a, (u16)w01); lds_st<u16>(k2a + 32, (u16)(w01 >> 16)); lds_st<u16>(k2a + 64, (u16)w23); lds_st<u16>(k2a + 96, (u16)(w23 >> 16));
            FENCE();
            u32 gv[19];
#pragma unroll
            for (int e = 0; e < 19; ++e) gv[e] = lds_ld<u16>(ga[e]);
            wait1(vps, 3);
#pragma unroll
            for (int k = 0; k < 9; ++k) {
                lds_st<u32>(cpa + 4 * k, gv[2 * k] | (gv[2 * k + 1] << 16));
                lds_st<u32>(cpa + 2368 + 4 * k, gv[2 * k + 1] | (gv[2 * k + 2] << 16));
            }
            FENCE();
        }
        SPINW(peek1(zl0 + Z_UT) != 0, 1);
        __builtin_amdgcn_s_setprio(2);
        f32x4 acc[8];
#pragma unroll
        for (int i = 0; i < 8; ++i) acc[i] = f32x4{};
        {
            bf16x8 bfr[2][8], afr[4];
            auto lda = [&](int n) { const int r = n >> 3, i = n & 7; const u32 o = ab + 4 * (72 * i + 16 * (3 - r));
                u32x4 av = {lds_ld<u32>(o), lds_ld<u32>(o + 4), lds_ld<u32>(o + 8), lds_ld<u32>(o + 12)}; return __builtin_bit_cast(bf16x8, av); };
            afr[0] = lda(0);
#pragma unroll
            for (int m = 0; m < 8; ++m) bfr[0][m] = lds_ld<bf16x8>(bb + 1024 * m + 384);
            afr[1] = lda(1); afr[2] = lda(2);
            SBAR();
#pragma unroll
            for (int r = 0; r < 4; ++r) {
                const int cu = r & 1, nx = cu ^ 1;
#pragma unroll
                for (int i = 0; i < 8; ++i) {
                    const int n = 8 * r + i;
                    if (n + 3 < 32) afr[(n + 3) & 3] = lda(n + 3);
                    if (r < 3 && i < 4) { bfr[nx][2 * i] = lds_ld<bf16x8>(bb + 2048 * i + 128 * (2 - r)); bfr[nx][2 * i + 1] = lds_ld<bf16x8>(bb + 2048 * i + 1024 + 128 * (2 - r)); }
#pragma unroll
                    for (int cb = i; cb < 8; ++cb) acc[cb] = MFMA16(afr[n & 3], bfr[cu][cb - i], acc[cb]);
                    SBAR();
                }
            }
        }
        FENCE();
        __builtin_amdgcn_s_setprio(3);
        {
            f32x4 kp[4];
            if (half) {
#pragma unroll
                for (int k = 0; k < 4; ++k) { lds_st<f32x4>(xa + k * 1024, acc[k]); kp[k] = acc[4 + k]; }
            } else {
#pragma unroll
                for (int k = 0; k < 4; ++k) { lds_st<f32x4>(xa + k * 1024, acc[4 + k]); kp[k] = acc[k]; }
            }
            sig(vms, 4);
            wait1(vps, 4);
#pragma unroll
            for (int k = 0; k < 4; ++k) kp[k] += lds_ld<f32x4>(xp + k * 1024);
            sig(vms, 5);
            st128<0>(wss, u32x4{pkbf(kp[0][0], kp[0][1]), pkbf(kp[0][2], kp[0][3]), pkbf(kp[1][0], kp[1][1]), pkbf(kp[1][2], kp[1][3])}, (u32)lane * 16, WS_YT + (u32)d * 4096 + (u32)half * 2048);
            st128<1024>(wss, u32x4{pkbf(kp[2][0], kp[2][1]), pkbf(kp[2][2], kp[2][3]), pkbf(kp[3][0], kp[3][1]), pkbf(kp[3][2], kp[3][3])}, (u32)lane * 16, WS_YT + (u32)d * 4096 + (u32)half * 2048);
            WAITV(0);
            __builtin_amdgcn_raw_buffer_store_b64(u32x2{ta, tb}, ws, lane ? OOB : 0u, WS_YFLAG + (u32)(half * 512 + d) * FS, 16);
        }
    }
}

#if defined(__HIP_DEVICE_COMPILE__)
#pragma clang attribute pop
#endif

extern "C" void kernel_launch(void* const* d_in, const int* in_sizes, int n_in, void* d_out, int out_size, void* d_ws, size_t ws_size, hipStream_t stream) {
    Params p;
    for (int i = 0; i < 18; ++i) p.in[i] = d_in[i];
    p.out = (float*)d_out;
    p.ws = (char*)d_ws;
    (void)hipFuncSetAttribute((const void*)ssm_fused, hipFuncAttributeMaxDynamicSharedMemorySize, LDS_BYTES);
    ssm_fused<<<256, 768, LDS_BYTES, stream>>>(p);
}
```
